```python
import torch

def _softmax(x, axis=-1):
    return torch.softmax(x, dim=axis)
def _gelu(x):
    return torch.nn.functional.gelu(x, approximate="tanh")
def _silu(x):
    return torch.nn.functional.silu(x)
def _sigmoid(x):
    return torch.sigmoid(x)
def _layernorm(x, axis=-1, eps=1e-5):
    m = x.mean(dim=axis, keepdim=True)
    v = x.var(dim=axis, keepdim=True, unbiased=False)
    return (x - m) / torch.sqrt(v + eps)
def _scatter_add(tgt, idx, src, axis=0):
    return tgt.clone().index_add_(axis, idx, src)
def _segment_sum(src, seg, n):
    out = torch.zeros((n,) + src.shape[1:], dtype=src.dtype)
    return out.index_add_(0, seg, src)
def _pad(x, pw):
    flat = tuple(v for pair in reversed(pw) for v in pair)
    return torch.nn.functional.pad(x, flat)

kernel_name = 'parallel_sum__sliding_block__moe_arch_mod__prompt'

def setup_inputs():

    _w = (lambda a: (lambda t: t.to(torch.bfloat16) if t.is_floating_point() else t)(torch.as_tensor(a)))
    def _A_setup():
        B, S, D = 1, 2048, 640
        return {
            "x": _w((lambda *s: torch.randn(*s, dtype=torch.bfloat16) * 0.05)(B, S, D)),
            "wqkv": _w((lambda *s: torch.randn(*s, dtype=torch.bfloat16) * 0.05)(D, 3 * D)),
            "wo": _w((lambda *s: torch.randn(*s, dtype=torch.bfloat16) * 0.05)(D, D)),
        }
    _A_in = _A_setup()

    def _B_setup():
        B, S, D, F, C = 1, 2048, 640, 1536, 2
        return {
            "x": _w((lambda *s: torch.randn(*s, dtype=torch.bfloat16) * 0.05)(B, S, D)),
            "w_r": _w((lambda *s: torch.randn(*s, dtype=torch.bfloat16) * 0.05)(D)),
            "w_gate": _w((lambda *s: torch.randn(*s, dtype=torch.bfloat16) * 0.05)(D, F)),
            "w_up": _w((lambda *s: torch.randn(*s, dtype=torch.bfloat16) * 0.05)(D, F)),
            "w_down": _w((lambda *s: torch.randn(*s, dtype=torch.bfloat16) * 0.05)(F, D)),
        }
    _B_in = _B_setup()

    return {
        "A_x": _A_in["x"],
        "A_wqkv": _A_in["wqkv"],
        "A_wo": _A_in["wo"],
        "B_x": _B_in["x"],
        "B_w_r": _B_in["w_r"],
        "B_w_gate": _B_in["w_gate"],
        "B_w_up": _B_in["w_up"],
        "B_w_down": _B_in["w_down"],
    }

def reference(A_x, A_wqkv, A_wo, B_x, B_w_r, B_w_gate, B_w_up, B_w_down):
    def _A_ref(x, wqkv, wo):
        B, S, D = x.shape
        H = 32; Dh = D // H; W = 256
        qkv = (lambda x, *d: x.permute(*d))((x @ wqkv).reshape(B, S, 3, H, Dh), 2, 0, 3, 1, 4)
        q, k, v = qkv[0], qkv[1], qkv[2]
        scores = torch.einsum("bhqd,bhkd->bhqk", q, k) * (Dh ** -0.5)
        idx = torch.arange(S)
        mask = (idx[:, None] - idx[None, :] >= 0) & (idx[:, None] - idx[None, :] < W)
        scores = torch.where(mask[None, None], scores, -1e9)
        p = _softmax(scores, axis=-1)
        o = torch.einsum("bhqk,bhkd->bhqd", p, v)
        return (lambda x, *d: x.permute(*d))(o, 0, 2, 1, 3).reshape(B, S, D) @ wo

    def _B_ref(x, w_r, w_gate, w_up, w_down):
        B, S, D = x.shape
        C = 2
        r = x @ w_r
        idx = torch.argsort(-r, axis=-1)[:, :C]
        idx_e = (lambda x, n, axis: torch.repeat_interleave(x, n, dim=axis))(idx.reshape(B, C, 1), D, axis=2)
        x_sel = (lambda x, i, axis: torch.take_along_dim(x, i, dim=axis))(x, idx_e, axis=1)
        g = _silu(x_sel @ w_gate)
        u = x_sel @ w_up
        y = (g * u) @ w_down
        r_sel = _sigmoid((lambda x, i, axis: torch.take_along_dim(x, i, dim=axis))(r, idx, axis=1))
        y = y * r_sel.reshape(B, C, 1)
        b_off = (torch.arange(B) * S).reshape(B, 1)
        flat_idx = (idx + b_off).reshape(B * C)
        zero = (x * 0.0).reshape(B * S, D)
        upd = _scatter_add(zero, flat_idx, y.reshape(B * C, D), axis=0)
        return x + upd.reshape(B, S, D)

    x = A_x
    xn = _layernorm(x, axis=-1)
    return x + _A_ref(xn, A_wqkv, A_wo) + _B_ref(xn, B_w_r, B_w_gate, B_w_up, B_w_down)
```

```cpp
#include <hip/hip_runtime.h>
#include <hip/hip_bf16.h>
#include <stdint.h>
typedef unsigned short u16;
constexpr int S = 2048, D = 640, H = 32, DH = 20, W = 256, F = 1536;
__device__ __forceinline__ float b2f(u16 v) { return __uint_as_float(((unsigned)v) << 16); }
__device__ __forceinline__ u16 f2b(float f) { unsigned u = __float_as_uint(f); u += 0x7fffu + ((u >> 16) & 1u); return (u16)(u >> 16); }
__device__ __forceinline__ float rb(float f) { return b2f(f2b(f)); }

__global__ void k_ln(const u16* x, const u16* wr, u16* xn, float* r) {
  int t = blockIdx.x, l = threadIdx.x;
  float v[10]; float s = 0;
  for (int i = 0; i < 10; ++i) { v[i] = b2f(x[t * D + l + 64 * i]); s += v[i]; }
  for (int m = 32; m >= 1; m >>= 1) s += __shfl_xor(s, m);
  float mean = s / D; float q = 0;
  for (int i = 0; i < 10; ++i) { float d = v[i] - mean; q += d * d; }
  for (int m = 32; m >= 1; m >>= 1) q += __shfl_xor(q, m);
  float mb = rb(mean), vb = rb(q / D);
  float den = rb(sqrtf(rb(vb + 1e-5f)));
  float acc = 0;
  for (int i = 0; i < 10; ++i) { float y = rb(rb(v[i] - mb) / den); xn[t * D + l + 64 * i] = f2b(y); acc += y * b2f(wr[l + 64 * i]); }
  for (int m = 32; m >= 1; m >>= 1) acc += __shfl_xor(acc, m);
  if (l == 0) r[t] = rb(acc);
}
__global__ void k_gemm(const u16* A, const u16* B, u16* C, int M, int N, int K) {
  __shared__ float As[16][17], Bs[16][17];
  int tx = threadIdx.x, ty = threadIdx.y; int row = blockIdx.y * 16 + ty, col = blockIdx.x * 16 + tx;
  float acc = 0;
  for (int k0 = 0; k0 < K; k0 += 16) {
    As[ty][tx] = b2f(A[row * K + k0 + tx]); Bs[ty][tx] = b2f(B[(k0 + ty) * N + col]);
    __syncthreads();
    for (int k = 0; k < 16; ++k) acc += As[ty][k] * Bs[k][tx];
    __syncthreads();
  }
  C[row * N + col] = f2b(acc);
}
__global__ void k_attn(const u16* qkv, u16* o) {
  int t = blockIdx.x, h = blockIdx.y, l = threadIdx.x;
  float q[DH]; for (int d = 0; d < DH; ++d) q[d] = b2f(qkv[t * 3 * D + h * DH + d]);
  float sc[4]; float mx = -1e30f;
  for (int i = 0; i < 4; ++i) { int j = t - (l + 64 * i); float s = -1e30f;
    if (j >= 0) { s = 0; for (int d = 0; d < DH; ++d) s += q[d] * b2f(qkv[j * 3 * D + D + h * DH + d]); s = rb(rb(s) * 0.22360679774997896f); }
    sc[i] = s; mx = fmaxf(mx, s); }
  for (int m = 32; m >= 1; m >>= 1) mx = fmaxf(mx, __shfl_xor(mx, m));
  float sum = 0; for (int i = 0; i < 4; ++i) { int j = t - (l + 64 * i); sc[i] = j >= 0 ? expf(sc[i] - mx) : 0.f; sum += sc[i]; }
  for (int m = 32; m >= 1; m >>= 1) sum += __shfl_xor(sum, m);
  for (int d = 0; d < DH; ++d) { float a = 0;
    for (int i = 0; i < 4; ++i) { int j = t - (l + 64 * i); if (j >= 0) a += rb(sc[i] / sum) * b2f(qkv[j * 3 * D + 2 * D + h * DH + d]); }
    for (int m = 32; m >= 1; m >>= 1) a += __shfl_xor(a, m);
    if (l == 0) o[t * D + h * DH + d] = f2b(a); }
}
__global__ void k_top2(const float* r, int* idx) {
  if (threadIdx.x == 0) { int b0 = 0; for (int i = 1; i < S; ++i) if (r[i] > r[b0]) b0 = i;
    int b1 = b0 == 0 ? 1 : 0; for (int i = 0; i < S; ++i) if (i != b0 && r[i] > r[b1]) b1 = i; idx[0] = b0; idx[1] = b1; }
}
__global__ void k_gu(const u16* xn, const int* idx, const u16* wg, const u16* wu, u16* hbuf) {
  int f = blockIdx.x * 64 + threadIdx.x, c = blockIdx.y; const u16* xr = xn + idx[c] * D; float g = 0, u = 0;
  for (int k = 0; k < D; ++k) { float xv = b2f(xr[k]); g += xv * b2f(wg[k * F + f]); u += xv * b2f(wu[k * F + f]); }
  g = rb(g); u = rb(u); g = rb(g / (1.f + expf(-g))); hbuf[c * F + f] = f2b(g * u);
}
__global__ void k_down(const u16* hbuf, const int* idx, const float* r, const u16* wd, float* y) {
  int n = blockIdx.x * 64 + threadIdx.x, c = blockIdx.y; float a = 0;
  for (int k = 0; k < F; ++k) a += b2f(hbuf[c * F + k]) * b2f(wd[k * D + n]);
  float sg = rb(1.f / (1.f + expf(-r[idx[c]]))); y[c * D + n] = rb(rb(a) * sg);
}
__global__ void k_fin(const u16* x, const u16* xn, const u16* ao, const int* idx, const float* y, u16* out) {
  int i = blockIdx.x * 256 + threadIdx.x; int t = i / D, n = i % D; float upd = 0;
  if (t == idx[0]) upd = y[n]; else if (t == idx[1]) upd = y[D + n];
  out[i] = f2b(rb(b2f(x[i]) + b2f(ao[i])) + rb(b2f(xn[i]) + upd));
}
extern "C" void kernel_launch(void* const* d_in, const int* in_sizes, int n_in, void* d_out, int out_size, void* d_ws, size_t ws_size, hipStream_t stream) {
  const u16 *x = (const u16*)d_in[0], *wqkv = (const u16*)d_in[1], *wo = (const u16*)d_in[2], *wr = (const u16*)d_in[4], *wg = (const u16*)d_in[5], *wu = (const u16*)d_in[6], *wd = (const u16*)d_in[7];
  char* ws = (char*)d_ws; u16* xn = (u16*)ws; u16* qkv = (u16*)(ws + (4 << 20)); u16* o = (u16*)(ws + (16 << 20)); u16* ao = (u16*)(ws + (20 << 20));
  float* r = (float*)(ws + (24 << 20)); int* idx = (int*)(ws + (25 << 20)); u16* hb = (u16*)(ws + (26 << 20)); float* y = (float*)(ws + (27 << 20));
  k_ln<<<S, 64, 0, stream>>>(x, wr, xn, r);
  k_gemm<<<dim3(3 * D / 16, S / 16), dim3(16, 16), 0, stream>>>(xn, wqkv, qkv, S, 3 * D, D);
  k_attn<<<dim3(S, H), 64, 0, stream>>>(qkv, o);
  k_gemm<<<dim3(D / 16, S / 16), dim3(16, 16), 0, stream>>>(o, wo, ao, S, D, D);
  k_top2<<<1, 64, 0, stream>>>(r, idx);
  k_gu<<<dim3(F / 64, 2), 64, 0, stream>>>(xn, idx, wg, wu, hb);
  k_down<<<dim3(D / 64, 2), 64, 0, stream>>>(hb, idx, r, wd, y);
  k_fin<<<S * D / 256, 256, 0, stream>>>(x, xn, ao, idx, y, (u16*)d_out);
}
```

```cpp
#include <hip/hip_runtime.h>
#include <hip/hip_bf16.h>
#include <stdint.h>
typedef unsigned short u16;
constexpr int S = 2048, D = 640, H = 32, DH = 20, W = 256, F = 1536;
__device__ __forceinline__ float b2f(u16 v) { return __uint_as_float(((unsigned)v) << 16); }
__device__ __forceinline__ u16 f2b(float f) { unsigned u = __float_as_uint(f); u += 0x7fffu + ((u >> 16) & 1u); return (u16)(u >> 16); }
__device__ __forceinline__ float rb(float f) { return b2f(f2b(f)); }

__global__ void k_ln(const u16* x, const u16* wr, u16* xn, float* r) {
  int t = blockIdx.x, l = threadIdx.x;
  float v[10]; float s = 0;
  for (int i = 0; i < 10; ++i) { v[i] = b2f(x[t * D + l + 64 * i]); s += v[i]; }
  for (int m = 32; m >= 1; m >>= 1) s += __shfl_xor(s, m);
  float mean = s / D; float q = 0;
  for (int i = 0; i < 10; ++i) { float d = v[i] - mean; q += d * d; }
  for (int m = 32; m >= 1; m >>= 1) q += __shfl_xor(q, m);
  float mb = rb(mean), vb = rb(q / D);
  float den = rb(sqrtf(rb(vb + 1e-5f)));
  float acc = 0;
  for (int i = 0; i < 10; ++i) { float y = rb(rb(v[i] - mb) / den); xn[t * D + l + 64 * i] = f2b(y); acc += y * b2f(wr[l + 64 * i]); }
  for (int m = 32; m >= 1; m >>= 1) acc += __shfl_xor(acc, m);
  if (l == 0) r[t] = rb(acc);
}
__global__ void k_gemm(const u16* A, const u16* B, u16* C, int M, int N, int K) {
  __shared__ float As[16][17], Bs[16][17];
  int tx = threadIdx.x, ty = threadIdx.y; int row = blockIdx.y * 16 + ty, col = blockIdx.x * 16 + tx;
  float acc = 0;
  for (int k0 = 0; k0 < K; k0 += 16) {
    As[ty][tx] = b2f(A[row * K + k0 + tx]); Bs[ty][tx] = b2f(B[(k0 + ty) * N + col]);
    __syncthreads();
    for (int k = 0; k < 16; ++k) acc += As[ty][k] * Bs[k][tx];
    __syncthreads();
  }
  C[row * N + col] = f2b(acc);
}
__global__ void k_attn(const u16* qkv, u16* o) {
  int t = blockIdx.x, h = blockIdx.y, l = threadIdx.x;
  float q[DH]; for (int d = 0; d < DH; ++d) q[d] = b2f(qkv[t * 3 * D + h * DH + d]);
  float sc[4]; float mx = -1e30f;
  for (int i = 0; i < 4; ++i) { int j = t - (l + 64 * i); float s = -1e30f;
    if (j >= 0) { s = 0; for (int d = 0; d < DH; ++d) s += q[d] * b2f(qkv[j * 3 * D + D + h * DH + d]); s = rb(rb(s) * 0.22360679774997896f); }
    sc[i] = s; mx = fmaxf(mx, s); }
  for (int m = 32; m >= 1; m >>= 1) mx = fmaxf(mx, __shfl_xor(mx, m));
  float sum = 0; for (int i = 0; i < 4; ++i) { int j = t - (l + 64 * i); sc[i] = j >= 0 ? expf(sc[i] - mx) : 0.f; sum += sc[i]; }
  for (int m = 32; m >= 1; m >>= 1) sum += __shfl_xor(sum, m);
  for (int d = 0; d < DH; ++d) { float a = 0;
    for (int i = 0; i < 4; ++i) { int j = t - (l + 64 * i); if (j >= 0) a += rb(sc[i] / sum) * b2f(qkv[j * 3 * D + 2 * D + h * DH + d]); }
    for (int m = 32; m >= 1; m >>= 1) a += __shfl_xor(a, m);
    if (l == 0) o[t * D + h * DH + d] = f2b(a); }
}
__global__ void k_top2(const float* r, int* idx) {
  if (threadIdx.x == 0) { int b0 = 0; for (int i = 1; i < S; ++i) if (r[i] > r[b0]) b0 = i;
    int b1 = b0 == 0 ? 1 : 0; for (int i = 0; i < S; ++i) if (i != b0 && r[i] > r[b1]) b1 = i; idx[0] = b0; idx[1] = b1; }
}
__global__ void k_gu(const u16* xn, const int* idx, const u16* wg, const u16* wu, u16* hbuf) {
  int f = blockIdx.x * 64 + threadIdx.x, c = blockIdx.y; const u16* xr = xn + idx[c] * D; float g = 0, u = 0;
  for (int k = 0; k < D; ++k) { float xv = b2f(xr[k]); g += xv * b2f(wg[k * F + f]); u += xv * b2f(wu[k * F + f]); }
  g = rb(g); u = rb(u); g = rb(g / (1.f + expf(-g))); hbuf[c * F + f] = f2b(g * u);
}
__global__ void k_down(const u16* hbuf, const int* idx, const float* r, const u16* wd, float* y) {
  int n = blockIdx.x * 64 + threadIdx.x, c = blockIdx.y; float a = 0;
  for (int k = 0; k < F; ++k) a += b2f(hbuf[c * F + k]) * b2f(wd[k * D + n]);
  float sg = rb(1.f / (1.f + expf(-r[idx[c]]))); y[c * D + n] = rb(rb(a) * sg);
}
__global__ void k_fin(const u16* x, const u16* xn, const u16* ao, const int* idx, const float* y, u16* out) {
  int i = blockIdx.x * 256 + threadIdx.x; int t = i / D, n = i % D; float upd = 0;
  if (t == idx[0]) upd = y[n]; else if (t == idx[1]) upd = y[D + n];
  out[i] = f2b(rb(b2f(x[i]) + b2f(ao[i])) + rb(b2f(xn[i]) + upd));
}

typedef unsigned u32x4 __attribute__((ext_vector_type(4)));
#define LAS __attribute__((address_space(3)))
__device__ __forceinline__ void glds16(const void* g, unsigned lds_base) {
  unsigned sv; asm volatile("s_mov_b32 %0, m0\n\ts_mov_b32 m0, %2\n\ts_nop 0\n\tglobal_load_lds_dwordx4 %1, off\n\ts_mov_b32 m0, %0" : "=&s"(sv) : "v"(g), "s"(lds_base) : "memory"); }
__device__ __forceinline__ unsigned long long rt() { return __builtin_amdgcn_s_memrealtime(); }
__global__ void k_spin(const unsigned long long* ts, int n, int mode) {
  if (threadIdx.x == 0) {
    unsigned long long s = ~0ull, e = 0, sum = 0;
    for (int i = 0; i < n; ++i) { unsigned long long a = ts[2 * i], b = ts[2 * i + 1]; s = a < s ? a : s; e = b > e ? b : e; sum += b - a; }
    unsigned long long span = mode == 0 ? (e - s) : sum / n; if (span > 3000) span = 3000;
    unsigned long long t0 = rt();
    while (rt() - t0 < span * 1000) __builtin_amdgcn_s_sleep(32);
  }
}
template<int XON, int WON>
__global__ __launch_bounds__(512, 2) void kload(const u16* x, const u16* w, unsigned long long* ts, unsigned* sink) {
  __shared__ __attribute__((aligned(16))) char smem[163840];
  unsigned long long t0 = rt();
  int b = blockIdx.x, xcd = b & 7, i = b >> 3, tr, hg;
  tr = (xcd >> 1) * 4 + (i >> 3); hg = (xcd & 1) * 8 + (i & 7);
  int tid = threadIdx.x, lane = tid & 63, wid = __builtin_amdgcn_readfirstlane(tid >> 6);
  unsigned lds0 = (unsigned)(size_t)smem;
  u32x4 xr[20];
  const char* xp = (const char*)x + (size_t)(tr * 128 + wid * 16 + (lane >> 3)) * 1280 + (lane & 7) * 16;
  int ch = lane & 15; int colb = (hg % 15) * 256 + ch * 16;
  const char* wp = (const char*)w + (size_t)(wid * 4 + (lane >> 4)) * 3840 + colb;
#pragma unroll
  for (int s = 0; s < 20; ++s) {
    if (XON) asm volatile("global_load_dwordx4 %0, %1, off offset:%2" : "=v"(xr[s]) : "v"(xp + (s & 1) * 8 * 1280), "i"((s >> 1) * 128) : "memory");
    if (WON) glds16(wp + (size_t)s * 32 * 3840, lds0 + (s * 8 + wid) * 1024);
  }
  asm volatile("s_waitcnt vmcnt(0)" ::: "memory");
  unsigned long long t1 = rt();
  unsigned acc = 0;
  if (XON) {
#pragma unroll
    for (int s = 0; s < 20; ++s) asm volatile("" : "+v"(xr[s]));
#pragma unroll
    for (int s = 0; s < 20; ++s) acc ^= xr[s][0] ^ xr[s][1] ^ xr[s][2] ^ xr[s][3];
  }
  __syncthreads();
  acc ^= *(unsigned*)(smem + tid * 64);
  if (acc == 0x12345678) sink[0] = acc;
  if (lane == 0) { unsigned long long* p = ts + (b * 8 + wid) * 2; p[0] = t0; p[1] = t1; }
}
#define R4(x) x x x x
#define R16(x) R4(R4(x))
#define R64(x) R4(R16(x))
#define R256(x) R4(R64(x))
#define R1024(x) R4(R256(x))
__global__ void kline(float* out, unsigned long long* ts) {
  float a = threadIdx.x, b = a + 1, c = a + 2, d = a + 3;
  unsigned long long r0 = rt();
  asm volatile(R1024("v_fma_f32 %0, %0, %0, %0\n v_fma_f32 %1, %1, %1, %1\n v_fma_f32 %2, %2, %2, %2\n v_fma_f32 %3, %3, %3, %3\n") : "+v"(a), "+v"(b), "+v"(c), "+v"(d));
  unsigned long long r1 = rt();
  out[blockIdx.x * blockDim.x + threadIdx.x] = a + b + c + d;
  if ((threadIdx.x & 63) == 0) { int w = blockIdx.x * (blockDim.x / 64) + threadIdx.x / 64; ts[w * 2] = r0; ts[w * 2 + 1] = r1; }
}
__global__ void klat(const u16* x, unsigned long long* ts, unsigned* sink) {
  unsigned long long r0 = rt();
  unsigned v; asm volatile("global_load_dword %0, %1, off\n s_waitcnt vmcnt(0)" : "=v"(v) : "v"((const char*)x + blockIdx.x * 10240 + threadIdx.x * 4) : "memory");
  unsigned long long r1 = rt();
  if (v == 0x12345678) sink[0] = v;
  if (threadIdx.x == 0) { ts[blockIdx.x * 2] = r0; ts[blockIdx.x * 2 + 1] = r1; }
}
extern "C" void kernel_launch(void* const* d_in, const int* in_sizes, int n_in, void* d_out, int out_size, void* d_ws, size_t ws_size, hipStream_t stream) {
  const u16 *x = (const u16*)d_in[0], *wqkv = (const u16*)d_in[1], *wo = (const u16*)d_in[2], *wr = (const u16*)d_in[4], *wg = (const u16*)d_in[5], *wu = (const u16*)d_in[6], *wd = (const u16*)d_in[7];
  char* ws = (char*)d_ws; u16* xn = (u16*)ws; u16* qkv = (u16*)(ws + (4 << 20)); u16* o = (u16*)(ws + (16 << 20)); u16* ao = (u16*)(ws + (20 << 20));
  float* r = (float*)(ws + (24 << 20)); int* idx = (int*)(ws + (25 << 20)); u16* hb = (u16*)(ws + (26 << 20)); float* y = (float*)(ws + (27 << 20));
  unsigned long long* ts = (unsigned long long*)(ws + (40 << 20)); unsigned* sink = (unsigned*)(ws + (41 << 20));
  klat<<<256,64,0,stream>>>(x,ts,sink); k_spin<<<1,64,0,stream>>>(ts,256,1);
  k_ln<<<S, 64, 0, stream>>>(x, wr, xn, r);
  k_gemm<<<dim3(3 * D / 16, S / 16), dim3(16, 16), 0, stream>>>(xn, wqkv, qkv, S, 3 * D, D);
  k_attn<<<dim3(S, H), 64, 0, stream>>>(qkv, o);
  k_gemm<<<dim3(D / 16, S / 16), dim3(16, 16), 0, stream>>>(o, wo, ao, S, D, D);
  k_top2<<<1, 64, 0, stream>>>(r, idx);
  k_gu<<<dim3(F / 64, 2), 64, 0, stream>>>(xn, idx, wg, wu, hb);
  k_down<<<dim3(D / 64, 2), 64, 0, stream>>>(hb, idx, r, wd, y);
  k_fin<<<S * D / 256, 256, 0, stream>>>(x, xn, ao, idx, y, (u16*)d_out);
}
```

```cpp
#include <hip/hip_runtime.h>
#include <hip/hip_bf16.h>
#include <stdint.h>
typedef unsigned short u16;
constexpr int S = 2048, D = 640, H = 32, DH = 20, W = 256, F = 1536;
__device__ __forceinline__ float b2f(u16 v) { return __uint_as_float(((unsigned)v) << 16); }
__device__ __forceinline__ u16 f2b(float f) { unsigned u = __float_as_uint(f); u += 0x7fffu + ((u >> 16) & 1u); return (u16)(u >> 16); }
__device__ __forceinline__ float rb(float f) { return b2f(f2b(f)); }

__global__ void k_ln(const u16* x, const u16* wr, u16* xn, float* r) {
  int t = blockIdx.x, l = threadIdx.x;
  float v[10]; float s = 0;
  for (int i = 0; i < 10; ++i) { v[i] = b2f(x[t * D + l + 64 * i]); s += v[i]; }
  for (int m = 32; m >= 1; m >>= 1) s += __shfl_xor(s, m);
  float mean = s / D; float q = 0;
  for (int i = 0; i < 10; ++i) { float d = v[i] - mean; q += d * d; }
  for (int m = 32; m >= 1; m >>= 1) q += __shfl_xor(q, m);
  float mb = rb(mean), vb = rb(q / D);
  float den = rb(sqrtf(rb(vb + 1e-5f)));
  float acc = 0;
  for (int i = 0; i < 10; ++i) { float y = rb(rb(v[i] - mb) / den); xn[t * D + l + 64 * i] = f2b(y); acc += y * b2f(wr[l + 64 * i]); }
  for (int m = 32; m >= 1; m >>= 1) acc += __shfl_xor(acc, m);
  if (l == 0) r[t] = rb(acc);
}
__global__ void k_gemm(const u16* A, const u16* B, u16* C, int M, int N, int K) {
  __shared__ float As[16][17], Bs[16][17];
  int tx = threadIdx.x, ty = threadIdx.y; int row = blockIdx.y * 16 + ty, col = blockIdx.x * 16 + tx;
  float acc = 0;
  for (int k0 = 0; k0 < K; k0 += 16) {
    As[ty][tx] = b2f(A[row * K + k0 + tx]); Bs[ty][tx] = b2f(B[(k0 + ty) * N + col]);
    __syncthreads();
    for (int k = 0; k < 16; ++k) acc += As[ty][k] * Bs[k][tx];
    __syncthreads();
  }
  C[row * N + col] = f2b(acc);
}
__global__ void k_attn(const u16* qkv, u16* o) {
  int t = blockIdx.x, h = blockIdx.y, l = threadIdx.x;
  float q[DH]; for (int d = 0; d < DH; ++d) q[d] = b2f(qkv[t * 3 * D + h * DH + d]);
  float sc[4]; float mx = -1e30f;
  for (int i = 0; i < 4; ++i) { int j = t - (l + 64 * i); float s = -1e30f;
    if (j >= 0) { s = 0; for (int d = 0; d < DH; ++d) s += q[d] * b2f(qkv[j * 3 * D + D + h * DH + d]); s = rb(rb(s) * 0.22360679774997896f); }
    sc[i] = s; mx = fmaxf(mx, s); }
  for (int m = 32; m >= 1; m >>= 1) mx = fmaxf(mx, __shfl_xor(mx, m));
  float sum = 0; for (int i = 0; i < 4; ++i) { int j = t - (l + 64 * i); sc[i] = j >= 0 ? expf(sc[i] - mx) : 0.f; sum += sc[i]; }
  for (int m = 32; m >= 1; m >>= 1) sum += __shfl_xor(sum, m);
  for (int d = 0; d < DH; ++d) { float a = 0;
    for (int i = 0; i < 4; ++i) { int j = t - (l + 64 * i); if (j >= 0) a += rb(sc[i] / sum) * b2f(qkv[j * 3 * D + 2 * D + h * DH + d]); }
    for (int m = 32; m >= 1; m >>= 1) a += __shfl_xor(a, m);
    if (l == 0) o[t * D + h * DH + d] = f2b(a); }
}
__global__ void k_top2(const float* r, int* idx) {
  if (threadIdx.x == 0) { int b0 = 0; for (int i = 1; i < S; ++i) if (r[i] > r[b0]) b0 = i;
    int b1 = b0 == 0 ? 1 : 0; for (int i = 0; i < S; ++i) if (i != b0 && r[i] > r[b1]) b1 = i; idx[0] = b0; idx[1] = b1; }
}
__global__ void k_gu(const u16* xn, const int* idx, const u16* wg, const u16* wu, u16* hbuf) {
  int f = blockIdx.x * 64 + threadIdx.x, c = blockIdx.y; const u16* xr = xn + idx[c] * D; float g = 0, u = 0;
  for (int k = 0; k < D; ++k) { float xv = b2f(xr[k]); g += xv * b2f(wg[k * F + f]); u += xv * b2f(wu[k * F + f]); }
  g = rb(g); u = rb(u); g = rb(g / (1.f + expf(-g))); hbuf[c * F + f] = f2b(g * u);
}
__global__ void k_down(const u16* hbuf, const int* idx, const float* r, const u16* wd, float* y) {
  int n = blockIdx.x * 64 + threadIdx.x, c = blockIdx.y; float a = 0;
  for (int k = 0; k < F; ++k) a += b2f(hbuf[c * F + k]) * b2f(wd[k * D + n]);
  float sg = rb(1.f / (1.f + expf(-r[idx[c]]))); y[c * D + n] = rb(rb(a) * sg);
}
__global__ void k_fin(const u16* x, const u16* xn, const u16* ao, const int* idx, const float* y, u16* out) {
  int i = blockIdx.x * 256 + threadIdx.x; int t = i / D, n = i % D; float upd = 0;
  if (t == idx[0]) upd = y[n]; else if (t == idx[1]) upd = y[D + n];
  out[i] = f2b(rb(b2f(x[i]) + b2f(ao[i])) + rb(b2f(xn[i]) + upd));
}

typedef unsigned u32x4 __attribute__((ext_vector_type(4)));
__device__ __forceinline__ void glds16(const void* g, unsigned lds_base) {
  unsigned sv; asm volatile("s_mov_b32 %0, m0\n\ts_mov_b32 m0, %2\n\ts_nop 0\n\tglobal_load_lds_dwordx4 %1, off\n\ts_mov_b32 m0, %0" : "=&s"(sv) : "v"(g), "s"(lds_base) : "memory"); }
__device__ __forceinline__ unsigned long long rt() { return __builtin_amdgcn_s_memrealtime(); }
__global__ void k_spin(const unsigned long long* ts, int mode) {
  __shared__ unsigned long long a[256], b[256];
  a[threadIdx.x] = ts[2 * threadIdx.x]; b[threadIdx.x] = ts[2 * threadIdx.x + 1];
  __syncthreads();
  if (threadIdx.x == 0) {
    unsigned long long s = ~0ull, e = 0, sum = 0;
    for (int i = 0; i < 256; ++i) { s = a[i] < s ? a[i] : s; e = b[i] > e ? b[i] : e; sum += b[i] - a[i]; }
    unsigned long long span = mode == 0 ? (e - s) * 1000 : sum * 1000 / 256; if (span > 3000000) span = 3000000;
    unsigned long long t0 = rt();
    while (rt() - t0 < span) __builtin_amdgcn_s_sleep(32);
  }
}
template<int XM, int WM, int MAP, int ROT, int RING>
__global__ __launch_bounds__(512, 2) void kload(const u16* x, const u16* w, unsigned long long* ts, unsigned* sink) {
  __shared__ __attribute__((aligned(16))) char smem[163840];
  unsigned long long t0 = rt();
  int b = blockIdx.x, xcd = b & 7, i = b >> 3, tr, hg, trl, hgl;
  if (MAP == 0) { trl = i >> 4; hgl = i & 15; tr = xcd * 2 + trl; hg = hgl; }
  else if (MAP == 1) { trl = i >> 3; hgl = i & 7; tr = (xcd >> 1) * 4 + trl; hg = (xcd & 1) * 8 + hgl; }
  else if (MAP == 2) { trl = i >> 2; hgl = i & 3; tr = (xcd >> 2) * 8 + trl; hg = (xcd & 3) * 4 + hgl; }
  else { trl = i >> 1; hgl = i & 1; tr = trl; hg = xcd * 2 + hgl; }
  int tid = threadIdx.x, lane = tid & 63, wid = __builtin_amdgcn_readfirstlane(tid >> 6);
  unsigned lds0 = (unsigned)(size_t)smem;
  int rot = 0;
  if (ROT == 1) rot = (hgl + trl * 3 + xcd) % 10;
  if (ROT == 2) rot = (xcd * 5 / 4) % 10;
  const char* xp = (const char*)x + (size_t)(tr * 128 + wid * 16 + (lane >> 3)) * 1280 + (lane & 7) * 16;
  const char* wp = (const char*)w + (size_t)(wid * 8 + (lane >> 4)) * 3840 + (hg % 15) * 256 + (lane & 15) * 16;
  u32x4 xr[20], wr[20];
#pragma unroll
  for (int s0 = 0; s0 < 10; ++s0) {
    int s = s0 + rot; if (s >= 10) s -= 10;
    if (RING && s0 >= RING) { if (RING == 4) asm volatile("s_waitcnt vmcnt(12)\n s_barrier" ::: "memory"); if (RING == 3) asm volatile("s_waitcnt vmcnt(8)\n s_barrier" ::: "memory"); }
#pragma unroll
    for (int j = 0; j < 2; ++j) {
      if (XM == 1) asm volatile("global_load_dwordx4 %0, %1, off" : "=v"(xr[s0 * 2 + j]) : "v"(xp + j * 8 * 1280 + s * 128) : "memory");
      if (XM == 2) glds16(xp + j * 8 * 1280 + s * 128, lds0 + (((s0 % 5) * 4 + j) * 8 + wid) * 1024);
      if (WM == 1) glds16(wp + (size_t)(s * 64 + j * 4) * 3840, lds0 + (((s0 % 5) * 4 + 2 + j) * 8 + wid) * 1024);
      if (WM == 2) asm volatile("global_load_dwordx4 %0, %1, off" : "=v"(wr[s0 * 2 + j]) : "v"(wp + (size_t)(s * 64 + j * 4) * 3840) : "memory");
    }
  }
  asm volatile("s_waitcnt vmcnt(0)" ::: "memory");
  __syncthreads();
  unsigned long long t1 = rt();
  unsigned acc = *(unsigned*)(smem + tid * 64);
  if (XM == 1) {
#pragma unroll
    for (int s = 0; s < 20; ++s) { asm volatile("" : "+v"(xr[s])); acc ^= xr[s][0] ^ xr[s][1] ^ xr[s][2] ^ xr[s][3]; } }
  if (WM == 2) {
#pragma unroll
    for (int s = 0; s < 20; ++s) { asm volatile("" : "+v"(wr[s])); acc ^= wr[s][0] ^ wr[s][1] ^ wr[s][2] ^ wr[s][3]; } }
  if (acc == 0x12345678) sink[0] = acc;
  if (tid == 0) { ts[b * 2] = t0; ts[b * 2 + 1] = t1; }
}
extern "C" void kernel_launch(void* const* d_in, const int* in_sizes, int n_in, void* d_out, int out_size, void* d_ws, size_t ws_size, hipStream_t stream) {
  const u16 *x = (const u16*)d_in[0], *wqkv = (const u16*)d_in[1], *wo = (const u16*)d_in[2], *wr = (const u16*)d_in[4], *wg = (const u16*)d_in[5], *wu = (const u16*)d_in[6], *wd = (const u16*)d_in[7];
  char* ws = (char*)d_ws; u16* xn = (u16*)ws; u16* qkv = (u16*)(ws + (4 << 20)); u16* o = (u16*)(ws + (16 << 20)); u16* ao = (u16*)(ws + (20 << 20));
  float* r = (float*)(ws + (24 << 20)); int* idx = (int*)(ws + (25 << 20)); u16* hb = (u16*)(ws + (26 << 20)); float* y = (float*)(ws + (27 << 20));
  unsigned long long* ts = (unsigned long long*)(ws + (40 << 20)); unsigned* sink = (unsigned*)(ws + (41 << 20));
  kload<2,1,0,1,0><<<256,512,0,stream>>>(x,wqkv,ts,sink); k_spin<<<1,256,0,stream>>>(ts,0);
  k_ln<<<S, 64, 0, stream>>>(x, wr, xn, r);
  k_gemm<<<dim3(3 * D / 16, S / 16), dim3(16, 16), 0, stream>>>(xn, wqkv, qkv, S, 3 * D, D);
  k_attn<<<dim3(S, H), 64, 0, stream>>>(qkv, o);
  k_gemm<<<dim3(D / 16, S / 16), dim3(16, 16), 0, stream>>>(o, wo, ao, S, D, D);
  k_top2<<<1, 64, 0, stream>>>(r, idx);
  k_gu<<<dim3(F / 64, 2), 64, 0, stream>>>(xn, idx, wg, wu, hb);
  k_down<<<dim3(D / 64, 2), 64, 0, stream>>>(hb, idx, r, wd, y);
  k_fin<<<S * D / 256, 256, 0, stream>>>(x, xn, ao, idx, y, (u16*)d_out);
}
```

```cpp
#include <hip/hip_runtime.h>
#include <stdint.h>

typedef unsigned short u16;
typedef unsigned long long u64;
typedef short bf16x8 __attribute__((ext_vector_type(8)));
typedef short s16x4 __attribute__((ext_vector_type(4)));
typedef float f32x2 __attribute__((ext_vector_type(2)));
typedef float f32x4 __attribute__((ext_vector_type(4)));
typedef float f32x16 __attribute__((ext_vector_type(16)));
typedef unsigned u32x2 __attribute__((ext_vector_type(2)));
typedef unsigned u32x4 __attribute__((ext_vector_type(4)));
typedef __bf16 bf2 __attribute__((ext_vector_type(2)));
#define LAS __attribute__((address_space(3)))
#define GAS __attribute__((address_space(1)))
#define DI __device__ __forceinline__

constexpr unsigned WS_GEN = 0, WS_FLAG1 = 4096, WS_FLAG2 = 131072, WS_CAND = 262144, WS_STATS = 278528, WS_TOP = 294912, WS_PARTA = 524288,
                   WS_YPART = 1572864, WS_QKV = 2097152, WS_O = 10485760, WS_END = 13631488, WS_STAMP = 16777216;
constexpr unsigned L_WO = 0, L_KV = 102400, L_FFNA = 65536;
constexpr unsigned KVS = 3264, L_V = 1536, VPS = 576;
constexpr unsigned L_RED = L_KV, L_UPD = L_KV + 43008;
constexpr unsigned L_KEY = 154624, L_TOP = L_KEY + 32, L_CNT = L_KEY + 48  , L_STAT = L_KEY + 64, L_CS = L_STAT + 1024, L_TMP = L_CS + 1024;
constexpr unsigned L_HB = 122880;
constexpr unsigned SPIN_CAP = 1u << 20;
constexpr unsigned ONE2 = 0x3f803f80u;
#define CODE_KB 30

constexpr int UP[20] = {0, 0, 1, 1, 2, 2, 3, 3, 4, 4, 5, 6, 6, 7, 7, 8, 8, 9, 9, 9}, UQ[18] = {0, 1, 0, 1, 0, 1, 0, 1, 0, 1, 1, 0, 1, 0, 1, 0, 1, 0};
template <int N> struct IC { static constexpr int value = N; };

struct Params { const u16 *x, *wqkv, *wo, *wr, *wg, *wu, *wd; u16* out; char* ws; };

#define ST(i) do {} while (0)

#define FST(i) do {} while (0)
#define WAITV(n) asm volatile("s_waitcnt vmcnt(" #n ")" ::: "memory")
#define REQ0 3
#define LA 3
#define LAMODE 1
#define BC_AT 7
#define FORCE_FB 0
#define WO1 2
#define OPRE 4
#define PRIO 0
#define WAITN_(n) WAITV(n)
#define WAITN(n) WAITN_(n)
#define WO_PRE 6
#define BAR() do { __builtin_amdgcn_s_waitcnt(0xC07F); asm volatile("s_barrier" ::: "memory"); } while (0)
#define FENCE() __builtin_amdgcn_sched_barrier(0)
#define MFMA32(a, b, c) __builtin_amdgcn_mfma_f32_32x32x16_bf16((a), (b), (c), 0, 0, 0)
#define MFMA16(a, b, c) __builtin_amdgcn_mfma_f32_16x16x32_bf16((a), (b), (c), 0, 0, 0)

DI void glds(unsigned voff, const void* sbase, unsigned lds) {
  asm volatile("s_mov_b32 m0, %2\n\ts_nop 0\n\tglobal_load_lds_dwordx4 %0, %1" :: "v"(voff), "s"(sbase), "s"(lds) : "memory");
}
DI bf16x8 lds128(unsigned a) { return *(const LAS bf16x8*)a; }
DI s16x4 ldstr(unsigned a) { return __builtin_amdgcn_ds_read_tr16_b64_v4i16((LAS s16x4*)a); }
DI bf16x8 trfrag(unsigned a, unsigned a2) { s16x4 lo = ldstr(a), hi = ldstr(a2); return __builtin_shufflevector(lo, hi, 0, 1, 2, 3, 4, 5, 6, 7); }
DI unsigned pk(float a, float b) { f32x2 v = {a, b}; return __builtin_bit_cast(unsigned, __builtin_convertvector(v, bf2)); }
DI float rb(float a) { return __uint_as_float(pk(a, 0.f) << 16); }
DI float blo(unsigned v) { return __uint_as_float(v << 16); }
DI float bhi(unsigned v) { return __uint_as_float(v & 0xffff0000u); }
DI float dot2(unsigned a, unsigned b, float c) { return __builtin_amdgcn_fdot2_f32_bf16(__builtin_bit_cast(bf2, a), __builtin_bit_cast(bf2, b), c, false); }
DI float xhalf_sum(float v) { auto r = __builtin_amdgcn_permlane32_swap(__float_as_uint(v), __float_as_uint(v), false, false); return __uint_as_float(r[0]) + __uint_as_float(r[1]); }
DI u64 ldg64(const char* a) { return __hip_atomic_load((GAS u64*)a, __ATOMIC_RELAXED, __HIP_MEMORY_SCOPE_AGENT); }
DI void stg64(char* a, u64 v) { __hip_atomic_store((GAS u64*)a, v, __ATOMIC_RELAXED, __HIP_MEMORY_SCOPE_AGENT); }
DI u64 gran(unsigned v, unsigned tag) { return ((u64)tag << 32) | v; }
template <int CTRL> DI unsigned dpp(unsigned v) { return __builtin_amdgcn_update_dpp(0u, v, CTRL, 0xf, 0xf, true); }
DI unsigned wave_umax(unsigned v) {
  v = max(v, dpp<0x128>(v)); v = max(v, dpp<0x124>(v)); v = max(v, dpp<0x122>(v)); v = max(v, dpp<0x121>(v));
  auto r = __builtin_amdgcn_permlane16_swap(v, v, false, false); v = max(r[0], r[1]);
  auto q = __builtin_amdgcn_permlane32_swap(v, v, false, false); return max(q[0], q[1]);
}
DI float sum32(float v) {
  v += __uint_as_float(dpp<0x128>(__float_as_uint(v))); v += __uint_as_float(dpp<0x124>(__float_as_uint(v)));
  v += __uint_as_float(dpp<0x122>(__float_as_uint(v))); v += __uint_as_float(dpp<0x121>(__float_as_uint(v)));
  auto r = __builtin_amdgcn_permlane16_swap(__float_as_uint(v), __float_as_uint(v), false, false);
  return __uint_as_float(r[0]) + __uint_as_float(r[1]);
}
DI void ln_stats(float s1, float s2, float& mb, float& db) {
  float mean = s1 * (1.f / 640.f); float var = fmaxf(s2 * (1.f / 640.f) - mean * mean, 0.f);
  mb = rb(mean); db = rb(__builtin_amdgcn_sqrtf(rb(rb(var) + 1e-5f)));
}
DI void poll(const char* addr, bool act, u64 want) {
#pragma unroll 1
  for (unsigned sp = 0; sp < SPIN_CAP; ++sp) { u64 v = ldg64(addr); if (__all(!act || v == want)) break; __builtin_amdgcn_s_sleep(1); }
  asm volatile("" ::: "memory");
}
DI bool cand_top2(const char* ws, int lane, unsigned tag, unsigned& k1, unsigned& k2) {
  bool ok = true; unsigned a = 0, b = 0;
#pragma unroll 2
  for (int e0 = 0; e0 < 32; e0 += 16) {
    u64 g[16];
#pragma unroll
    for (int e = 0; e < 16; ++e) g[e] = ldg64(ws + WS_CAND + (e0 + e) * 512 + lane * 8);
#pragma unroll
    for (int e = 0; e < 16; ++e) { const unsigned v = (unsigned)g[e]; ok &= (unsigned)(g[e] >> 32) == tag; b = max(b, min(a, v)); a = max(a, v); }
  }
  if (!__all(ok)) return false;
  k1 = wave_umax(a); k2 = wave_umax(a == k1 ? b : a);
  k1 = __builtin_amdgcn_readfirstlane(k1); k2 = __builtin_amdgcn_readfirstlane(k2);
  return true;
}
struct WoOff { unsigned r[5], l[5]; };
DI WoOff wo_offsets(int lane) {
  WoOff o;
#pragma unroll
  for (int m = 0; m < 5; ++m) {
    const unsigned L = 64 * m + lane, rho = (L * 205) >> 11, c = L - rho * 10, q = rho & 3, g = (rho >> 2) & 3, t = rho >> 4;
    o.r[m] = (24 * (g >> 1) + 8 * (g & 1) + 4 * t + q) * 1280 + c * 16; o.l[m] = (40 * g + 16 + 4 * t + q) * 1280 + c * 16;
    asm volatile("" : "+v"(o.r[m]), "+v"(o.l[m]));
  }
  return o;
}
DI void wo_steps(int s0, int s1, const WoOff& o, const u16* wo, int cs80, unsigned lds0) {
#pragma unroll 1
  for (int u = s0; u < s1; ++u) {
    const int kh = u >= 10, st = u - 10 * kh;
    const char* sp = (const char*)wo + cs80 * 160 + (kh * 320 + (st < 8 ? st * 40 : (st - 8) * 160)) * 1280;
    const unsigned d = lds0 + L_WO + u * 5120;
    if (st < 8) {
#pragma unroll
      for (int m = 0; m < 5; ++m) glds(o.r[m], sp, d + m * 1024);
    } else {
#pragma unroll
      for (int m = 0; m < 5; ++m) glds(o.l[m], sp, d + m * 1024);
    }
  }
}
DI void lds_meet(unsigned addr, unsigned want, int lane) {
  __builtin_amdgcn_s_waitcnt(0xC07F);
  if (lane == 0) __hip_atomic_fetch_add((LAS unsigned*)addr, 1u, __ATOMIC_RELAXED, __HIP_MEMORY_SCOPE_WORKGROUP);
#pragma unroll 1
  for (unsigned sp = 0; sp < SPIN_CAP; ++sp) { if (__builtin_amdgcn_readfirstlane(*(volatile LAS unsigned*)addr) >= want) break; __builtin_amdgcn_s_sleep(1); }
  asm volatile("" ::: "memory");
}

__global__ __launch_bounds__(512, 2) void fused_block(Params p) {
  __shared__ __attribute__((aligned(1024))) char smem[163840];
  const unsigned lds0 = (unsigned)(size_t)smem;
  const int tid = threadIdx.x, lane = tid & 63, w = __builtin_amdgcn_readfirstlane(tid >> 6);
  const int b = blockIdx.x, xc = b & 7, i = b >> 3;
  const bool spare = i < 2;
  const int xp = xc >> 1, odd = xc & 1;
  const int cs80 = i & 7;
  char* const ws = p.ws;
  const auto rs = __builtin_amdgcn_make_buffer_rsrc(ws, 0, WS_END, 0x00020000);
  const int h = lane >> 5, l31 = lane & 31, g4 = lane >> 4, q4 = (lane & 15) >> 2, p4 = lane & 3;
  unsigned tag = 0;
  auto lds_tag = [&]() { tag = __builtin_amdgcn_readfirstlane(*(const LAS unsigned*)(lds0 + L_KEY)); };
  ST(0);
  const unsigned j30 = (unsigned)(i - 2) & 31u, j7 = (j30 * 37) >> 8;
  const unsigned trl = j30 < 28 ? j7 : (2 * odd + j30) & 3, ct = j30 < 28 ? j30 - j7 * 7 + odd * 8 : 7;
  unsigned sn = 3 * (trl + ct - 3 * (((trl + ct) * 43) >> 7)) + ((xc * 3) >> 3);
  const unsigned tr = 4 * xp + trl;
  const int rb8 = 64 * xp + 32 * odd + i;
  const char* const xt = (const char*)p.x + tr * (128 * 1280);
  const char* const wt = (const char*)p.wqkv + ct * 256;
  const unsigned x0 = (lane >> 3) * 1280 + (((lane & 7) ^ g4) * 16), x1 = (8 + (lane >> 3)) * 1280 + (((lane & 7) ^ (4 + g4)) * 16);
  const unsigned w0 = g4 * 3840 + (((lane & 15) ^ (g4 << 2)) * 16), w1 = w0 + 4 * 3840;
  auto pieces = [&](unsigned e, unsigned slot) {
    const char* xs = xt + (sn * 128 + e * (16 * 1280)); const char* wsb = wt + (sn * 64 + e * 8) * 3840; const unsigned d = lds0 + slot * 32768 + e * 2048;
    glds(x0, xs, d); glds(w0, wsb, d + 16384); glds(x1, xs, d + 1024); glds(w1, wsb, d + 16384 + 1024);
  };
  if (!spare) { pieces(w, 0); sn = sn == 9 ? 0 : sn + 1; }
  FST(28);

  const int rrow = 8 * rb8 + 2 * w + h;
  u32x4 rx[3], rw[3]; unsigned pfv = 0, genv = 0; float a1 = 0.f, a2 = 0.f, ar = 0.f, sw = 0.f;
  auto router_load = [&]() {
    if (CODE_KB && lane < CODE_KB / 2) asm volatile("global_load_dword %0, %1, %2" : "+v"(pfv) : "v"(((i & 3) * 4 + w) * (CODE_KB * 64) + lane * 128), "s"(__builtin_amdgcn_s_getpc()) : "memory");
    const char* xr = (const char*)p.x + rrow * 1280 + l31 * 16; const char* wr = (const char*)p.wr + l31 * 16;
    const int t3 = l31 < 16 ? 1024 : 0;
    rx[0] = *(const GAS u32x4*)xr; rx[1] = *(const GAS u32x4*)(xr + 512); rw[0] = *(const GAS u32x4*)wr; rw[1] = *(const GAS u32x4*)(wr + 512);
    rx[2] = *(const GAS u32x4*)(xr + t3); rw[2] = *(const GAS u32x4*)(wr + t3);
    genv = __hip_atomic_load((GAS unsigned*)(ws + WS_GEN), __ATOMIC_RELAXED, __HIP_MEMORY_SCOPE_AGENT);
    FENCE();
  };
  auto router_dots = [&]() {
    if (l31 >= 16) rx[2] = rw[2] = (u32x4){};
#pragma unroll
    for (int e = 0; e < 3; ++e)
#pragma unroll
      for (int c = 0; c < 4; ++c) { a1 = dot2(rx[e][c], ONE2, a1); a2 = dot2(rx[e][c], rx[e][c], a2); ar = dot2(rx[e][c], rw[e][c], ar); sw = dot2(rw[e][c], ONE2, sw); }
    tag = __builtin_amdgcn_readfirstlane(genv) + 1u;
    if (tid == 0) *(LAS unsigned*)(lds0 + L_KEY) = tag;
    asm volatile("" : "+v"(a1), "+v"(a2), "+v"(ar), "+v"(sw) : "v"(pfv));
  };
  float rmb = 0.f, rdb = 0.f; unsigned rk = 0, rv = 0;
  auto router_step = [&](int k) {
    auto fdpp = [&](float& v, int st) { const unsigned u = __float_as_uint(v); v += __uint_as_float(st == 0 ? dpp<0x128>(u) : st == 1 ? dpp<0x124>(u) : st == 2 ? dpp<0x122>(u) : dpp<0x121>(u)); };
    auto fswp = [&](float& v) { auto r = __builtin_amdgcn_permlane16_swap(__float_as_uint(v), __float_as_uint(v), false, false); v = __uint_as_float(r[0]) + __uint_as_float(r[1]); };
    if (k < 4) { fdpp(a1, k); fdpp(a2, k); fdpp(ar, k); fdpp(sw, k); }
    if (k == 4) { fswp(a1); fswp(a2); }
    if (k == 5) { fswp(ar); fswp(sw); }
    if (k == 6) { a1 *= 1.f / 640.f; a2 = fmaxf(a2 * (1.f / 640.f) - a1 * a1, 0.f); }
    if (k == 7) { rmb = rb(a1); a2 = rb(a2) + 1e-5f; }
    if (k == 8) { a2 = __builtin_amdgcn_sqrtf(rb(a2)); ar -= rmb * sw; }
    if (k == 9) { rdb = rb(a2); a2 = __builtin_amdgcn_rcpf(rdb); }
    if (k == 10) { rk = pk(ar * a2, 0.f) & 0xffffu; rv = (__float_as_uint(rmb) >> 16) | __float_as_uint(rdb); }
    if (k == 11) rk = (rk & 0x8000u ? (~rk & 0xffffu) : (rk | 0x8000u)) << 16;
    if (k == 12) rv = l31 ? rk | (2047u - rrow) : rv;
    if (k == 13) { if (l31 < 2) __builtin_amdgcn_raw_buffer_store_b64((u32x2){rv, tag}, rs, (l31 ? WS_CAND : WS_STATS) + rrow * 8, 0, 16); }
    asm volatile("" : "+v"(a1), "+v"(a2), "+v"(ar), "+v"(sw), "+v"(rmb), "+v"(rdb), "+v"(rk), "+v"(rv));
  };

  if (!spare) {
    if (w < 4) {
      router_load();
      const int fh = w >> 1, th = w & 1;
      unsigned xa[4], wa[2], ko[4];
#pragma unroll
      for (int e = 0; e < 4; ++e) { const int ks = (2 * th + e) & 3, row = 64 * th + l31; xa[e] = lds0 + row * 128 + (((2 * ks + h) ^ ((row >> 1) & 7)) * 16); ko[e] = ks * 4096; }
#pragma unroll
      for (int f = 0; f < 2; ++f) wa[f] = lds0 + 16384 + (8 * h + q4) * 256 + (((4 * (2 * fh + f) + 2 * (g4 & 1) + (p4 >> 1)) ^ (q4 << 2)) * 16) + 8 * (p4 & 1);
      f32x16 acc[2][2] = {};
      f32x4 cs0 = {}, cs1 = {};
      const unsigned selw = (lane & 15) == (g4 & 1) ? ONE2 : 0u;
      const bf16x8 sel = __builtin_bit_cast(bf16x8, (u32x4){selw, selw, selw, selw});
      struct Fr { bf16x8 v[4]; };
      Fr FA[4], FB[4];
      auto rd = [&](Fr* F, int n, unsigned so) {
        const int e = n >> 2, j = n & 3;
        if (j < 2) { const unsigned a = wa[j] + (so + ko[e]); F[e].v[j] = trfrag(a, a + 1024); } else F[e].v[j] = lds128(xa[e] + so + (j - 2) * 4096);
      };
      auto colsum = [&](const Fr* C, int e) { cs0 = MFMA16(C[e].v[0], sel, cs0); cs1 = MFMA16(C[e].v[1], sel, cs1); };
      auto body = [&](Fr* N, const Fr* C, unsigned so, int r0 = 99) {
#pragma unroll
        for (int n = 0; n < 16; ++n) {
          const int e = n >> 2, f = (n >> 1) & 1, t = n & 1;
          acc[f][t] = MFMA32(C[e].v[f], C[e].v[2 + t], acc[f][t]);
          if (n < 4) { rd(N, 2 * n, so); rd(N, 2 * n + 1, so); } else if (n < 12) rd(N, n + 4, so);
          if (n >= 4 && r0 + n - 4 < 14) router_step(r0 + n - 4);
          FENCE();
        }
        colsum(C, 0); colsum(C, 1);
        FENCE();
      };
      if (CODE_KB) WAITV(8); else WAITV(7);
      BAR(); ST(1);
#pragma unroll
      for (int n = 0; n < 16; ++n) rd(FA, n, 0);
      router_dots();
      BAR();
#pragma unroll
      for (int s = 1; s <= 7; s += 2) { FST(2 * s); body(FB, FA, (s & 3) * 32768, s == 1 ? 0 : 99); FST(2 * s + 1); BAR(); FST(2 * s + 2); body(FA, FB, ((s + 1) & 3) * 32768, s == 1 ? 12 : 99); FST(2 * s + 3); BAR(); }
      FST(18);
      body(FB, FA, 32768);
      colsum(FB, 0); colsum(FB, 1);
      if ((lane & 15) < 2) { const unsigned d = lds0 + L_CS + w * 256 + ((lane & 15) * 16 + 4 * g4) * 4; *(LAS f32x4*)d = cs0; *(LAS f32x4*)(d + 128) = cs1; }
      FST(23); BAR(); FST(19);
      const unsigned ca = lds0 + L_CS + fh * 512 + l31 * 4;
      const float c0 = *(const LAS float*)ca + *(const LAS float*)(ca + 256), c1 = *(const LAS float*)(ca + 128) + *(const LAS float*)(ca + 384);
      const f32x2 st0 = *(const LAS f32x2*)(lds0 + L_STAT + (64 * th + l31) * 8), st1 = *(const LAS f32x2*)(lds0 + L_STAT + (64 * th + 32 + l31) * 8);
      const float qs = ct < 5 ? 0.22360679774997896f * 1.4426950408889634f : 1.f;
      const unsigned qo = WS_QKV + (4 * tr + 2 * th) * 122880 + (16 * ct + 8 * fh + h) * 512 + l31 * 16;
      bf16x8 ax[2], bx[2]; float rr[2]; unsigned pw[8];
      auto split3 = [&](float c) {
        const float hi = rb(c), r1 = c - hi, mid = rb(r1);
        return __builtin_bit_cast(bf16x8, (u32x4){h ? 0u : pk(hi, mid), h ? 0u : pk(r1 - mid, 0.f), 0u, 0u});
      };
      auto epi = [&](int tile, int part) {
        const int f = tile & 1, t = tile >> 1;
#pragma unroll
        for (int g = 2 * part; g < 2 * part + 2; ++g) {
          float v0 = acc[f][t][2 * g] * rr[t]; asm("" : "+v"(v0));
          pw[g] = pk(v0, acc[f][t][2 * g + 1] * rr[t]);
        }
        if (part & 1) {
          const int g = part >> 1;
          auto r0 = __builtin_amdgcn_permlane32_swap(pw[4 * g], pw[4 * g + 2], false, false);
          auto r1 = __builtin_amdgcn_permlane32_swap(pw[4 * g + 1], pw[4 * g + 3], false, false);
          __builtin_amdgcn_raw_buffer_store_b128((u32x4){r0[0], r1[0], r0[1], r1[1]}, rs, qo + t * 122880 + (4 * f + 2 * g) * 512, 0, 16);
        }
      };
#pragma unroll
      for (int n = 0; n < 20; ++n) {
        const int tile = n < 8 ? n >> 2 : n < 10 ? n - 8 : n < 15 ? 2 : 3, e = n < 8 ? n & 3 : n < 10 ? 4 : (n - 10) % 5, f = tile & 1, t = tile >> 1;
        acc[f][t] = e < 4 ? MFMA32(FB[e].v[f], FB[e].v[2 + t], acc[f][t]) : MFMA32(ax[f], bx[t], acc[f][t]);
        if (n == 4) { ax[0] = split3(c0); const unsigned m = pk(-st0[0], -st0[0]); bx[0] = __builtin_bit_cast(bf16x8, (u32x4){m, m, 0u, 0u}); rr[0] = st0[1] * qs; }
        if (n == 5) { ax[1] = split3(c1); const unsigned m = pk(-st1[0], -st1[0]); bx[1] = __builtin_bit_cast(bf16x8, (u32x4){m, m, 0u, 0u}); rr[1] = st1[1] * qs; }
        if (n == 4 || n == 5) asm volatile("" : "+v"(ax[n - 4]), "+v"(bx[n - 4]), "+v"(rr[n - 4]));
        if (n >= 10) epi((n - 10) >> 2, (n - 10) & 3);
        FENCE();
        if (n == 9 || n == 13 || n == 17) FST(24 + (n - 9) / 4);
      }
      ST(2); FST(20);
      epi(2, 2); epi(2, 3);
#pragma unroll
      for (int part = 0; part < 4; ++part) epi(3, part);
      FST(21);
      WAITV(0);
      if (lane == 0 && __hip_atomic_fetch_add((LAS unsigned*)(lds0 + L_CNT), 1u, __ATOMIC_RELAXED, __HIP_MEMORY_SCOPE_WORKGROUP) == 3u)
        stg64(ws + WS_FLAG1 + (tr * 16 + ct) * 256, gran(tag, ~tag));
      ST(3); FST(22);
    } else {
      const int j = w - 4;
      auto issue = [&](int slot) { pieces(2 * j, slot); pieces(2 * j + 1, slot); sn = sn == 9 ? 0 : sn + 1; };
      issue(1); FST(29);
      if (tid == 384) *(LAS u32x4*)(lds0 + L_CNT) = (u32x4){};
      const unsigned au = tr * 15 + ct, cg = (au * 205) >> 11, kg = au - cg * 10;
      WAITV(8); FST(31); BAR();
#pragma unroll 1
      for (int s = 2; s < 2 + LAMODE; ++s) { issue(s & 3); WAITV(8); BAR(); }
      issue((2 + LAMODE) & 3);
#pragma unroll 1
      for (int s = 3 + LAMODE; s < 10; ++s) { FST(3 * s - 8); issue(s & 3); FST(3 * s - 7); WAITV(16); FST(3 * s - 6); BAR(); }
      lds_tag();
      if (w == 6) {
        const char* ga = ws + WS_STATS + tr * 1024;
        u32x2 g0, g1;
        asm volatile("global_load_dwordx2 %0, %2, %3 sc1\n\tglobal_load_dwordx2 %1, %2, %3 offset:512 sc1\n\t"
                     "s_waitcnt vmcnt(10)\n\ts_barrier\n\ts_waitcnt vmcnt(2)\n\ts_barrier\n\ts_waitcnt vmcnt(0)" : "=&v"(g0), "=&v"(g1) : "v"(lane * 8), "s"(ga) : "memory");
#pragma unroll 1
        for (unsigned sp = 0; sp < SPIN_CAP && !__all(g0[1] == tag && g1[1] == tag); ++sp) {
          g0 = __builtin_bit_cast(u32x2, ldg64(ga + lane * 8)); g1 = __builtin_bit_cast(u32x2, ldg64(ga + 512 + lane * 8)); }
        *(LAS f32x2*)(lds0 + L_STAT + lane * 8) = (f32x2){blo(g0[0]), __builtin_amdgcn_rcpf(bhi(g0[0]))};
        *(LAS f32x2*)(lds0 + L_STAT + 512 + lane * 8) = (f32x2){blo(g1[0]), __builtin_amdgcn_rcpf(bhi(g1[0]))};
        FST(23); BAR();
      } else if (w < 6) {
        unsigned pf = 0;
        const unsigned po = ((i >> 3) * 160 + j * 96 + (lane >> 1)) * 1280 + (lane & 1) * 128;
        const char* pb = (const char*)p.wo + ((cs80 * 160) & ~127);
        asm volatile("s_waitcnt vmcnt(8)\n\ts_barrier\n\tglobal_load_dword %0, %1, %2" : "+v"(pf) : "v"(po), "s"(pb) : "memory");
        asm volatile("global_load_dword %0, %1, %2" : "+v"(pf) : "v"(po + 32 * 1280), "s"(pb) : "memory");
        if (j == 0) asm volatile("global_load_dword %0, %1, %2\n\ts_waitcnt vmcnt(3)" : "+v"(pf) : "v"(po + 64 * 1280), "s"(pb) : "memory");
        else WAITV(2);
        BAR(); BAR();
        asm volatile("s_waitcnt vmcnt(0)" : "+v"(pf) :: "memory");
      }
      else {
        const unsigned fo = (lane >> 3) * 3072 + (lane & 7) * 16, fb = kg * 64 * 3072 + cg * 128;
        WAITV(8); BAR();
#pragma unroll
        for (int e = 0; e < 8; ++e) glds(fo, (const char*)p.wg + fb + e * 8 * 3072, lds0 + L_FFNA + e * 1024);
        WAITV(8); BAR();
#pragma unroll
        for (int e = 0; e < 8; ++e) glds(fo, (const char*)p.wu + fb + e * 8 * 3072, lds0 + L_FFNA + 8192 + e * 1024);
        FST(23); BAR();
        u64 tg = 0;
#pragma unroll 1
        for (unsigned sp = 0; sp < SPIN_CAP; ++sp) { tg = ldg64(ws + WS_TOP + xc * 256 + (lane & 1) * 8); if (__all((unsigned)(tg >> 32) == tag)) break; }
        if (lane < 2) *(LAS unsigned*)(lds0 + L_TOP + lane * 4) = (unsigned)tg;
        u32x4 xc0 = {ONE2, ONE2, ONE2, ONE2}, xc1 = xc0;
        if ((lane & 15) < 2) { const char* xr = (const char*)p.x + ((2047u - (unsigned)tg) & 2047u) * 1280 + kg * 128 + g4 * 16; xc0 = *(const GAS u32x4*)xr; xc1 = *(const GAS u32x4*)(xr + 64); }
        bf16x8 af[8][2];
#pragma unroll
        for (int n = 0; n < 8; ++n) { const unsigned a = lds0 + L_FFNA + (n >> 2) * 8192 + (8 * g4 + q4) * 128 + (n & 3) * 32 + 8 * p4; af[n][0] = trfrag(a, a + 512); af[n][1] = trfrag(a + 4096, a + 4608); }
#pragma unroll
        for (int n = 0; n < 8; ++n) {
          const f32x4 y = MFMA16(af[n][1], __builtin_bit_cast(bf16x8, xc1), MFMA16(af[n][0], __builtin_bit_cast(bf16x8, xc0), (f32x4){}));
          if ((lane & 15) < 3) *(LAS f32x4*)(lds0 + L_TMP + (((n >> 2) * 3 + (lane & 15)) * 64 + 16 * (n & 3) + 4 * g4) * 4) = y;
        }
#pragma unroll
        for (int n = 0; n < 6; ++n) stg64(ws + WS_PARTA + (au * 384 + n * 64 + lane) * 8, gran(*(const LAS unsigned*)(lds0 + L_TMP + (n * 64 + lane) * 4), tag));
      }
    }
  } else {
    const int sp_ = 2 * xc + i;
    if (w < 4) {
      router_load(); router_dots();
#pragma unroll
      for (int k = 0; k < 14; ++k) router_step(k);
    }
#pragma unroll 1
    for (int e = 0; e < 15; ++e) {
      const unsigned L = 64 * (8 * e + w) + lane, r = L / 80, pc = L - r * 80;
      glds(r * 1280 + (pc ^ (((r & 3) | ((r >> 1) & 4)) << 1)) * 16, (const char*)p.wd + sp_ * 96 * 1280, lds0 + (8 * e + w) * 1024);
    }
    if (tid == 384) *(LAS u32x4*)(lds0 + L_CNT) = (u32x4){};
    BAR();
    if (w >= 4) lds_tag();
    ST(2);
    if (w == 7) {
      unsigned k1 = 0, k2 = 0;
      if (i == 0) {
        __builtin_amdgcn_s_sleep(16);
#pragma unroll 1
        for (unsigned sp = 0; sp < SPIN_CAP; ++sp) { if (cand_top2(ws, lane, tag, k1, k2)) break; __builtin_amdgcn_s_sleep(4); }
        if (lane < 2) stg64(ws + WS_TOP + xc * 256 + lane * 8, gran(lane ? k2 : k1, tag));
        if (b == 0 && lane == 0) __hip_atomic_store((GAS unsigned*)(ws + WS_GEN), tag, __ATOMIC_RELAXED, __HIP_MEMORY_SCOPE_AGENT);
      } else {
        __builtin_amdgcn_s_sleep(32);
#pragma unroll 1
        for (unsigned sp = 0; sp < SPIN_CAP; ++sp) {
          const u64 v = ldg64(ws + WS_TOP + xc * 256 + (lane & 1) * 8);
          k1 = __builtin_amdgcn_readlane((unsigned)v, 0); k2 = __builtin_amdgcn_readlane((unsigned)v, 1);
          if (__all((unsigned)(v >> 32) == tag)) break;
          __builtin_amdgcn_s_sleep(4);
        }
      }
      if (lane == 0) *(LAS u32x2*)(lds0 + L_TOP) = (u32x2){k1, k2};
    }
  }
  ST(4); FST(35);
  const u64 ftag = gran(tag, ~tag);

  const int tg3 = w & 3, kh = w >> 2, tok0 = 256 * xc + 64 * (i >> 3), n0 = 80 * cs80;
  const bool two = w == 4 || w == 5;
  const unsigned it0 = tid, it1 = two ? tid + 256 : tid, tk0 = (it0 * 6554) >> 16, tk1 = (it1 * 6554) >> 16, ch0 = it0 - 10 * tk0, ch1 = it1 - 10 * tk1;
  const unsigned eo0 = (tok0 + tk0) * 1280 + n0 * 2 + ch0 * 16, eo1 = (tok0 + tk1) * 1280 + n0 * 2 + ch1 * 16;
  u32x4 xv0, xv1; u64 sg0_, sg1_;
  auto pre3 = [&]() {
    xv0 = *(const GAS u32x4*)((const char*)p.x + eo0); sg0_ = ldg64(ws + WS_STATS + (tok0 + tk0) * 8);
    xv1 = *(const GAS u32x4*)((const char*)p.x + eo1); sg1_ = ldg64(ws + WS_STATS + (tok0 + tk1) * 8);
  };

  {
    const int R = i >> 2, hd = 4 * xc + (i & 3), p0 = (5 * hd) >> 1, oh = hd & 1;
    const int pf = p0 + oh, pp = oh ? p0 : p0 + 2, cc = (lane & 1 ? p0 + 2 : p0) >> 4;
    if (w >= 4) {
      __builtin_amdgcn_s_setprio(2);
      const int j = w - 4;
      const WoOff woo = wo_offsets(lane);
      if (WO1 && !spare) {
#pragma unroll 1
        for (unsigned sp = 0; sp < SPIN_CAP; ++sp) { if (__builtin_amdgcn_readfirstlane(*(volatile LAS unsigned*)(lds0 + L_CNT)) >= 4u) break; __builtin_amdgcn_s_sleep(1); }
        wo_steps(WO1 * j, WO1 * j + WO1, woo, p.wo, cs80, lds0);
      }
      const int kt[4] = {6 + j, j < 2 ? 4 + j : 8 + j, j, 12 + j};
      { const int e = lane >> 2, T = 8 * R - 8 + (e == 0 ? kt[0] : e == 1 ? kt[1] : e == 2 ? kt[2] : kt[3]);
        const bool act = lane < 16 && T >= 0;
        poll(ws + WS_FLAG1 + (act ? ((T >> 2) * 16 + (lane & 2 ? 10 : 5) + cc) * 256 : 0), act, ftag); }
      ST(5); FST(36);
      const unsigned so0 = (80 + pf + h) * 512 + l31 * 16, so1 = (h ? (160 + pf) * 512 : (80 + pp) * 512 + 8 * oh) + l31 * 16, so2 = (h ? (160 + pp) * 512 + 8 * oh : (161 + pf) * 512) + l31 * 16;
      struct T3 { u32x4 a, b, c; };
      auto fetch = [&](int t) {
        const int T = 8 * R - 8 + t; const unsigned m = T < 0 ? 0x40000000u : 0u, o = WS_QKV + max(T, 0) * 122880;
        return T3{__builtin_amdgcn_raw_buffer_load_b128(rs, so0 | m, o, 16), __builtin_amdgcn_raw_buffer_load_b128(rs, so1 | m, o, 16), __builtin_amdgcn_raw_buffer_load_b128(rs, so2 | m, o, 16)};
      };
      auto stage = [&](int t, T3 v) {
        const unsigned kd = lds0 + L_KV + (R == 0 && t < 8 ? 7 : t) * KVS + l31 * 16;
        if (h) v.c[2] = v.c[3] = ONE2;
        *(LAS u32x4*)(kd + h * 512) = v.a; *(LAS u32x4*)(kd + 1024 + h * 512) = v.b; *(LAS u32x4*)(kd + L_V + (1 + h) * VPS) = v.c;
      };
      const T3 t0 = fetch(kt[0]), t1 = fetch(kt[1]);
      FENCE();
      stage(kt[0], t0);
      FENCE();
      const T3 t2 = fetch(kt[2]), t3 = fetch(kt[3]);
      FENCE();
      BAR(); ST(6); FST(39);
      stage(kt[1], t1);
      BAR(); FST(40);
      stage(kt[2], t2); stage(kt[3], t3);
      BAR(); FST(41);
      pre3();
      if (spare) {
        const int sp_ = 2 * xc + i;
        const u32x2 kk = *(const LAS u32x2*)(lds0 + L_TOP);
        const char* sa0 = ws + WS_STATS + ((2047u - kk[0]) & 2047u) * 8; const char* sa1 = ws + WS_STATS + ((2047u - kk[1]) & 2047u) * 8;
        const char* ga[3]; float sum[3]; u64 sg0, sg1;
#pragma unroll
        for (int r = 0; r < 3; ++r) { const unsigned e = min(144 * j + 64 * r + lane, 575), rc = e / 96, F = 96 * sp_ + e - 96 * rc; ga[r] = ws + WS_PARTA + ((F >> 6) * 3840 + rc * 64 + (F & 63)) * 8; }
#pragma unroll 1
        for (unsigned sp = 0; sp < SPIN_CAP; ++sp) {
          sg0 = ldg64(sa0); sg1 = ldg64(sa1);
          bool ok = (unsigned)(sg0 >> 32) == tag && (unsigned)(sg1 >> 32) == tag;
#pragma unroll
          for (int r = 0; r < 3; ++r) { sum[r] = 0.f;
#pragma unroll
            for (int kg = 0; kg < 10; ++kg) { const u64 u = ldg64(ga[r] + kg * 3072); ok &= (unsigned)(u >> 32) == tag; sum[r] += __uint_as_float((unsigned)u); } }
          if (__all(ok)) break;
        }
#pragma unroll
        for (int r = 0; r < 3; ++r) if (64 * r + lane < 144) *(LAS float*)(lds0 + L_TMP + (144 * j + 64 * r + lane) * 4) = sum[r];
        lds_meet(lds0 + L_CNT + 8, 4, lane);
        const unsigned hb = lds0 + L_TMP + 2304 + j * 384;
#pragma unroll
        for (int r = 0; r < 3; ++r) {
          const int e = 64 * r + lane, tk = e >= 96; const unsigned a = lds0 + L_TMP + (e - 96 * tk) * 4, sg = (unsigned)(tk ? sg1 : sg0);
          const float m = blo(sg), rd = __builtin_amdgcn_rcpf(bhi(sg));
          const float g = rb((*(const LAS float*)(a + tk * 384) - m * *(const LAS float*)(a + 768)) * rd);
          const float u = rb((*(const LAS float*)(a + 1152 + tk * 384) - m * *(const LAS float*)(a + 1920)) * rd);
          *(LAS u16*)(hb + e * 2) = (u16)pk(rb(g * __builtin_amdgcn_rcpf(1.f + __expf(-g))) * u, 0.f);
        }
        const unsigned yb = lds0 + L_HB + j * 1280;
#pragma unroll 1
        for (int n2 = 0; n2 < 2; ++n2) {
          const unsigned da = lds0 + (8 * g4 + q4) * 1280 + (((2 * j + n2) ^ (q4 | ((g4 & 1) << 2))) * 32) + 8 * p4;
          f32x4 y[5] = {};
#pragma unroll
          for (int s = 0; s < 3; ++s) {
            const bf16x8 hf = lds128(hb + (lane & 1) * 192 + g4 * 16 + s * 64);
#pragma unroll
            for (int n = 0; n < 5; ++n) y[n] = MFMA16(trfrag(da + s * 40960 + n * 256, da + s * 40960 + n * 256 + 5120), hf, y[n]);
          }
          if ((lane & 15) < 2) {
#pragma unroll
            for (int n = 0; n < 5; ++n) *(LAS f32x4*)(yb + ((lane & 15) * 160 + 32 * n + 16 * n2 + 4 * g4) * 4) = y[n];
          }
        }
#pragma unroll
        for (int n = 0; n < 5; ++n) {
          const int e = 64 * n + lane, tk = e >= 160, r = e - 160 * tk;
          stg64(ws + WS_YPART + (sp_ * 1280 + tk * 640 + 128 * (r >> 5) + 32 * j + (r & 31)) * 8, gran(*(const LAS unsigned*)(yb + e * 4), tag));
        }
        lds_meet(lds0 + L_CNT + 8, 8, lane);
        ST(3);
      }
      if (spare) wo_steps(5 * j, 5 * j + 5, woo, p.wo, cs80, lds0); else wo_steps(4 * WO1 + (5 - WO1) * j, 4 * WO1 + (5 - WO1) * (j + 1), woo, p.wo, cs80, lds0);
      FST(42);
      WAITV(0);
      FST(43);
      __builtin_amdgcn_s_setprio(0);
    } else {
      const int lo = w < 2, tu = 2 * w + 1 - lo, tv = 2 * w + lo;
      poll(ws + WS_FLAG1 + ((2 * R + (w >> 1)) * 16 + cc) * 256, lane < 2, ftag);
      ST(5); FST(36);
      bf16x8 qf[2][2];
      { const unsigned a0 = (pf + h) * 512 + l31 * 16, a1 = pp * 512 + l31 * 16 + oh * 8, ou = WS_QKV + (8 * R + tu) * 122880, ov = WS_QKV + (8 * R + tv) * 122880;
        const u32x4 u0 = __builtin_amdgcn_raw_buffer_load_b128(rs, a0, ou, 16), v0 = __builtin_amdgcn_raw_buffer_load_b128(rs, a0, ov, 16);
        const u32x2 u1 = __builtin_amdgcn_raw_buffer_load_b64(rs, a1, ou, 16), v1 = __builtin_amdgcn_raw_buffer_load_b64(rs, a1, ov, 16);
        qf[0][0] = __builtin_bit_cast(bf16x8, u0); qf[0][1] = __builtin_bit_cast(bf16x8, (u32x4){h ? 0u : u1[0], h ? 0u : u1[1], 0u, 0u});
        qf[1][0] = __builtin_bit_cast(bf16x8, v0); qf[1][1] = __builtin_bit_cast(bf16x8, (u32x4){h ? 0u : v1[0], h ? 0u : v1[1], 0u, 0u}); }
      f32x16 cz = {}, ca, cb;
#pragma unroll
      for (int r = 0; r < 16; ++r) {
        const bool le = (r & 3) + 8 * (r >> 2) + 4 * h <= l31;
        const float d = le ? 0.f : -1e30f, f = le || R == 0 ? -1e30f : 0.f;
        ca[r] = lo ? d : f; cb[r] = lo ? f : d;
      }
      const unsigned ka = lds0 + L_KV + h * 512 + l31 * 16;
      const int pi = min(4 * (g4 & 1) + p4, 5);
      const unsigned va = lds0 + L_KV + L_V + (pi >> 1) * VPS + (pi & 1) * 8 + (4 * h + q4) * 16;
      struct KF { bf16x8 a, b; };
      auto kread = [&](int kt) {
        const unsigned a = ka + kt * KVS;
        return KF{lds128(a), lds128(a + 1024)};
      };
      auto vread = [&](int kt) { const unsigned v = va + kt * KVS; return KF{trfrag(v, v + 128), trfrag(v + 256, v + 384)}; };
      auto ex = [&](const f32x16& s, int e) { return pk(__builtin_amdgcn_exp2f(s[2 * e]), __builtin_amdgcn_exp2f(s[2 * e + 1])); };
      const int P0 = lo ? 6 : 8, P1 = w == 0 ? 4 : w == 1 ? 8 : w == 2 ? 6 : 10, P2 = lo ? 2 + 2 * w : 6 + 2 * w;
      const int tp[10] = {P0, P0 + 1, P1, P1 + 1, lo ? 2 * w + 8 : 2 * w + 1, lo ? 2 * w + 9 : 2 * w, P2, P2 + 1, lo ? 2 * w + 1 : 2 * w + 8, lo ? 2 * w : 2 * w + 9};
      auto at = [&](int ps) { const int e = (ps == 1 || ps == 3 || ps == 7), t = tp[ps - e]; return (R == 0 && t < 8 ? 8 : t) + e; };
      auto setz = [&](int ps) { if (R == 0) { const float z = tp[ps] < 8 ? -1e30f : 0.f;
#pragma unroll
          for (int r = 0; r < 16; ++r) cz[r] = z; } };
      setz(0);
      asm volatile("" : "+v"(ca), "+v"(cb), "+v"(cz), "+v"(qf[0][0]), "+v"(qf[0][1]), "+v"(qf[1][0]), "+v"(qf[1][1]));
      BAR(); ST(6); FST(39);
      KF K0, K1, K2, V0, V1, V2; f32x16 S0, S1, o[2] = {};
      K0 = kread(at(0)); V0 = vread(at(0));
      S0 = MFMA32(K0.b, qf[0][1], MFMA32(K0.a, qf[0][0], cz));
      u32x4 pa = {}, pb = {}; float x0, x1, x2, x3;
#define SET3(N, p) ((p) % 3 == 0 ? N##0 : (p) % 3 == 1 ? N##1 : N##2)
#define EXP4(s, e) do { x0 = __builtin_amdgcn_exp2f(s[4 * e]); x1 = __builtin_amdgcn_exp2f(s[4 * e + 1]); x2 = __builtin_amdgcn_exp2f(s[4 * e + 2]); x3 = __builtin_amdgcn_exp2f(s[4 * e + 3]); \
                        asm volatile("" : "+v"(x0), "+v"(x1), "+v"(x2), "+v"(x3)); FENCE(); } while (0)
#define PACK2(d, e) do { d[e] = pk(x0, x1); d[e + 1] = pk(x2, x3); asm volatile("" : "+v"(d)); FENCE(); } while (0)
      auto unit = [&](auto U, f32x16& sc, f32x16& sn) {
        constexpr int u = decltype(U)::value, np = u + 1 < 18 ? UP[u + 1] : 0, nq = u + 1 < 18 ? UQ[u + 1] : 0, pu = u > 0 ? u - 1 : 0;
        if constexpr (u == 2 || u == BC_AT) { BAR(); if constexpr (u == BC_AT) pre3(); }
        FST(40 + u);
        if constexpr (u + 1 < 18) { if constexpr (np != UP[u]) setz(np); sn = MFMA32(SET3(K, np).a, qf[nq][0], u + 1 == 8 || u + 1 == 10 ? ca : u + 1 >= 16 ? cb : cz); FENCE(); }
        if constexpr (u > 0) PACK2(pb, 2);
        EXP4(sc, 0);
        if constexpr (u > 0) { o[UQ[pu]] = MFMA32(SET3(V, UP[pu]).b, __builtin_bit_cast(bf16x8, pb), o[UQ[pu]]); FENCE(); }
        if constexpr (u + 2 < 18 && UP[u + 2] != UP[u + 1]) SET3(K, UP[u + 2]) = kread(at(UP[u + 2]));
        if constexpr (u + 1 < 18 && np != UP[u]) SET3(V, np) = vread(at(np));
        PACK2(pa, 0);
        EXP4(sc, 1);
        if constexpr (u + 1 < 18) { sn = MFMA32(SET3(K, np).b, qf[nq][1], sn); FENCE(); }
        PACK2(pa, 2);
        EXP4(sc, 2);
        o[UQ[u]] = MFMA32(SET3(V, UP[u]).a, __builtin_bit_cast(bf16x8, pa), o[UQ[u]]); FENCE();
        PACK2(pb, 0);
        EXP4(sc, 3);
      };
#define UNIT2(n) unit(IC<n>{}, S0, S1); unit(IC<n + 1>{}, S1, S0);
      UNIT2(0) UNIT2(2) UNIT2(4) UNIT2(6) UNIT2(8) UNIT2(10) UNIT2(12) UNIT2(14) UNIT2(16)
      PACK2(pb, 2);
      o[0] = MFMA32(V0.b, __builtin_bit_cast(bf16x8, pb), o[0]);
      auto rowsum = [&](const f32x16& a) { auto r = __builtin_amdgcn_permlane32_swap(__float_as_uint(a[8]), __float_as_uint(a[8]), false, false); return __uint_as_float(r[1]); };
      float l[2] = {rowsum(o[1]), rowsum(o[0])};
      if (FORCE_FB || !__all(l[0] > 0x1p-60f && l[0] < 0x1p60f && l[1] > 0x1p-60f && l[1] < 0x1p60f)) {
        int qt = tu;
#pragma unroll 1
        for (int e = 0; e < 2; ++e) {
          const int k0 = R > 0 ? qt : 8;
          auto score = [&](int kt, float base) {
            f32x16 c;
#pragma unroll
            for (int r = 0; r < 16; ++r) { const bool le = (r & 3) + 8 * (r >> 2) + 4 * h <= l31; c[r] = (kt == qt + 8 && !le) || (kt == qt && le) ? -1e30f : base; }
            const KF k = kread(kt);
            return MFMA32(k.b, qf[0][1], MFMA32(k.a, qf[0][0], c));
          };
          float m = -3e38f;
#pragma unroll 1
          for (int kt = k0; kt <= qt + 8; ++kt) { const f32x16 s = score(kt, 0.f);
#pragma unroll
            for (int r = 0; r < 16; ++r) m = fmaxf(m, s[r]); }
          { auto r = __builtin_amdgcn_permlane32_swap(__float_as_uint(m), __float_as_uint(m), false, false); m = fmaxf(__uint_as_float(r[0]), __uint_as_float(r[1])); }
          o[0] = (f32x16){};
#pragma unroll 1
          for (int kt = k0; kt <= qt + 8; ++kt) {
            const f32x16 s = score(kt, -m); const KF v = vread(kt);
            o[0] = MFMA32(v.a, __builtin_bit_cast(bf16x8, (u32x4){ex(s, 0), ex(s, 1), ex(s, 2), ex(s, 3)}), o[0]);
            o[0] = MFMA32(v.b, __builtin_bit_cast(bf16x8, (u32x4){ex(s, 4), ex(s, 5), ex(s, 6), ex(s, 7)}), o[0]);
          }
          const f32x16 to = o[0]; o[0] = o[1]; o[1] = to;
          const bf16x8 t0 = qf[0][0], t1 = qf[0][1]; qf[0][0] = qf[1][0]; qf[0][1] = qf[1][1]; qf[1][0] = t0; qf[1][1] = t1;
          qt = tv;
        }
        l[0] = rowsum(o[1]); l[1] = rowsum(o[0]);
      }
      ST(7); FST(59);
#pragma unroll
      for (int e = 0; e < 2; ++e) {
        const f32x16& a = o[1 - e]; const float ri = __builtin_amdgcn_rcpf(l[e]);
        const unsigned e0 = pk(a[0] * ri, a[1] * ri), e1 = pk(a[2] * ri, a[3] * ri), e2 = pk(a[4] * ri, a[5] * ri), e3 = pk(a[6] * ri, a[7] * ri);
        const u32x2 el = {pk(a[8] * ri, a[9] * ri), pk(a[10] * ri, a[11] * ri)};
        auto r0 = __builtin_amdgcn_permlane32_swap(e0, e2, false, false), r1 = __builtin_amdgcn_permlane32_swap(e1, e3, false, false);
        const unsigned ob = WS_O + (16 * R + 2 * (e ? tu : tv) + (l31 >> 4)) * 20480;
        __builtin_amdgcn_raw_buffer_store_b128((u32x4){r0[0], r1[0], r0[1], r1[1]}, rs, ob + (2 * hd + h) * 256 + (lane & 15) * 16, 0, 16);
        if (h == 0) __builtin_amdgcn_raw_buffer_store_b64(el, rs, ob + 16384 + hd * 128 + (lane & 15) * 8, 0, 16);
      }
      WAITV(0);
      if (lane == 0 && __hip_atomic_fetch_add((LAS unsigned*)(lds0 + L_CNT + 4), 1u, __ATOMIC_RELAXED, __HIP_MEMORY_SCOPE_WORKGROUP) == 3u)
        stg64(ws + WS_FLAG2 + (R * 32 + hd) * 256, ftag);
      ST(8); FST(60);
    }
    BAR();
  }

  {
    const int tg = tg3;
    const unsigned wa = lds0 + L_WO + (kh * 320 + 4 * g4 + q4) * 160 + 8 * p4;
    constexpr int NPRE = 6;
    bf16x8 wf[NPRE][5];
#pragma unroll
    for (int s = 0; s < NPRE; ++s)
#pragma unroll
      for (int n = 0; n < 5; ++n) wf[s][n] = trfrag(wa + s * 5120 + n * 32, wa + s * 5120 + 2560 + n * 32);
#pragma unroll 1
    for (unsigned sp = 0; sp < SPIN_CAP && !__all((unsigned)(sg0_ >> 32) == tag && (unsigned)(sg1_ >> 32) == tag); ++sp) {
      sg0_ = ldg64(ws + WS_STATS + (tok0 + tk0) * 8); sg1_ = ldg64(ws + WS_STATS + (tok0 + tk1) * 8); }
    f32x4 base[4];
#pragma unroll
    for (int e = 0; e < 4; ++e) {
      const unsigned sg = (unsigned)(e < 2 ? sg0_ : sg1_); const float m = blo(sg), rd = __builtin_amdgcn_rcpf(bhi(sg));
#pragma unroll
      for (int c = 0; c < 4; ++c) { const unsigned u = (e < 2 ? xv0 : xv1)[2 * (e & 1) + (c >> 1)]; const float v = c & 1 ? bhi(u) : blo(u); base[e][c] = v + (v - m) * rd; }
    }
    const u32x2 kk12 = *(const LAS u32x2*)(lds0 + L_TOP);
    const unsigned k1 = __builtin_amdgcn_readfirstlane(kk12[0]), k2 = __builtin_amdgcn_readfirstlane(kk12[1]);
    const unsigned i1 = 2047u - (k1 & 0xffffu) - tok0, i2 = 2047u - (k2 & 0xffffu) - tok0;
    if ((i1 < 64u || i2 < 64u) && w < 3) {
      const int c = tid >= 80, col = tid - 80 * c;
      const char* ya = ws + WS_YPART + (c * 640 + n0 + (tid < 160 ? col : 0)) * 8;
      float s = 0.f;
#pragma unroll 1
      for (unsigned sp = 0; sp < SPIN_CAP; ++sp) {
        bool ok = true; s = 0.f;
#pragma unroll 8
        for (int e = 0; e < 16; ++e) { u64 v = ldg64(ya + e * 10240); ok &= (unsigned)(v >> 32) == tag; s += __uint_as_float((unsigned)v); }
        if (__all(ok)) break;
      }
      const unsigned kk = (c ? k2 : k1) >> 16;
      const float r = blo(kk & 0x8000u ? kk & 0x7fffu : ~kk & 0xffffu);
      if (tid < 160) *(LAS float*)(lds0 + L_UPD + tid * 4) = rb(rb(s) * rb(__builtin_amdgcn_rcpf(1.f + __expf(-r))));
    }
#pragma unroll
    for (int s = 0; s < NPRE; ++s)
#pragma unroll
      for (int n = 0; n < 5; ++n) asm volatile("" : "+v"(wf[s][n]));
    asm volatile("" : "+v"(base[0]), "+v"(base[1]), "+v"(base[2]), "+v"(base[3]));
    poll(ws + WS_FLAG2 + (xc * 32 + 16 * kh + (lane & 15)) * 256, lane < 16, ftag);
    ST(9); FST(61);
    const unsigned ob = WS_O + (tok0 / 16 + tg) * 20480;
    u32x4 of[10];
    auto oload = [&](int s) {
      if (s < 8) of[s] = __builtin_amdgcn_raw_buffer_load_b128(rs, lane * 16 + s * 1024, ob + kh * 8192, 16);
      else {
        const unsigned a = (8 * (s - 8) + 2 * g4) * 128 + (lane & 15) * 8;
        const u32x2 u0 = __builtin_amdgcn_raw_buffer_load_b64(rs, a, ob + 16384 + kh * 2048, 16), u1 = __builtin_amdgcn_raw_buffer_load_b64(rs, a + 128, ob + 16384 + kh * 2048, 16);
        of[s] = (u32x4){u0[0], u0[1], u1[0], u1[1]};
      }
    };
#pragma unroll
    for (int s = 0; s < OPRE; ++s) oload(s);
    FENCE();
    FST(62);
    f32x4 acc[5] = {};
#pragma unroll
    for (int s = 0; s < 10; ++s) {
      if (s + OPRE < 10) oload(s + OPRE);
      FENCE();
#pragma unroll
      for (int n = 0; n < 5; ++n) acc[n] = MFMA16(wf[s % NPRE][n], __builtin_bit_cast(bf16x8, of[s]), acc[n]);
      if (s + NPRE < 10) {
#pragma unroll
        for (int n = 0; n < 5; ++n) wf[s][n] = trfrag(wa + (s + NPRE) * 5120 + n * 32, wa + (s + NPRE) * 5120 + 2560 + n * 32);
      }
      FENCE();
    }
    ST(10); FST(63);
#pragma unroll
    for (int n = 0; n < 5; ++n) *(LAS f32x4*)(lds0 + L_RED + kh * 21504 + (16 * tg + (lane & 15)) * 336 + (16 * n + 4 * g4) * 4) = acc[n];
    BAR();
    auto fin = [&](unsigned tk, unsigned ch, unsigned eo, f32x4 b0, f32x4 b1) {
      const unsigned a = lds0 + L_RED + tk * 336 + ch * 32;
      b0 += *(const LAS f32x4*)a + *(const LAS f32x4*)(a + 21504); b1 += *(const LAS f32x4*)(a + 16) + *(const LAS f32x4*)(a + 21504 + 16);
      if (tk == i1 || tk == i2) { const unsigned u = lds0 + L_UPD + (tk == i1 ? 0 : 320) + ch * 32; b0 += *(const LAS f32x4*)u; b1 += *(const LAS f32x4*)(u + 16); }
      *(GAS u32x4*)((char*)p.out + eo) = (u32x4){pk(b0[0], b0[1]), pk(b0[2], b0[3]), pk(b1[0], b1[1]), pk(b1[2], b1[3])};
    };
    fin(tk0, ch0, eo0, base[0], base[1]);
    if (two) fin(tk1, ch1, eo1, base[2], base[3]);
    ST(11);
  }
}

extern "C" void kernel_launch(void* const* d_in, const int* in_sizes, int n_in, void* d_out, int out_size, void* d_ws, size_t ws_size,
                              hipStream_t stream) {
  Params p;
  p.x = (const u16*)d_in[0]; p.wqkv = (const u16*)d_in[1]; p.wo = (const u16*)d_in[2]; p.wr = (const u16*)d_in[4];
  p.wg = (const u16*)d_in[5]; p.wu = (const u16*)d_in[6]; p.wd = (const u16*)d_in[7]; p.out = (u16*)d_out; p.ws = (char*)d_ws;
  fused_block<<<256, 512, 0, stream>>>(p);
}
```

```cpp
#define STAMP
#include <hip/hip_runtime.h>
#include <stdint.h>

typedef unsigned short u16;
typedef unsigned long long u64;
typedef short bf16x8 __attribute__((ext_vector_type(8)));
typedef short s16x4 __attribute__((ext_vector_type(4)));
typedef float f32x2 __attribute__((ext_vector_type(2)));
typedef float f32x4 __attribute__((ext_vector_type(4)));
typedef float f32x16 __attribute__((ext_vector_type(16)));
typedef unsigned u32x2 __attribute__((ext_vector_type(2)));
typedef unsigned u32x4 __attribute__((ext_vector_type(4)));
typedef __bf16 bf2 __attribute__((ext_vector_type(2)));
#define LAS __attribute__((address_space(3)))
#define GAS __attribute__((address_space(1)))
#define DI __device__ __forceinline__

constexpr unsigned WS_GEN = 0, WS_FLAG1 = 4096, WS_FLAG2 = 131072, WS_CAND = 262144, WS_STATS = 278528, WS_TOP = 294912, WS_PARTA = 524288,
                   WS_YPART = 1572864, WS_QKV = 2097152, WS_O = 10485760, WS_END = 13631488, WS_STAMP = 16777216;
constexpr unsigned L_WO = 0, L_KV = 102400, L_FFNA = 65536;
constexpr unsigned KVS = 3264, L_V = 1536, VPS = 576;
constexpr unsigned L_RED = L_KV, L_UPD = L_KV + 43008;
constexpr unsigned L_KEY = 154624, L_TOP = L_KEY + 32, L_CNT = L_KEY + 48  , L_STAT = L_KEY + 64, L_CS = L_STAT + 1024, L_TMP = L_CS + 1024;
constexpr unsigned L_HB = 122880;
constexpr unsigned SPIN_CAP = 1u << 20;
constexpr unsigned ONE2 = 0x3f803f80u;
#define CODE_KB 30

constexpr int UP[20] = {0, 0, 1, 1, 2, 2, 3, 3, 4, 4, 5, 6, 6, 7, 7, 8, 8, 9, 9, 9}, UQ[18] = {0, 1, 0, 1, 0, 1, 0, 1, 0, 1, 1, 0, 1, 0, 1, 0, 1, 0};
template <int N> struct IC { static constexpr int value = N; };

struct Params { const u16 *x, *wqkv, *wo, *wr, *wg, *wu, *wd; u16* out; char* ws; };

#define ST(i) do { if (lane == 0) { GAS u64* sp_ = (GAS u64*)(p.ws + WS_STAMP) + ((blockIdx.x * 8 + w) * 48 + (i)) * 2; sp_[0] = __builtin_amdgcn_s_memrealtime(); sp_[1] = __builtin_amdgcn_s_memtime(); } } while (0)

#define FST(i) do {} while (0)
#define WAITV(n) asm volatile("s_waitcnt vmcnt(" #n ")" ::: "memory")
#define REQ0 3
#define LA 3
#define LAMODE 1
#define BC_AT 7
#define FORCE_FB 0
#define WO1 2
#define OPRE 4
#define PRIO 0
#define WAITN_(n) WAITV(n)
#define WAITN(n) WAITN_(n)
#define WO_PRE 6
#define BAR() do { __builtin_amdgcn_s_waitcnt(0xC07F); asm volatile("s_barrier" ::: "memory"); } while (0)
#define FENCE() __builtin_amdgcn_sched_barrier(0)
#define MFMA32(a, b, c) __builtin_amdgcn_mfma_f32_32x32x16_bf16((a), (b), (c), 0, 0, 0)
#define MFMA16(a, b, c) __builtin_amdgcn_mfma_f32_16x16x32_bf16((a), (b), (c), 0, 0, 0)

DI void glds(unsigned voff, const void* sbase, unsigned lds) {
  asm volatile("s_mov_b32 m0, %2\n\ts_nop 0\n\tglobal_load_lds_dwordx4 %0, %1" :: "v"(voff), "s"(sbase), "s"(lds) : "memory");
}
DI bf16x8 lds128(unsigned a) { return *(const LAS bf16x8*)a; }
DI s16x4 ldstr(unsigned a) { return __builtin_amdgcn_ds_read_tr16_b64_v4i16((LAS s16x4*)a); }
DI bf16x8 trfrag(unsigned a, unsigned a2) { s16x4 lo = ldstr(a), hi = ldstr(a2); return __builtin_shufflevector(lo, hi, 0, 1, 2, 3, 4, 5, 6, 7); }
DI unsigned pk(float a, float b) { f32x2 v = {a, b}; return __builtin_bit_cast(unsigned, __builtin_convertvector(v, bf2)); }
DI float rb(float a) { return __uint_as_float(pk(a, 0.f) << 16); }
DI float blo(unsigned v) { return __uint_as_float(v << 16); }
DI float bhi(unsigned v) { return __uint_as_float(v & 0xffff0000u); }
DI float dot2(unsigned a, unsigned b, float c) { return __builtin_amdgcn_fdot2_f32_bf16(__builtin_bit_cast(bf2, a), __builtin_bit_cast(bf2, b), c, false); }
DI float xhalf_sum(float v) { auto r = __builtin_amdgcn_permlane32_swap(__float_as_uint(v), __float_as_uint(v), false, false); return __uint_as_float(r[0]) + __uint_as_float(r[1]); }
DI u64 ldg64(const char* a) { return __hip_atomic_load((GAS u64*)a, __ATOMIC_RELAXED, __HIP_MEMORY_SCOPE_AGENT); }
DI void stg64(char* a, u64 v) { __hip_atomic_store((GAS u64*)a, v, __ATOMIC_RELAXED, __HIP_MEMORY_SCOPE_AGENT); }
DI u64 gran(unsigned v, unsigned tag) { return ((u64)tag << 32) | v; }
template <int CTRL> DI unsigned dpp(unsigned v) { return __builtin_amdgcn_update_dpp(0u, v, CTRL, 0xf, 0xf, true); }
DI unsigned wave_umax(unsigned v) {
  v = max(v, dpp<0x128>(v)); v = max(v, dpp<0x124>(v)); v = max(v, dpp<0x122>(v)); v = max(v, dpp<0x121>(v));
  auto r = __builtin_amdgcn_permlane16_swap(v, v, false, false); v = max(r[0], r[1]);
  auto q = __builtin_amdgcn_permlane32_swap(v, v, false, false); return max(q[0], q[1]);
}
DI float sum32(float v) {
  v += __uint_as_float(dpp<0x128>(__float_as_uint(v))); v += __uint_as_float(dpp<0x124>(__float_as_uint(v)));
  v += __uint_as_float(dpp<0x122>(__float_as_uint(v))); v += __uint_as_float(dpp<0x121>(__float_as_uint(v)));
  auto r = __builtin_amdgcn_permlane16_swap(__float_as_uint(v), __float_as_uint(v), false, false);
  return __uint_as_float(r[0]) + __uint_as_float(r[1]);
}
DI void ln_stats(float s1, float s2, float& mb, float& db) {
  float mean = s1 * (1.f / 640.f); float var = fmaxf(s2 * (1.f / 640.f) - mean * mean, 0.f);
  mb = rb(mean); db = rb(__builtin_amdgcn_sqrtf(rb(rb(var) + 1e-5f)));
}
DI void poll(const char* addr, bool act, u64 want) {
#pragma unroll 1
  for (unsigned sp = 0; sp < SPIN_CAP; ++sp) { u64 v = ldg64(addr); if (__all(!act || v == want)) break; __builtin_amdgcn_s_sleep(1); }
  asm volatile("" ::: "memory");
}
DI bool cand_top2(const char* ws, int lane, unsigned tag, unsigned& k1, unsigned& k2) {
  bool ok = true; unsigned a = 0, b = 0;
#pragma unroll 2
  for (int e0 = 0; e0 < 32; e0 += 16) {
    u64 g[16];
#pragma unroll
    for (int e = 0; e < 16; ++e) g[e] = ldg64(ws + WS_CAND + (e0 + e) * 512 + lane * 8);
#pragma unroll
    for (int e = 0; e < 16; ++e) { const unsigned v = (unsigned)g[e]; ok &= (unsigned)(g[e] >> 32) == tag; b = max(b, min(a, v)); a = max(a, v); }
  }
  if (!__all(ok)) return false;
  k1 = wave_umax(a); k2 = wave_umax(a == k1 ? b : a);
  k1 = __builtin_amdgcn_readfirstlane(k1); k2 = __builtin_amdgcn_readfirstlane(k2);
  return true;
}
struct WoOff { unsigned r[5], l[5]; };
DI WoOff wo_offsets(int lane) {
  WoOff o;
#pragma unroll
  for (int m = 0; m < 5; ++m) {
    const unsigned L = 64 * m + lane, rho = (L * 205) >> 11, c = L - rho * 10, q = rho & 3, g = (rho >> 2) & 3, t = rho >> 4;
    o.r[m] = (24 * (g >> 1) + 8 * (g & 1) + 4 * t + q) * 1280 + c * 16; o.l[m] = (40 * g + 16 + 4 * t + q) * 1280 + c * 16;
    asm volatile("" : "+v"(o.r[m]), "+v"(o.l[m]));
  }
  return o;
}
DI void wo_steps(int s0, int s1, const WoOff& o, const u16* wo, int cs80, unsigned lds0) {
#pragma unroll 1
  for (int u = s0; u < s1; ++u) {
    const int kh = u >= 10, st = u - 10 * kh;
    const char* sp = (const char*)wo + cs80 * 160 + (kh * 320 + (st < 8 ? st * 40 : (st - 8) * 160)) * 1280;
    const unsigned d = lds0 + L_WO + u * 5120;
    if (st < 8) {
#pragma unroll
      for (int m = 0; m < 5; ++m) glds(o.r[m], sp, d + m * 1024);
    } else {
#pragma unroll
      for (int m = 0; m < 5; ++m) glds(o.l[m], sp, d + m * 1024);
    }
  }
}
DI void lds_meet(unsigned addr, unsigned want, int lane) {
  __builtin_amdgcn_s_waitcnt(0xC07F);
  if (lane == 0) __hip_atomic_fetch_add((LAS unsigned*)addr, 1u, __ATOMIC_RELAXED, __HIP_MEMORY_SCOPE_WORKGROUP);
#pragma unroll 1
  for (unsigned sp = 0; sp < SPIN_CAP; ++sp) { if (__builtin_amdgcn_readfirstlane(*(volatile LAS unsigned*)addr) >= want) break; __builtin_amdgcn_s_sleep(1); }
  asm volatile("" ::: "memory");
}

__global__ __launch_bounds__(512, 2) void fused_block(Params p) {
  __shared__ __attribute__((aligned(1024))) char smem[163840];
  const unsigned lds0 = (unsigned)(size_t)smem;
  const int tid = threadIdx.x, lane = tid & 63, w = __builtin_amdgcn_readfirstlane(tid >> 6);
  const int b = blockIdx.x, xc = b & 7, i = b >> 3;
  const bool spare = i < 2;
  const int xp = xc >> 1, odd = xc & 1;
  const int cs80 = i & 7;
  char* const ws = p.ws;
  const auto rs = __builtin_amdgcn_make_buffer_rsrc(ws, 0, WS_END, 0x00020000);
  const int h = lane >> 5, l31 = lane & 31, g4 = lane >> 4, q4 = (lane & 15) >> 2, p4 = lane & 3;
  unsigned tag = 0;
  auto lds_tag = [&]() { tag = __builtin_amdgcn_readfirstlane(*(const LAS unsigned*)(lds0 + L_KEY)); };
  ST(0);
  const unsigned j30 = (unsigned)(i - 2) & 31u, j7 = (j30 * 37) >> 8;
  const unsigned trl = j30 < 28 ? j7 : (2 * odd + j30) & 3, ct = j30 < 28 ? j30 - j7 * 7 + odd * 8 : 7;
  unsigned sn = 3 * (trl + ct - 3 * (((trl + ct) * 43) >> 7)) + ((xc * 3) >> 3);
  const unsigned tr = 4 * xp + trl;
  const int rb8 = 64 * xp + 32 * odd + i;
  const char* const xt = (const char*)p.x + tr * (128 * 1280);
  const char* const wt = (const char*)p.wqkv + ct * 256;
  const unsigned x0 = (lane >> 3) * 1280 + (((lane & 7) ^ g4) * 16), x1 = (8 + (lane >> 3)) * 1280 + (((lane & 7) ^ (4 + g4)) * 16);
  const unsigned w0 = g4 * 3840 + (((lane & 15) ^ (g4 << 2)) * 16), w1 = w0 + 4 * 3840;
  auto pieces = [&](unsigned e, unsigned slot) {
    const char* xs = xt + (sn * 128 + e * (16 * 1280)); const char* wsb = wt + (sn * 64 + e * 8) * 3840; const unsigned d = lds0 + slot * 32768 + e * 2048;
    glds(x0, xs, d); glds(w0, wsb, d + 16384); glds(x1, xs, d + 1024); glds(w1, wsb, d + 16384 + 1024);
  };
  if (!spare) { pieces(w, 0); sn = sn == 9 ? 0 : sn + 1; }
  FST(28);

  const int rrow = 8 * rb8 + 2 * w + h;
  u32x4 rx[3], rw[3]; unsigned pfv = 0, genv = 0; float a1 = 0.f, a2 = 0.f, ar = 0.f, sw = 0.f;
  auto router_load = [&]() {
    if (CODE_KB && lane < CODE_KB / 2) asm volatile("global_load_dword %0, %1, %2" : "+v"(pfv) : "v"(((i & 3) * 4 + w) * (CODE_KB * 64) + lane * 128), "s"(__builtin_amdgcn_s_getpc()) : "memory");
    const char* xr = (const char*)p.x + rrow * 1280 + l31 * 16; const char* wr = (const char*)p.wr + l31 * 16;
    const int t3 = l31 < 16 ? 1024 : 0;
    rx[0] = *(const GAS u32x4*)xr; rx[1] = *(const GAS u32x4*)(xr + 512); rw[0] = *(const GAS u32x4*)wr; rw[1] = *(const GAS u32x4*)(wr + 512);
    rx[2] = *(const GAS u32x4*)(xr + t3); rw[2] = *(const GAS u32x4*)(wr + t3);
    genv = __hip_atomic_load((GAS unsigned*)(ws + WS_GEN), __ATOMIC_RELAXED, __HIP_MEMORY_SCOPE_AGENT);
    FENCE();
  };
  auto router_dots = [&]() {
    if (l31 >= 16) rx[2] = rw[2] = (u32x4){};
#pragma unroll
    for (int e = 0; e < 3; ++e)
#pragma unroll
      for (int c = 0; c < 4; ++c) { a1 = dot2(rx[e][c], ONE2, a1); a2 = dot2(rx[e][c], rx[e][c], a2); ar = dot2(rx[e][c], rw[e][c], ar); sw = dot2(rw[e][c], ONE2, sw); }
    tag = __builtin_amdgcn_readfirstlane(genv) + 1u;
    if (tid == 0) *(LAS unsigned*)(lds0 + L_KEY) = tag;
    asm volatile("" : "+v"(a1), "+v"(a2), "+v"(ar), "+v"(sw) : "v"(pfv));
  };
  float rmb = 0.f, rdb = 0.f; unsigned rk = 0, rv = 0;
  auto router_step = [&](int k) {
    auto fdpp = [&](float& v, int st) { const unsigned u = __float_as_uint(v); v += __uint_as_float(st == 0 ? dpp<0x128>(u) : st == 1 ? dpp<0x124>(u) : st == 2 ? dpp<0x122>(u) : dpp<0x121>(u)); };
    auto fswp = [&](float& v) { auto r = __builtin_amdgcn_permlane16_swap(__float_as_uint(v), __float_as_uint(v), false, false); v = __uint_as_float(r[0]) + __uint_as_float(r[1]); };
    if (k < 4) { fdpp(a1, k); fdpp(a2, k); fdpp(ar, k); fdpp(sw, k); }
    if (k == 4) { fswp(a1); fswp(a2); }
    if (k == 5) { fswp(ar); fswp(sw); }
    if (k == 6) { a1 *= 1.f / 640.f; a2 = fmaxf(a2 * (1.f / 640.f) - a1 * a1, 0.f); }
    if (k == 7) { rmb = rb(a1); a2 = rb(a2) + 1e-5f; }
    if (k == 8) { a2 = __builtin_amdgcn_sqrtf(rb(a2)); ar -= rmb * sw; }
    if (k == 9) { rdb = rb(a2); a2 = __builtin_amdgcn_rcpf(rdb); }
    if (k == 10) { rk = pk(ar * a2, 0.f) & 0xffffu; rv = (__float_as_uint(rmb) >> 16) | __float_as_uint(rdb); }
    if (k == 11) rk = (rk & 0x8000u ? (~rk & 0xffffu) : (rk | 0x8000u)) << 16;
    if (k == 12) rv = l31 ? rk | (2047u - rrow) : rv;
    if (k == 13) { if (l31 < 2) __builtin_amdgcn_raw_buffer_store_b64((u32x2){rv, tag}, rs, (l31 ? WS_CAND : WS_STATS) + rrow * 8, 0, 16); }
    asm volatile("" : "+v"(a1), "+v"(a2), "+v"(ar), "+v"(sw), "+v"(rmb), "+v"(rdb), "+v"(rk), "+v"(rv));
  };

  if (!spare) {
    if (w < 4) {
      router_load();
      const int fh = w >> 1, th = w & 1;
      unsigned xa[4], wa[2], ko[4];
#pragma unroll
      for (int e = 0; e < 4; ++e) { const int ks = (2 * th + e) & 3, row = 64 * th + l31; xa[e] = lds0 + row * 128 + (((2 * ks + h) ^ ((row >> 1) & 7)) * 16); ko[e] = ks * 4096; }
#pragma unroll
      for (int f = 0; f < 2; ++f) wa[f] = lds0 + 16384 + (8 * h + q4) * 256 + (((4 * (2 * fh + f) + 2 * (g4 & 1) + (p4 >> 1)) ^ (q4 << 2)) * 16) + 8 * (p4 & 1);
      f32x16 acc[2][2] = {};
      f32x4 cs0 = {}, cs1 = {};
      const unsigned selw = (lane & 15) == (g4 & 1) ? ONE2 : 0u;
      const bf16x8 sel = __builtin_bit_cast(bf16x8, (u32x4){selw, selw, selw, selw});
      struct Fr { bf16x8 v[4]; };
      Fr FA[4], FB[4];
      auto rd = [&](Fr* F, int n, unsigned so) {
        const int e = n >> 2, j = n & 3;
        if (j < 2) { const unsigned a = wa[j] + (so + ko[e]); F[e].v[j] = trfrag(a, a + 1024); } else F[e].v[j] = lds128(xa[e] + so + (j - 2) * 4096);
      };
      auto colsum = [&](const Fr* C, int e) { cs0 = MFMA16(C[e].v[0], sel, cs0); cs1 = MFMA16(C[e].v[1], sel, cs1); };
      auto body = [&](Fr* N, const Fr* C, unsigned so, int r0 = 99) {
#pragma unroll
        for (int n = 0; n < 16; ++n) {
          const int e = n >> 2, f = (n >> 1) & 1, t = n & 1;
          acc[f][t] = MFMA32(C[e].v[f], C[e].v[2 + t], acc[f][t]);
          if (n < 4) { rd(N, 2 * n, so); rd(N, 2 * n + 1, so); } else if (n < 12) rd(N, n + 4, so);
          if (n >= 4 && r0 + n - 4 < 14) router_step(r0 + n - 4);
          FENCE();
        }
        colsum(C, 0); colsum(C, 1);
        FENCE();
      };
      if (CODE_KB) WAITV(8); else WAITV(7);
      BAR(); ST(1);
#pragma unroll
      for (int n = 0; n < 16; ++n) rd(FA, n, 0);
      router_dots();
      BAR();
#pragma unroll
      for (int s = 1; s <= 7; s += 2) { FST(2 * s); body(FB, FA, (s & 3) * 32768, s == 1 ? 0 : 99); FST(2 * s + 1); BAR(); FST(2 * s + 2); body(FA, FB, ((s + 1) & 3) * 32768, s == 1 ? 12 : 99); FST(2 * s + 3); BAR(); }
      FST(18);
      body(FB, FA, 32768);
      colsum(FB, 0); colsum(FB, 1);
      if ((lane & 15) < 2) { const unsigned d = lds0 + L_CS + w * 256 + ((lane & 15) * 16 + 4 * g4) * 4; *(LAS f32x4*)d = cs0; *(LAS f32x4*)(d + 128) = cs1; }
      FST(23); BAR(); FST(19);
      const unsigned ca = lds0 + L_CS + fh * 512 + l31 * 4;
      const float c0 = *(const LAS float*)ca + *(const LAS float*)(ca + 256), c1 = *(const LAS float*)(ca + 128) + *(const LAS float*)(ca + 384);
      const f32x2 st0 = *(const LAS f32x2*)(lds0 + L_STAT + (64 * th + l31) * 8), st1 = *(const LAS f32x2*)(lds0 + L_STAT + (64 * th + 32 + l31) * 8);
      const float qs = ct < 5 ? 0.22360679774997896f * 1.4426950408889634f : 1.f;
      const unsigned qo = WS_QKV + (4 * tr + 2 * th) * 122880 + (16 * ct + 8 * fh + h) * 512 + l31 * 16;
      bf16x8 ax[2], bx[2]; float rr[2]; unsigned pw[8];
      auto split3 = [&](float c) {
        const float hi = rb(c), r1 = c - hi, mid = rb(r1);
        return __builtin_bit_cast(bf16x8, (u32x4){h ? 0u : pk(hi, mid), h ? 0u : pk(r1 - mid, 0.f), 0u, 0u});
      };
      auto epi = [&](int tile, int part) {
        const int f = tile & 1, t = tile >> 1;
#pragma unroll
        for (int g = 2 * part; g < 2 * part + 2; ++g) {
          float v0 = acc[f][t][2 * g] * rr[t]; asm("" : "+v"(v0));
          pw[g] = pk(v0, acc[f][t][2 * g + 1] * rr[t]);
        }
        if (part & 1) {
          const int g = part >> 1;
          auto r0 = __builtin_amdgcn_permlane32_swap(pw[4 * g], pw[4 * g + 2], false, false);
          auto r1 = __builtin_amdgcn_permlane32_swap(pw[4 * g + 1], pw[4 * g + 3], false, false);
          __builtin_amdgcn_raw_buffer_store_b128((u32x4){r0[0], r1[0], r0[1], r1[1]}, rs, qo + t * 122880 + (4 * f + 2 * g) * 512, 0, 16);
        }
      };
#pragma unroll
      for (int n = 0; n < 20; ++n) {
        const int tile = n < 8 ? n >> 2 : n < 10 ? n - 8 : n < 15 ? 2 : 3, e = n < 8 ? n & 3 : n < 10 ? 4 : (n - 10) % 5, f = tile & 1, t = tile >> 1;
        acc[f][t] = e < 4 ? MFMA32(FB[e].v[f], FB[e].v[2 + t], acc[f][t]) : MFMA32(ax[f], bx[t], acc[f][t]);
        if (n == 4) { ax[0] = split3(c0); const unsigned m = pk(-st0[0], -st0[0]); bx[0] = __builtin_bit_cast(bf16x8, (u32x4){m, m, 0u, 0u}); rr[0] = st0[1] * qs; }
        if (n == 5) { ax[1] = split3(c1); const unsigned m = pk(-st1[0], -st1[0]); bx[1] = __builtin_bit_cast(bf16x8, (u32x4){m, m, 0u, 0u}); rr[1] = st1[1] * qs; }
        if (n == 4 || n == 5) asm volatile("" : "+v"(ax[n - 4]), "+v"(bx[n - 4]), "+v"(rr[n - 4]));
        if (n >= 10) epi((n - 10) >> 2, (n - 10) & 3);
        FENCE();
        if (n == 9 || n == 13 || n == 17) FST(24 + (n - 9) / 4);
      }
      ST(2); FST(20);
      epi(2, 2); epi(2, 3);
#pragma unroll
      for (int part = 0; part < 4; ++part) epi(3, part);
      FST(21);
      WAITV(0);
      if (lane == 0 && __hip_atomic_fetch_add((LAS unsigned*)(lds0 + L_CNT), 1u, __ATOMIC_RELAXED, __HIP_MEMORY_SCOPE_WORKGROUP) == 3u)
        stg64(ws + WS_FLAG1 + (tr * 16 + ct) * 256, gran(tag, ~tag));
      ST(3); FST(22);
    } else {
      const int j = w - 4;
      auto issue = [&](int slot) { pieces(2 * j, slot); pieces(2 * j + 1, slot); sn = sn == 9 ? 0 : sn + 1; };
      issue(1); FST(29);
      if (tid == 384) *(LAS u32x4*)(lds0 + L_CNT) = (u32x4){};
      const unsigned au = tr * 15 + ct, cg = (au * 205) >> 11, kg = au - cg * 10;
      WAITV(8); FST(31); BAR();
#pragma unroll 1
      for (int s = 2; s < 2 + LAMODE; ++s) { issue(s & 3); WAITV(8); BAR(); }
      issue((2 + LAMODE) & 3);
#pragma unroll 1
      for (int s = 3 + LAMODE; s < 10; ++s) { FST(3 * s - 8); issue(s & 3); FST(3 * s - 7); WAITV(16); FST(3 * s - 6); BAR(); }
      lds_tag();
      if (w == 6) {
        const char* ga = ws + WS_STATS + tr * 1024;
        u32x2 g0, g1;
        asm volatile("global_load_dwordx2 %0, %2, %3 sc1\n\tglobal_load_dwordx2 %1, %2, %3 offset:512 sc1\n\t"
                     "s_waitcnt vmcnt(10)\n\ts_barrier\n\ts_waitcnt vmcnt(2)\n\ts_barrier\n\ts_waitcnt vmcnt(0)" : "=&v"(g0), "=&v"(g1) : "v"(lane * 8), "s"(ga) : "memory");
#pragma unroll 1
        for (unsigned sp = 0; sp < SPIN_CAP && !__all(g0[1] == tag && g1[1] == tag); ++sp) {
          g0 = __builtin_bit_cast(u32x2, ldg64(ga + lane * 8)); g1 = __builtin_bit_cast(u32x2, ldg64(ga + 512 + lane * 8)); }
        *(LAS f32x2*)(lds0 + L_STAT + lane * 8) = (f32x2){blo(g0[0]), __builtin_amdgcn_rcpf(bhi(g0[0]))};
        *(LAS f32x2*)(lds0 + L_STAT + 512 + lane * 8) = (f32x2){blo(g1[0]), __builtin_amdgcn_rcpf(bhi(g1[0]))};
        FST(23); BAR();
      } else if (w < 6) {
        unsigned pf = 0;
        const unsigned po = ((i >> 3) * 160 + j * 96 + (lane >> 1)) * 1280 + (lane & 1) * 128;
        const char* pb = (const char*)p.wo + ((cs80 * 160) & ~127);
        asm volatile("s_waitcnt vmcnt(8)\n\ts_barrier\n\tglobal_load_dword %0, %1, %2" : "+v"(pf) : "v"(po), "s"(pb) : "memory");
        asm volatile("global_load_dword %0, %1, %2" : "+v"(pf) : "v"(po + 32 * 1280), "s"(pb) : "memory");
        if (j == 0) asm volatile("global_load_dword %0, %1, %2\n\ts_waitcnt vmcnt(3)" : "+v"(pf) : "v"(po + 64 * 1280), "s"(pb) : "memory");
        else WAITV(2);
        BAR(); BAR();
        asm volatile("s_waitcnt vmcnt(0)" : "+v"(pf) :: "memory");
      }
      else {
        const unsigned fo = (lane >> 3) * 3072 + (lane & 7) * 16, fb = kg * 64 * 3072 + cg * 128;
        WAITV(8); BAR();
#pragma unroll
        for (int e = 0; e < 8; ++e) glds(fo, (const char*)p.wg + fb + e * 8 * 3072, lds0 + L_FFNA + e * 1024);
        WAITV(8); BAR();
#pragma unroll
        for (int e = 0; e < 8; ++e) glds(fo, (const char*)p.wu + fb + e * 8 * 3072, lds0 + L_FFNA + 8192 + e * 1024);
        FST(23); BAR();
        u64 tg = 0;
#pragma unroll 1
        for (unsigned sp = 0; sp < SPIN_CAP; ++sp) { tg = ldg64(ws + WS_TOP + xc * 256 + (lane & 1) * 8); if (__all((unsigned)(tg >> 32) == tag)) break; }
        if (lane < 2) *(LAS unsigned*)(lds0 + L_TOP + lane * 4) = (unsigned)tg;
        u32x4 xc0 = {ONE2, ONE2, ONE2, ONE2}, xc1 = xc0;
        if ((lane & 15) < 2) { const char* xr = (const char*)p.x + ((2047u - (unsigned)tg) & 2047u) * 1280 + kg * 128 + g4 * 16; xc0 = *(const GAS u32x4*)xr; xc1 = *(const GAS u32x4*)(xr + 64); }
        bf16x8 af[8][2];
#pragma unroll
        for (int n = 0; n < 8; ++n) { const unsigned a = lds0 + L_FFNA + (n >> 2) * 8192 + (8 * g4 + q4) * 128 + (n & 3) * 32 + 8 * p4; af[n][0] = trfrag(a, a + 512); af[n][1] = trfrag(a + 4096, a + 4608); }
#pragma unroll
        for (int n = 0; n < 8; ++n) {
          const f32x4 y = MFMA16(af[n][1], __builtin_bit_cast(bf16x8, xc1), MFMA16(af[n][0], __builtin_bit_cast(bf16x8, xc0), (f32x4){}));
          if ((lane & 15) < 3) *(LAS f32x4*)(lds0 + L_TMP + (((n >> 2) * 3 + (lane & 15)) * 64 + 16 * (n & 3) + 4 * g4) * 4) = y;
        }
#pragma unroll
        for (int n = 0; n < 6; ++n) stg64(ws + WS_PARTA + (au * 384 + n * 64 + lane) * 8, gran(*(const LAS unsigned*)(lds0 + L_TMP + (n * 64 + lane) * 4), tag));
      }
    }
  } else {
    const int sp_ = 2 * xc + i;
    if (w < 4) {
      router_load(); router_dots();
#pragma unroll
      for (int k = 0; k < 14; ++k) router_step(k);
    }
#pragma unroll 1
    for (int e = 0; e < 15; ++e) {
      const unsigned L = 64 * (8 * e + w) + lane, r = L / 80, pc = L - r * 80;
      glds(r * 1280 + (pc ^ (((r & 3) | ((r >> 1) & 4)) << 1)) * 16, (const char*)p.wd + sp_ * 96 * 1280, lds0 + (8 * e + w) * 1024);
    }
    if (tid == 384) *(LAS u32x4*)(lds0 + L_CNT) = (u32x4){};
    BAR();
    if (w >= 4) lds_tag();
    ST(2);
    if (w == 7) {
      unsigned k1 = 0, k2 = 0;
      if (i == 0) {
        __builtin_amdgcn_s_sleep(16);
#pragma unroll 1
        for (unsigned sp = 0; sp < SPIN_CAP; ++sp) { if (cand_top2(ws, lane, tag, k1, k2)) break; __builtin_amdgcn_s_sleep(4); }
        if (lane < 2) stg64(ws + WS_TOP + xc * 256 + lane * 8, gran(lane ? k2 : k1, tag));
        if (b == 0 && lane == 0) __hip_atomic_store((GAS unsigned*)(ws + WS_GEN), tag, __ATOMIC_RELAXED, __HIP_MEMORY_SCOPE_AGENT);
      } else {
        __builtin_amdgcn_s_sleep(32);
#pragma unroll 1
        for (unsigned sp = 0; sp < SPIN_CAP; ++sp) {
          const u64 v = ldg64(ws + WS_TOP + xc * 256 + (lane & 1) * 8);
          k1 = __builtin_amdgcn_readlane((unsigned)v, 0); k2 = __builtin_amdgcn_readlane((unsigned)v, 1);
          if (__all((unsigned)(v >> 32) == tag)) break;
          __builtin_amdgcn_s_sleep(4);
        }
      }
      if (lane == 0) *(LAS u32x2*)(lds0 + L_TOP) = (u32x2){k1, k2};
    }
  }
  ST(4); FST(35);
  const u64 ftag = gran(tag, ~tag);

  const int tg3 = w & 3, kh = w >> 2, tok0 = 256 * xc + 64 * (i >> 3), n0 = 80 * cs80;
  const bool two = w == 4 || w == 5;
  const unsigned it0 = tid, it1 = two ? tid + 256 : tid, tk0 = (it0 * 6554) >> 16, tk1 = (it1 * 6554) >> 16, ch0 = it0 - 10 * tk0, ch1 = it1 - 10 * tk1;
  const unsigned eo0 = (tok0 + tk0) * 1280 + n0 * 2 + ch0 * 16, eo1 = (tok0 + tk1) * 1280 + n0 * 2 + ch1 * 16;
  u32x4 xv0, xv1; u64 sg0_, sg1_;
  auto pre3 = [&]() {
    xv0 = *(const GAS u32x4*)((const char*)p.x + eo0); sg0_ = ldg64(ws + WS_STATS + (tok0 + tk0) * 8);
    xv1 = *(const GAS u32x4*)((const char*)p.x + eo1); sg1_ = ldg64(ws + WS_STATS + (tok0 + tk1) * 8);
  };

  {
    const int R = i >> 2, hd = 4 * xc + (i & 3), p0 = (5 * hd) >> 1, oh = hd & 1;
    const int pf = p0 + oh, pp = oh ? p0 : p0 + 2, cc = (lane & 1 ? p0 + 2 : p0) >> 4;
    if (w >= 4) {
      __builtin_amdgcn_s_setprio(2);
      const int j = w - 4;
      const WoOff woo = wo_offsets(lane);
      if (WO1 && !spare) {
#pragma unroll 1
        for (unsigned sp = 0; sp < SPIN_CAP; ++sp) { if (__builtin_amdgcn_readfirstlane(*(volatile LAS unsigned*)(lds0 + L_CNT)) >= 4u) break; __builtin_amdgcn_s_sleep(1); }
        wo_steps(WO1 * j, WO1 * j + WO1, woo, p.wo, cs80, lds0);
      }
      const int kt[4] = {6 + j, j < 2 ? 4 + j : 8 + j, j, 12 + j};
      { const int e = lane >> 2, T = 8 * R - 8 + (e == 0 ? kt[0] : e == 1 ? kt[1] : e == 2 ? kt[2] : kt[3]);
        const bool act = lane < 16 && T >= 0;
        poll(ws + WS_FLAG1 + (act ? ((T >> 2) * 16 + (lane & 2 ? 10 : 5) + cc) * 256 : 0), act, ftag); }
      ST(5); FST(36);
      const unsigned so0 = (80 + pf + h) * 512 + l31 * 16, so1 = (h ? (160 + pf) * 512 : (80 + pp) * 512 + 8 * oh) + l31 * 16, so2 = (h ? (160 + pp) * 512 + 8 * oh : (161 + pf) * 512) + l31 * 16;
      struct T3 { u32x4 a, b, c; };
      auto fetch = [&](int t) {
        const int T = 8 * R - 8 + t; const unsigned m = T < 0 ? 0x40000000u : 0u, o = WS_QKV + max(T, 0) * 122880;
        return T3{__builtin_amdgcn_raw_buffer_load_b128(rs, so0 | m, o, 16), __builtin_amdgcn_raw_buffer_load_b128(rs, so1 | m, o, 16), __builtin_amdgcn_raw_buffer_load_b128(rs, so2 | m, o, 16)};
      };
      auto stage = [&](int t, T3 v) {
        const unsigned kd = lds0 + L_KV + (R == 0 && t < 8 ? 7 : t) * KVS + l31 * 16;
        if (h) v.c[2] = v.c[3] = ONE2;
        *(LAS u32x4*)(kd + h * 512) = v.a; *(LAS u32x4*)(kd + 1024 + h * 512) = v.b; *(LAS u32x4*)(kd + L_V + (1 + h) * VPS) = v.c;
      };
      const T3 t0 = fetch(kt[0]), t1 = fetch(kt[1]);
      FENCE();
      stage(kt[0], t0);
      FENCE();
      const T3 t2 = fetch(kt[2]), t3 = fetch(kt[3]);
      FENCE();
      BAR(); ST(6); FST(39);
      stage(kt[1], t1);
      BAR(); FST(40);
      stage(kt[2], t2); stage(kt[3], t3);
      BAR(); FST(41);
      pre3();
      if (spare) {
        const int sp_ = 2 * xc + i;
        const u32x2 kk = *(const LAS u32x2*)(lds0 + L_TOP);
        const char* sa0 = ws + WS_STATS + ((2047u - kk[0]) & 2047u) * 8; const char* sa1 = ws + WS_STATS + ((2047u - kk[1]) & 2047u) * 8;
        const char* ga[3]; float sum[3]; u64 sg0, sg1;
#pragma unroll
        for (int r = 0; r < 3; ++r) { const unsigned e = min(144 * j + 64 * r + lane, 575), rc = e / 96, F = 96 * sp_ + e - 96 * rc; ga[r] = ws + WS_PARTA + ((F >> 6) * 3840 + rc * 64 + (F & 63)) * 8; }
#pragma unroll 1
        for (unsigned sp = 0; sp < SPIN_CAP; ++sp) {
          sg0 = ldg64(sa0); sg1 = ldg64(sa1);
          bool ok = (unsigned)(sg0 >> 32) == tag && (unsigned)(sg1 >> 32) == tag;
#pragma unroll
          for (int r = 0; r < 3; ++r) { sum[r] = 0.f;
#pragma unroll
            for (int kg = 0; kg < 10; ++kg) { const u64 u = ldg64(ga[r] + kg * 3072); ok &= (unsigned)(u >> 32) == tag; sum[r] += __uint_as_float((unsigned)u); } }
          if (__all(ok)) break;
        }
#pragma unroll
        for (int r = 0; r < 3; ++r) if (64 * r + lane < 144) *(LAS float*)(lds0 + L_TMP + (144 * j + 64 * r + lane) * 4) = sum[r];
        lds_meet(lds0 + L_CNT + 8, 4, lane);
        const unsigned hb = lds0 + L_TMP + 2304 + j * 384;
#pragma unroll
        for (int r = 0; r < 3; ++r) {
          const int e = 64 * r + lane, tk = e >= 96; const unsigned a = lds0 + L_TMP + (e - 96 * tk) * 4, sg = (unsigned)(tk ? sg1 : sg0);
          const float m = blo(sg), rd = __builtin_amdgcn_rcpf(bhi(sg));
          const float g = rb((*(const LAS float*)(a + tk * 384) - m * *(const LAS float*)(a + 768)) * rd);
          const float u = rb((*(const LAS float*)(a + 1152 + tk * 384) - m * *(const LAS float*)(a + 1920)) * rd);
          *(LAS u16*)(hb + e * 2) = (u16)pk(rb(g * __builtin_amdgcn_rcpf(1.f + __expf(-g))) * u, 0.f);
        }
        const unsigned yb = lds0 + L_HB + j * 1280;
#pragma unroll 1
        for (int n2 = 0; n2 < 2; ++n2) {
          const unsigned da = lds0 + (8 * g4 + q4) * 1280 + (((2 * j + n2) ^ (q4 | ((g4 & 1) << 2))) * 32) + 8 * p4;
          f32x4 y[5] = {};
#pragma unroll
          for (int s = 0; s < 3; ++s) {
            const bf16x8 hf = lds128(hb + (lane & 1) * 192 + g4 * 16 + s * 64);
#pragma unroll
            for (int n = 0; n < 5; ++n) y[n] = MFMA16(trfrag(da + s * 40960 + n * 256, da + s * 40960 + n * 256 + 5120), hf, y[n]);
          }
          if ((lane & 15) < 2) {
#pragma unroll
            for (int n = 0; n < 5; ++n) *(LAS f32x4*)(yb + ((lane & 15) * 160 + 32 * n + 16 * n2 + 4 * g4) * 4) = y[n];
          }
        }
#pragma unroll
        for (int n = 0; n < 5; ++n) {
          const int e = 64 * n + lane, tk = e >= 160, r = e - 160 * tk;
          stg64(ws + WS_YPART + (sp_ * 1280 + tk * 640 + 128 * (r >> 5) + 32 * j + (r & 31)) * 8, gran(*(const LAS unsigned*)(yb + e * 4), tag));
        }
        lds_meet(lds0 + L_CNT + 8, 8, lane);
        ST(3);
      }
      if (spare) wo_steps(5 * j, 5 * j + 5, woo, p.wo, cs80, lds0); else wo_steps(4 * WO1 + (5 - WO1) * j, 4 * WO1 + (5 - WO1) * (j + 1), woo, p.wo, cs80, lds0);
      FST(42);
      WAITV(0);
      FST(43);
      __builtin_amdgcn_s_setprio(0);
    } else {
      const int lo = w < 2, tu = 2 * w + 1 - lo, tv = 2 * w + lo;
      poll(ws + WS_FLAG1 + ((2 * R + (w >> 1)) * 16 + cc) * 256, lane < 2, ftag);
      ST(5); FST(36);
      bf16x8 qf[2][2];
      { const unsigned a0 = (pf + h) * 512 + l31 * 16, a1 = pp * 512 + l31 * 16 + oh * 8, ou = WS_QKV + (8 * R + tu) * 122880, ov = WS_QKV + (8 * R + tv) * 122880;
        const u32x4 u0 = __builtin_amdgcn_raw_buffer_load_b128(rs, a0, ou, 16), v0 = __builtin_amdgcn_raw_buffer_load_b128(rs, a0, ov, 16);
        const u32x2 u1 = __builtin_amdgcn_raw_buffer_load_b64(rs, a1, ou, 16), v1 = __builtin_amdgcn_raw_buffer_load_b64(rs, a1, ov, 16);
        qf[0][0] = __builtin_bit_cast(bf16x8, u0); qf[0][1] = __builtin_bit_cast(bf16x8, (u32x4){h ? 0u : u1[0], h ? 0u : u1[1], 0u, 0u});
        qf[1][0] = __builtin_bit_cast(bf16x8, v0); qf[1][1] = __builtin_bit_cast(bf16x8, (u32x4){h ? 0u : v1[0], h ? 0u : v1[1], 0u, 0u}); }
      f32x16 cz = {}, ca, cb;
#pragma unroll
      for (int r = 0; r < 16; ++r) {
        const bool le = (r & 3) + 8 * (r >> 2) + 4 * h <= l31;
        const float d = le ? 0.f : -1e30f, f = le || R == 0 ? -1e30f : 0.f;
        ca[r] = lo ? d : f; cb[r] = lo ? f : d;
      }
      const unsigned ka = lds0 + L_KV + h * 512 + l31 * 16;
      const int pi = min(4 * (g4 & 1) + p4, 5);
      const unsigned va = lds0 + L_KV + L_V + (pi >> 1) * VPS + (pi & 1) * 8 + (4 * h + q4) * 16;
      struct KF { bf16x8 a, b; };
      auto kread = [&](int kt) {
        const unsigned a = ka + kt * KVS;
        return KF{lds128(a), lds128(a + 1024)};
      };
      auto vread = [&](int kt) { const unsigned v = va + kt * KVS; return KF{trfrag(v, v + 128), trfrag(v + 256, v + 384)}; };
      auto ex = [&](const f32x16& s, int e) { return pk(__builtin_amdgcn_exp2f(s[2 * e]), __builtin_amdgcn_exp2f(s[2 * e + 1])); };
      const int P0 = lo ? 6 : 8, P1 = w == 0 ? 4 : w == 1 ? 8 : w == 2 ? 6 : 10, P2 = lo ? 2 + 2 * w : 6 + 2 * w;
      const int tp[10] = {P0, P0 + 1, P1, P1 + 1, lo ? 2 * w + 8 : 2 * w + 1, lo ? 2 * w + 9 : 2 * w, P2, P2 + 1, lo ? 2 * w + 1 : 2 * w + 8, lo ? 2 * w : 2 * w + 9};
      auto at = [&](int ps) { const int e = (ps == 1 || ps == 3 || ps == 7), t = tp[ps - e]; return (R == 0 && t < 8 ? 8 : t) + e; };
      auto setz = [&](int ps) { if (R == 0) { const float z = tp[ps] < 8 ? -1e30f : 0.f;
#pragma unroll
          for (int r = 0; r < 16; ++r) cz[r] = z; } };
      setz(0);
      asm volatile("" : "+v"(ca), "+v"(cb), "+v"(cz), "+v"(qf[0][0]), "+v"(qf[0][1]), "+v"(qf[1][0]), "+v"(qf[1][1]));
      BAR(); ST(6); FST(39);
      KF K0, K1, K2, V0, V1, V2; f32x16 S0, S1, o[2] = {};
      K0 = kread(at(0)); V0 = vread(at(0));
      S0 = MFMA32(K0.b, qf[0][1], MFMA32(K0.a, qf[0][0], cz));
      u32x4 pa = {}, pb = {}; float x0, x1, x2, x3;
#define SET3(N, p) ((p) % 3 == 0 ? N##0 : (p) % 3 == 1 ? N##1 : N##2)
#define EXP4(s, e) do { x0 = __builtin_amdgcn_exp2f(s[4 * e]); x1 = __builtin_amdgcn_exp2f(s[4 * e + 1]); x2 = __builtin_amdgcn_exp2f(s[4 * e + 2]); x3 = __builtin_amdgcn_exp2f(s[4 * e + 3]); \
                        asm volatile("" : "+v"(x0), "+v"(x1), "+v"(x2), "+v"(x3)); FENCE(); } while (0)
#define PACK2(d, e) do { d[e] = pk(x0, x1); d[e + 1] = pk(x2, x3); asm volatile("" : "+v"(d)); FENCE(); } while (0)
      auto unit = [&](auto U, f32x16& sc, f32x16& sn) {
        constexpr int u = decltype(U)::value, np = u + 1 < 18 ? UP[u + 1] : 0, nq = u + 1 < 18 ? UQ[u + 1] : 0, pu = u > 0 ? u - 1 : 0;
        if constexpr (u == 2 || u == BC_AT) { BAR(); if constexpr (u == BC_AT) pre3(); }
        FST(40 + u);
        if constexpr (u + 1 < 18) { if constexpr (np != UP[u]) setz(np); sn = MFMA32(SET3(K, np).a, qf[nq][0], u + 1 == 8 || u + 1 == 10 ? ca : u + 1 >= 16 ? cb : cz); FENCE(); }
        if constexpr (u > 0) PACK2(pb, 2);
        EXP4(sc, 0);
        if constexpr (u > 0) { o[UQ[pu]] = MFMA32(SET3(V, UP[pu]).b, __builtin_bit_cast(bf16x8, pb), o[UQ[pu]]); FENCE(); }
        if constexpr (u + 2 < 18 && UP[u + 2] != UP[u + 1]) SET3(K, UP[u + 2]) = kread(at(UP[u + 2]));
        if constexpr (u + 1 < 18 && np != UP[u]) SET3(V, np) = vread(at(np));
        PACK2(pa, 0);
        EXP4(sc, 1);
        if constexpr (u + 1 < 18) { sn = MFMA32(SET3(K, np).b, qf[nq][1], sn); FENCE(); }
        PACK2(pa, 2);
        EXP4(sc, 2);
        o[UQ[u]] = MFMA32(SET3(V, UP[u]).a, __builtin_bit_cast(bf16x8, pa), o[UQ[u]]); FENCE();
        PACK2(pb, 0);
        EXP4(sc, 3);
      };
#define UNIT2(n) unit(IC<n>{}, S0, S1); unit(IC<n + 1>{}, S1, S0);
      UNIT2(0) UNIT2(2) UNIT2(4) UNIT2(6) UNIT2(8) UNIT2(10) UNIT2(12) UNIT2(14) UNIT2(16)
      PACK2(pb, 2);
      o[0] = MFMA32(V0.b, __builtin_bit_cast(bf16x8, pb), o[0]);
      auto rowsum = [&](const f32x16& a) { auto r = __builtin_amdgcn_permlane32_swap(__float_as_uint(a[8]), __float_as_uint(a[8]), false, false); return __uint_as_float(r[1]); };
      float l[2] = {rowsum(o[1]), rowsum(o[0])};
      if (FORCE_FB || !__all(l[0] > 0x1p-60f && l[0] < 0x1p60f && l[1] > 0x1p-60f && l[1] < 0x1p60f)) {
        int qt = tu;
#pragma unroll 1
        for (int e = 0; e < 2; ++e) {
          const int k0 = R > 0 ? qt : 8;
          auto score = [&](int kt, float base) {
            f32x16 c;
#pragma unroll
            for (int r = 0; r < 16; ++r) { const bool le = (r & 3) + 8 * (r >> 2) + 4 * h <= l31; c[r] = (kt == qt + 8 && !le) || (kt == qt && le) ? -1e30f : base; }
            const KF k = kread(kt);
            return MFMA32(k.b, qf[0][1], MFMA32(k.a, qf[0][0], c));
          };
          float m = -3e38f;
#pragma unroll 1
          for (int kt = k0; kt <= qt + 8; ++kt) { const f32x16 s = score(kt, 0.f);
#pragma unroll
            for (int r = 0; r < 16; ++r) m = fmaxf(m, s[r]); }
          { auto r = __builtin_amdgcn_permlane32_swap(__float_as_uint(m), __float_as_uint(m), false, false); m = fmaxf(__uint_as_float(r[0]), __uint_as_float(r[1])); }
          o[0] = (f32x16){};
#pragma unroll 1
          for (int kt = k0; kt <= qt + 8; ++kt) {
            const f32x16 s = score(kt, -m); const KF v = vread(kt);
            o[0] = MFMA32(v.a, __builtin_bit_cast(bf16x8, (u32x4){ex(s, 0), ex(s, 1), ex(s, 2), ex(s, 3)}), o[0]);
            o[0] = MFMA32(v.b, __builtin_bit_cast(bf16x8, (u32x4){ex(s, 4), ex(s, 5), ex(s, 6), ex(s, 7)}), o[0]);
          }
          const f32x16 to = o[0]; o[0] = o[1]; o[1] = to;
          const bf16x8 t0 = qf[0][0], t1 = qf[0][1]; qf[0][0] = qf[1][0]; qf[0][1] = qf[1][1]; qf[1][0] = t0; qf[1][1] = t1;
          qt = tv;
        }
        l[0] = rowsum(o[1]); l[1] = rowsum(o[0]);
      }
      ST(7); FST(59);
#pragma unroll
      for (int e = 0; e < 2; ++e) {
        const f32x16& a = o[1 - e]; const float ri = __builtin_amdgcn_rcpf(l[e]);
        const unsigned e0 = pk(a[0] * ri, a[1] * ri), e1 = pk(a[2] * ri, a[3] * ri), e2 = pk(a[4] * ri, a[5] * ri), e3 = pk(a[6] * ri, a[7] * ri);
        const u32x2 el = {pk(a[8] * ri, a[9] * ri), pk(a[10] * ri, a[11] * ri)};
        auto r0 = __builtin_amdgcn_permlane32_swap(e0, e2, false, false), r1 = __builtin_amdgcn_permlane32_swap(e1, e3, false, false);
        const unsigned ob = WS_O + (16 * R + 2 * (e ? tu : tv) + (l31 >> 4)) * 20480;
        __builtin_amdgcn_raw_buffer_store_b128((u32x4){r0[0], r1[0], r0[1], r1[1]}, rs, ob + (2 * hd + h) * 256 + (lane & 15) * 16, 0, 16);
        if (h == 0) __builtin_amdgcn_raw_buffer_store_b64(el, rs, ob + 16384 + hd * 128 + (lane & 15) * 8, 0, 16);
      }
      WAITV(0);
      if (lane == 0 && __hip_atomic_fetch_add((LAS unsigned*)(lds0 + L_CNT + 4), 1u, __ATOMIC_RELAXED, __HIP_MEMORY_SCOPE_WORKGROUP) == 3u)
        stg64(ws + WS_FLAG2 + (R * 32 + hd) * 256, ftag);
      ST(8); FST(60);
    }
    BAR();
  }

  {
    const int tg = tg3;
    const unsigned wa = lds0 + L_WO + (kh * 320 + 4 * g4 + q4) * 160 + 8 * p4;
    constexpr int NPRE = 6;
    bf16x8 wf[NPRE][5];
#pragma unroll
    for (int s = 0; s < NPRE; ++s)
#pragma unroll
      for (int n = 0; n < 5; ++n) wf[s][n] = trfrag(wa + s * 5120 + n * 32, wa + s * 5120 + 2560 + n * 32);
#pragma unroll 1
    for (unsigned sp = 0; sp < SPIN_CAP && !__all((unsigned)(sg0_ >> 32) == tag && (unsigned)(sg1_ >> 32) == tag); ++sp) {
      sg0_ = ldg64(ws + WS_STATS + (tok0 + tk0) * 8); sg1_ = ldg64(ws + WS_STATS + (tok0 + tk1) * 8); }
    f32x4 base[4];
#pragma unroll
    for (int e = 0; e < 4; ++e) {
      const unsigned sg = (unsigned)(e < 2 ? sg0_ : sg1_); const float m = blo(sg), rd = __builtin_amdgcn_rcpf(bhi(sg));
#pragma unroll
      for (int c = 0; c < 4; ++c) { const unsigned u = (e < 2 ? xv0 : xv1)[2 * (e & 1) + (c >> 1)]; const float v = c & 1 ? bhi(u) : blo(u); base[e][c] = v + (v - m) * rd; }
    }
    const u32x2 kk12 = *(const LAS u32x2*)(lds0 + L_TOP);
    const unsigned k1 = __builtin_amdgcn_readfirstlane(kk12[0]), k2 = __builtin_amdgcn_readfirstlane(kk12[1]);
    const unsigned i1 = 2047u - (k1 & 0xffffu) - tok0, i2 = 2047u - (k2 & 0xffffu) - tok0;
    if ((i1 < 64u || i2 < 64u) && w < 3) {
      const int c = tid >= 80, col = tid - 80 * c;
      const char* ya = ws + WS_YPART + (c * 640 + n0 + (tid < 160 ? col : 0)) * 8;
      float s = 0.f;
#pragma unroll 1
      for (unsigned sp = 0; sp < SPIN_CAP; ++sp) {
        bool ok = true; s = 0.f;
#pragma unroll 8
        for (int e = 0; e < 16; ++e) { u64 v = ldg64(ya + e * 10240); ok &= (unsigned)(v >> 32) == tag; s += __uint_as_float((unsigned)v); }
        if (__all(ok)) break;
      }
      const unsigned kk = (c ? k2 : k1) >> 16;
      const float r = blo(kk & 0x8000u ? kk & 0x7fffu : ~kk & 0xffffu);
      if (tid < 160) *(LAS float*)(lds0 + L_UPD + tid * 4) = rb(rb(s) * rb(__builtin_amdgcn_rcpf(1.f + __expf(-r))));
    }
#pragma unroll
    for (int s = 0; s < NPRE; ++s)
#pragma unroll
      for (int n = 0; n < 5; ++n) asm volatile("" : "+v"(wf[s][n]));
    asm volatile("" : "+v"(base[0]), "+v"(base[1]), "+v"(base[2]), "+v"(base[3]));
    poll(ws + WS_FLAG2 + (xc * 32 + 16 * kh + (lane & 15)) * 256, lane < 16, ftag);
    ST(9); FST(61);
    const unsigned ob = WS_O + (tok0 / 16 + tg) * 20480;
    u32x4 of[10];
    auto oload = [&](int s) {
      if (s < 8) of[s] = __builtin_amdgcn_raw_buffer_load_b128(rs, lane * 16 + s * 1024, ob + kh * 8192, 16);
      else {
        const unsigned a = (8 * (s - 8) + 2 * g4) * 128 + (lane & 15) * 8;
        const u32x2 u0 = __builtin_amdgcn_raw_buffer_load_b64(rs, a, ob + 16384 + kh * 2048, 16), u1 = __builtin_amdgcn_raw_buffer_load_b64(rs, a + 128, ob + 16384 + kh * 2048, 16);
        of[s] = (u32x4){u0[0], u0[1], u1[0], u1[1]};
      }
    };
#pragma unroll
    for (int s = 0; s < OPRE; ++s) oload(s);
    FENCE();
    FST(62);
    f32x4 acc[5] = {};
#pragma unroll
    for (int s = 0; s < 10; ++s) {
      if (s + OPRE < 10) oload(s + OPRE);
      FENCE();
#pragma unroll
      for (int n = 0; n < 5; ++n) acc[n] = MFMA16(wf[s % NPRE][n], __builtin_bit_cast(bf16x8, of[s]), acc[n]);
      if (s + NPRE < 10) {
#pragma unroll
        for (int n = 0; n < 5; ++n) wf[s][n] = trfrag(wa + (s + NPRE) * 5120 + n * 32, wa + (s + NPRE) * 5120 + 2560 + n * 32);
      }
      FENCE();
    }
    ST(10); FST(63);
#pragma unroll
    for (int n = 0; n < 5; ++n) *(LAS f32x4*)(lds0 + L_RED + kh * 21504 + (16 * tg + (lane & 15)) * 336 + (16 * n + 4 * g4) * 4) = acc[n];
    BAR();
    auto fin = [&](unsigned tk, unsigned ch, unsigned eo, f32x4 b0, f32x4 b1) {
      const unsigned a = lds0 + L_RED + tk * 336 + ch * 32;
      b0 += *(const LAS f32x4*)a + *(const LAS f32x4*)(a + 21504); b1 += *(const LAS f32x4*)(a + 16) + *(const LAS f32x4*)(a + 21504 + 16);
      if (tk == i1 || tk == i2) { const unsigned u = lds0 + L_UPD + (tk == i1 ? 0 : 320) + ch * 32; b0 += *(const LAS f32x4*)u; b1 += *(const LAS f32x4*)(u + 16); }
      *(GAS u32x4*)((char*)p.out + eo) = (u32x4){pk(b0[0], b0[1]), pk(b0[2], b0[3]), pk(b1[0], b1[1]), pk(b1[2], b1[3])};
    };
    fin(tk0, ch0, eo0, base[0], base[1]);
    if (two) fin(tk1, ch1, eo1, base[2], base[3]);
    ST(11);
  }
}

__global__ void k_spin(const unsigned long long* st) {
  __shared__ unsigned long long a[256], b[256];
  unsigned long long mn = ~0ull, mx = 0;
  for (int w = 0; w < 8; ++w) { unsigned long long s0 = st[((threadIdx.x * 8 + w) * 48 + 0) * 2], e = st[((threadIdx.x * 8 + w) * 48 + 0) * 2]; mn = s0 < mn ? s0 : mn; if (true) mx = e > mx ? e : mx; }
  a[threadIdx.x] = mn; b[threadIdx.x] = mx;
  __syncthreads();
  if (threadIdx.x == 0) {
    unsigned long long s = ~0ull, e = 0, sum = 0;
    for (int i = 0; i < 256; ++i) s = a[i] < s ? a[i] : s;
    for (int i = 16; i < 256; ++i) { e = b[i] > e ? b[i] : e; sum += b[i] - s; }
    if (0 == 11 || 0 == 0) for (int i = 0; i < 16; ++i) e = b[i] > e ? b[i] : e;
    unsigned long long span = 0 ? sum * 100 / 240 : (e - s) * 100; if (span > 3000000) span = 3000000;
    unsigned long long t0 = __builtin_amdgcn_s_memrealtime();
    while (__builtin_amdgcn_s_memrealtime() - t0 < span) __builtin_amdgcn_s_sleep(8);
  }
}
extern "C" void kernel_launch(void* const* d_in, const int* in_sizes, int n_in, void* d_out, int out_size, void* d_ws, size_t ws_size,
                              hipStream_t stream) {
  Params p;
  p.x = (const u16*)d_in[0]; p.wqkv = (const u16*)d_in[1]; p.wo = (const u16*)d_in[2]; p.wr = (const u16*)d_in[4];
  p.wg = (const u16*)d_in[5]; p.wu = (const u16*)d_in[6]; p.wd = (const u16*)d_in[7]; p.out = (u16*)d_out; p.ws = (char*)d_ws;
  fused_block<<<256, 512, 0, stream>>>(p);
  k_spin<<<1, 256, 0, stream>>>((const unsigned long long*)((char*)d_ws + WS_STAMP));
}
```

```cpp
#define STAMP
#include <hip/hip_runtime.h>
#include <stdint.h>

typedef unsigned short u16;
typedef unsigned long long u64;
typedef short bf16x8 __attribute__((ext_vector_type(8)));
typedef short s16x4 __attribute__((ext_vector_type(4)));
typedef float f32x2 __attribute__((ext_vector_type(2)));
typedef float f32x4 __attribute__((ext_vector_type(4)));
typedef float f32x16 __attribute__((ext_vector_type(16)));
typedef unsigned u32x2 __attribute__((ext_vector_type(2)));
typedef unsigned u32x4 __attribute__((ext_vector_type(4)));
typedef __bf16 bf2 __attribute__((ext_vector_type(2)));
#define LAS __attribute__((address_space(3)))
#define GAS __attribute__((address_space(1)))
#define DI __device__ __forceinline__

constexpr unsigned WS_GEN = 0, WS_FLAG1 = 4096, WS_FLAG2 = 131072, WS_CAND = 262144, WS_STATS = 278528, WS_TOP = 294912, WS_PARTA = 524288,
                   WS_YPART = 1572864, WS_QKV = 2097152, WS_O = 10485760, WS_END = 13631488, WS_STAMP = 16777216;
constexpr unsigned L_WO = 0, L_KV = 102400, L_FFNA = 65536;
constexpr unsigned KVS = 3264, L_V = 1536, VPS = 576;
constexpr unsigned L_RED = L_KV, L_UPD = L_KV + 43008;
constexpr unsigned L_KEY = 154624, L_TOP = L_KEY + 32, L_CNT = L_KEY + 48  , L_STAT = L_KEY + 64, L_CS = L_STAT + 1024, L_TMP = L_CS + 1024;
constexpr unsigned L_HB = 122880;
constexpr unsigned SPIN_CAP = 1u << 20;
constexpr unsigned ONE2 = 0x3f803f80u;
#define CODE_KB 30

constexpr int UP[20] = {0, 0, 1, 1, 2, 2, 3, 3, 4, 4, 5, 6, 6, 7, 7, 8, 8, 9, 9, 9}, UQ[18] = {0, 1, 0, 1, 0, 1, 0, 1, 0, 1, 1, 0, 1, 0, 1, 0, 1, 0};
template <int N> struct IC { static constexpr int value = N; };

struct Params { const u16 *x, *wqkv, *wo, *wr, *wg, *wu, *wd; u16* out; char* ws; };

#define ST(i) do { if (lane == 0) { GAS u64* sp_ = (GAS u64*)(p.ws + WS_STAMP) + ((blockIdx.x * 8 + w) * 48 + (i)) * 2; sp_[0] = __builtin_amdgcn_s_memrealtime(); sp_[1] = __builtin_amdgcn_s_memtime(); } } while (0)

#define FST(i) do {} while (0)
#define WAITV(n) asm volatile("s_waitcnt vmcnt(" #n ")" ::: "memory")
#define REQ0 3
#define LA 3
#define LAMODE 1
#define BC_AT 7
#define FORCE_FB 0
#define WO1 2
#define OPRE 4
#define PRIO 0
#define WAITN_(n) WAITV(n)
#define WAITN(n) WAITN_(n)
#define WO_PRE 6
#define BAR() do { __builtin_amdgcn_s_waitcnt(0xC07F); asm volatile("s_barrier" ::: "memory"); } while (0)
#define FENCE() __builtin_amdgcn_sched_barrier(0)
#define MFMA32(a, b, c) __builtin_amdgcn_mfma_f32_32x32x16_bf16((a), (b), (c), 0, 0, 0)
#define MFMA16(a, b, c) __builtin_amdgcn_mfma_f32_16x16x32_bf16((a), (b), (c), 0, 0, 0)

DI void glds(unsigned voff, const void* sbase, unsigned lds) {
  asm volatile("s_mov_b32 m0, %2\n\ts_nop 0\n\tglobal_load_lds_dwordx4 %0, %1" :: "v"(voff), "s"(sbase), "s"(lds) : "memory");
}
DI bf16x8 lds128(unsigned a) { return *(const LAS bf16x8*)a; }
DI s16x4 ldstr(unsigned a) { return __builtin_amdgcn_ds_read_tr16_b64_v4i16((LAS s16x4*)a); }
DI bf16x8 trfrag(unsigned a, unsigned a2) { s16x4 lo = ldstr(a), hi = ldstr(a2); return __builtin_shufflevector(lo, hi, 0, 1, 2, 3, 4, 5, 6, 7); }
DI unsigned pk(float a, float b) { f32x2 v = {a, b}; return __builtin_bit_cast(unsigned, __builtin_convertvector(v, bf2)); }
DI float rb(float a) { return __uint_as_float(pk(a, 0.f) << 16); }
DI float blo(unsigned v) { return __uint_as_float(v << 16); }
DI float bhi(unsigned v) { return __uint_as_float(v & 0xffff0000u); }
DI float dot2(unsigned a, unsigned b, float c) { return __builtin_amdgcn_fdot2_f32_bf16(__builtin_bit_cast(bf2, a), __builtin_bit_cast(bf2, b), c, false); }
DI float xhalf_sum(float v) { auto r = __builtin_amdgcn_permlane32_swap(__float_as_uint(v), __float_as_uint(v), false, false); return __uint_as_float(r[0]) + __uint_as_float(r[1]); }
DI u64 ldg64(const char* a) { return __hip_atomic_load((GAS u64*)a, __ATOMIC_RELAXED, __HIP_MEMORY_SCOPE_AGENT); }
DI void stg64(char* a, u64 v) { __hip_atomic_store((GAS u64*)a, v, __ATOMIC_RELAXED, __HIP_MEMORY_SCOPE_AGENT); }
DI u64 gran(unsigned v, unsigned tag) { return ((u64)tag << 32) | v; }
template <int CTRL> DI unsigned dpp(unsigned v) { return __builtin_amdgcn_update_dpp(0u, v, CTRL, 0xf, 0xf, true); }
DI unsigned wave_umax(unsigned v) {
  v = max(v, dpp<0x128>(v)); v = max(v, dpp<0x124>(v)); v = max(v, dpp<0x122>(v)); v = max(v, dpp<0x121>(v));
  auto r = __builtin_amdgcn_permlane16_swap(v, v, false, false); v = max(r[0], r[1]);
  auto q = __builtin_amdgcn_permlane32_swap(v, v, false, false); return max(q[0], q[1]);
}
DI float sum32(float v) {
  v += __uint_as_float(dpp<0x128>(__float_as_uint(v))); v += __uint_as_float(dpp<0x124>(__float_as_uint(v)));
  v += __uint_as_float(dpp<0x122>(__float_as_uint(v))); v += __uint_as_float(dpp<0x121>(__float_as_uint(v)));
  auto r = __builtin_amdgcn_permlane16_swap(__float_as_uint(v), __float_as_uint(v), false, false);
  return __uint_as_float(r[0]) + __uint_as_float(r[1]);
}
DI void ln_stats(float s1, float s2, float& mb, float& db) {
  float mean = s1 * (1.f / 640.f); float var = fmaxf(s2 * (1.f / 640.f) - mean * mean, 0.f);
  mb = rb(mean); db = rb(__builtin_amdgcn_sqrtf(rb(rb(var) + 1e-5f)));
}
DI void poll(const char* addr, bool act, u64 want) {
#pragma unroll 1
  for (unsigned sp = 0; sp < SPIN_CAP; ++sp) { u64 v = ldg64(addr); if (__all(!act || v == want)) break; __builtin_amdgcn_s_sleep(1); }
  asm volatile("" ::: "memory");
}
DI bool cand_top2(const char* ws, int lane, unsigned tag, unsigned& k1, unsigned& k2) {
  bool ok = true; unsigned a = 0, b = 0;
#pragma unroll 2
  for (int e0 = 0; e0 < 32; e0 += 16) {
    u64 g[16];
#pragma unroll
    for (int e = 0; e < 16; ++e) g[e] = ldg64(ws + WS_CAND + (e0 + e) * 512 + lane * 8);
#pragma unroll
    for (int e = 0; e < 16; ++e) { const unsigned v = (unsigned)g[e]; ok &= (unsigned)(g[e] >> 32) == tag; b = max(b, min(a, v)); a = max(a, v); }
  }
  if (!__all(ok)) return false;
  k1 = wave_umax(a); k2 = wave_umax(a == k1 ? b : a);
  k1 = __builtin_amdgcn_readfirstlane(k1); k2 = __builtin_amdgcn_readfirstlane(k2);
  return true;
}
struct WoOff { unsigned r[5], l[5]; };
DI WoOff wo_offsets(int lane) {
  WoOff o;
#pragma unroll
  for (int m = 0; m < 5; ++m) {
    const unsigned L = 64 * m + lane, rho = (L * 205) >> 11, c = L - rho * 10, q = rho & 3, g = (rho >> 2) & 3, t = rho >> 4;
    o.r[m] = (24 * (g >> 1) + 8 * (g & 1) + 4 * t + q) * 1280 + c * 16; o.l[m] = (40 * g + 16 + 4 * t + q) * 1280 + c * 16;
    asm volatile("" : "+v"(o.r[m]), "+v"(o.l[m]));
  }
  return o;
}
DI void wo_steps(int s0, int s1, const WoOff& o, const u16* wo, int cs80, unsigned lds0) {
#pragma unroll 1
  for (int u = s0; u < s1; ++u) {
    const int kh = u >= 10, st = u - 10 * kh;
    const char* sp = (const char*)wo + cs80 * 160 + (kh * 320 + (st < 8 ? st * 40 : (st - 8) * 160)) * 1280;
    const unsigned d = lds0 + L_WO + u * 5120;
    if (st < 8) {
#pragma unroll
      for (int m = 0; m < 5; ++m) glds(o.r[m], sp, d + m * 1024);
    } else {
#pragma unroll
      for (int m = 0; m < 5; ++m) glds(o.l[m], sp, d + m * 1024);
    }
  }
}
DI void lds_meet(unsigned addr, unsigned want, int lane) {
  __builtin_amdgcn_s_waitcnt(0xC07F);
  if (lane == 0) __hip_atomic_fetch_add((LAS unsigned*)addr, 1u, __ATOMIC_RELAXED, __HIP_MEMORY_SCOPE_WORKGROUP);
#pragma unroll 1
  for (unsigned sp = 0; sp < SPIN_CAP; ++sp) { if (__builtin_amdgcn_readfirstlane(*(volatile LAS unsigned*)addr) >= want) break; __builtin_amdgcn_s_sleep(1); }
  asm volatile("" ::: "memory");
}

__global__ __launch_bounds__(512, 2) void fused_block(Params p) {
  __shared__ __attribute__((aligned(1024))) char smem[163840];
  const unsigned lds0 = (unsigned)(size_t)smem;
  const int tid = threadIdx.x, lane = tid & 63, w = __builtin_amdgcn_readfirstlane(tid >> 6);
  const int b = blockIdx.x, xc = b & 7, i = b >> 3;
  const bool spare = i < 2;
  const int xp = xc >> 1, odd = xc & 1;
  const int cs80 = i & 7;
  char* const ws = p.ws;
  const auto rs = __builtin_amdgcn_make_buffer_rsrc(ws, 0, WS_END, 0x00020000);
  const int h = lane >> 5, l31 = lane & 31, g4 = lane >> 4, q4 = (lane & 15) >> 2, p4 = lane & 3;
  unsigned tag = 0;
  auto lds_tag = [&]() { tag = __builtin_amdgcn_readfirstlane(*(const LAS unsigned*)(lds0 + L_KEY)); };
  ST(0);
  const unsigned j30 = (unsigned)(i - 2) & 31u, j7 = (j30 * 37) >> 8;
  const unsigned trl = j30 < 28 ? j7 : (2 * odd + j30) & 3, ct = j30 < 28 ? j30 - j7 * 7 + odd * 8 : 7;
  unsigned sn = 3 * (trl + ct - 3 * (((trl + ct) * 43) >> 7)) + ((xc * 3) >> 3);
  const unsigned tr = 4 * xp + trl;
  const int rb8 = 64 * xp + 32 * odd + i;
  const char* const xt = (const char*)p.x + tr * (128 * 1280);
  const char* const wt = (const char*)p.wqkv + ct * 256;
  const unsigned x0 = (lane >> 3) * 1280 + (((lane & 7) ^ g4) * 16), x1 = (8 + (lane >> 3)) * 1280 + (((lane & 7) ^ (4 + g4)) * 16);
  const unsigned w0 = g4 * 3840 + (((lane & 15) ^ (g4 << 2)) * 16), w1 = w0 + 4 * 3840;
  auto pieces = [&](unsigned e, unsigned slot) {
    const char* xs = xt + (sn * 128 + e * (16 * 1280)); const char* wsb = wt + (sn * 64 + e * 8) * 3840; const unsigned d = lds0 + slot * 32768 + e * 2048;
    glds(x0, xs, d); glds(w0, wsb, d + 16384); glds(x1, xs, d + 1024); glds(w1, wsb, d + 16384 + 1024);
  };
  if (!spare) { pieces(w, 0); sn = sn == 9 ? 0 : sn + 1; }
  FST(28);

  const int rrow = 8 * rb8 + 2 * w + h;
  u32x4 rx[3], rw[3]; unsigned pfv = 0, genv = 0; float a1 = 0.f, a2 = 0.f, ar = 0.f, sw = 0.f;
  auto router_load = [&]() {
    if (CODE_KB && lane < CODE_KB / 2) asm volatile("global_load_dword %0, %1, %2" : "+v"(pfv) : "v"(((i & 3) * 4 + w) * (CODE_KB * 64) + lane * 128), "s"(__builtin_amdgcn_s_getpc()) : "memory");
    const char* xr = (const char*)p.x + rrow * 1280 + l31 * 16; const char* wr = (const char*)p.wr + l31 * 16;
    const int t3 = l31 < 16 ? 1024 : 0;
    rx[0] = *(const GAS u32x4*)xr; rx[1] = *(const GAS u32x4*)(xr + 512); rw[0] = *(const GAS u32x4*)wr; rw[1] = *(const GAS u32x4*)(wr + 512);
    rx[2] = *(const GAS u32x4*)(xr + t3); rw[2] = *(const GAS u32x4*)(wr + t3);
    genv = __hip_atomic_load((GAS unsigned*)(ws + WS_GEN), __ATOMIC_RELAXED, __HIP_MEMORY_SCOPE_AGENT);
    FENCE();
  };
  auto router_dots = [&]() {
    if (l31 >= 16) rx[2] = rw[2] = (u32x4){};
#pragma unroll
    for (int e = 0; e < 3; ++e)
#pragma unroll
      for (int c = 0; c < 4; ++c) { a1 = dot2(rx[e][c], ONE2, a1); a2 = dot2(rx[e][c], rx[e][c], a2); ar = dot2(rx[e][c], rw[e][c], ar); sw = dot2(rw[e][c], ONE2, sw); }
    tag = __builtin_amdgcn_readfirstlane(genv) + 1u;
    if (tid == 0) *(LAS unsigned*)(lds0 + L_KEY) = tag;
    asm volatile("" : "+v"(a1), "+v"(a2), "+v"(ar), "+v"(sw) : "v"(pfv));
  };
  float rmb = 0.f, rdb = 0.f; unsigned rk = 0, rv = 0;
  auto router_step = [&](int k) {
    auto fdpp = [&](float& v, int st) { const unsigned u = __float_as_uint(v); v += __uint_as_float(st == 0 ? dpp<0x128>(u) : st == 1 ? dpp<0x124>(u) : st == 2 ? dpp<0x122>(u) : dpp<0x121>(u)); };
    auto fswp = [&](float& v) { auto r = __builtin_amdgcn_permlane16_swap(__float_as_uint(v), __float_as_uint(v), false, false); v = __uint_as_float(r[0]) + __uint_as_float(r[1]); };
    if (k < 4) { fdpp(a1, k); fdpp(a2, k); fdpp(ar, k); fdpp(sw, k); }
    if (k == 4) { fswp(a1); fswp(a2); }
    if (k == 5) { fswp(ar); fswp(sw); }
    if (k == 6) { a1 *= 1.f / 640.f; a2 = fmaxf(a2 * (1.f / 640.f) - a1 * a1, 0.f); }
    if (k == 7) { rmb = rb(a1); a2 = rb(a2) + 1e-5f; }
    if (k == 8) { a2 = __builtin_amdgcn_sqrtf(rb(a2)); ar -= rmb * sw; }
    if (k == 9) { rdb = rb(a2); a2 = __builtin_amdgcn_rcpf(rdb); }
    if (k == 10) { rk = pk(ar * a2, 0.f) & 0xffffu; rv = (__float_as_uint(rmb) >> 16) | __float_as_uint(rdb); }
    if (k == 11) rk = (rk & 0x8000u ? (~rk & 0xffffu) : (rk | 0x8000u)) << 16;
    if (k == 12) rv = l31 ? rk | (2047u - rrow) : rv;
    if (k == 13) { if (l31 < 2) __builtin_amdgcn_raw_buffer_store_b64((u32x2){rv, tag}, rs, (l31 ? WS_CAND : WS_STATS) + rrow * 8, 0, 16); }
    asm volatile("" : "+v"(a1), "+v"(a2), "+v"(ar), "+v"(sw), "+v"(rmb), "+v"(rdb), "+v"(rk), "+v"(rv));
  };

  if (!spare) {
    if (w < 4) {
      router_load();
      const int fh = w >> 1, th = w & 1;
      unsigned xa[4], wa[2], ko[4];
#pragma unroll
      for (int e = 0; e < 4; ++e) { const int ks = (2 * th + e) & 3, row = 64 * th + l31; xa[e] = lds0 + row * 128 + (((2 * ks + h) ^ ((row >> 1) & 7)) * 16); ko[e] = ks * 4096; }
#pragma unroll
      for (int f = 0; f < 2; ++f) wa[f] = lds0 + 16384 + (8 * h + q4) * 256 + (((4 * (2 * fh + f) + 2 * (g4 & 1) + (p4 >> 1)) ^ (q4 << 2)) * 16) + 8 * (p4 & 1);
      f32x16 acc[2][2] = {};
      f32x4 cs0 = {}, cs1 = {};
      const unsigned selw = (lane & 15) == (g4 & 1) ? ONE2 : 0u;
      const bf16x8 sel = __builtin_bit_cast(bf16x8, (u32x4){selw, selw, selw, selw});
      struct Fr { bf16x8 v[4]; };
      Fr FA[4], FB[4];
      auto rd = [&](Fr* F, int n, unsigned so) {
        const int e = n >> 2, j = n & 3;
        if (j < 2) { const unsigned a = wa[j] + (so + ko[e]); F[e].v[j] = trfrag(a, a + 1024); } else F[e].v[j] = lds128(xa[e] + so + (j - 2) * 4096);
      };
      auto colsum = [&](const Fr* C, int e) { cs0 = MFMA16(C[e].v[0], sel, cs0); cs1 = MFMA16(C[e].v[1], sel, cs1); };
      auto body = [&](Fr* N, const Fr* C, unsigned so, int r0 = 99) {
#pragma unroll
        for (int n = 0; n < 16; ++n) {
          const int e = n >> 2, f = (n >> 1) & 1, t = n & 1;
          acc[f][t] = MFMA32(C[e].v[f], C[e].v[2 + t], acc[f][t]);
          if (n < 4) { rd(N, 2 * n, so); rd(N, 2 * n + 1, so); } else if (n < 12) rd(N, n + 4, so);
          if (n >= 4 && r0 + n - 4 < 14) router_step(r0 + n - 4);
          FENCE();
        }
        colsum(C, 0); colsum(C, 1);
        FENCE();
      };
      if (CODE_KB) WAITV(8); else WAITV(7);
      BAR(); ST(1);
#pragma unroll
      for (int n = 0; n < 16; ++n) rd(FA, n, 0);
      router_dots();
      BAR();
#pragma unroll
      for (int s = 1; s <= 7; s += 2) { FST(2 * s); body(FB, FA, (s & 3) * 32768, s == 1 ? 0 : 99); FST(2 * s + 1); BAR(); FST(2 * s + 2); body(FA, FB, ((s + 1) & 3) * 32768, s == 1 ? 12 : 99); FST(2 * s + 3); BAR(); }
      FST(18);
      body(FB, FA, 32768);
      colsum(FB, 0); colsum(FB, 1);
      if ((lane & 15) < 2) { const unsigned d = lds0 + L_CS + w * 256 + ((lane & 15) * 16 + 4 * g4) * 4; *(LAS f32x4*)d = cs0; *(LAS f32x4*)(d + 128) = cs1; }
      FST(23); BAR(); FST(19);
      const unsigned ca = lds0 + L_CS + fh * 512 + l31 * 4;
      const float c0 = *(const LAS float*)ca + *(const LAS float*)(ca + 256), c1 = *(const LAS float*)(ca + 128) + *(const LAS float*)(ca + 384);
      const f32x2 st0 = *(const LAS f32x2*)(lds0 + L_STAT + (64 * th + l31) * 8), st1 = *(const LAS f32x2*)(lds0 + L_STAT + (64 * th + 32 + l31) * 8);
      const float qs = ct < 5 ? 0.22360679774997896f * 1.4426950408889634f : 1.f;
      const unsigned qo = WS_QKV + (4 * tr + 2 * th) * 122880 + (16 * ct + 8 * fh + h) * 512 + l31 * 16;
      bf16x8 ax[2], bx[2]; float rr[2]; unsigned pw[8];
      auto split3 = [&](float c) {
        const float hi = rb(c), r1 = c - hi, mid = rb(r1);
        return __builtin_bit_cast(bf16x8, (u32x4){h ? 0u : pk(hi, mid), h ? 0u : pk(r1 - mid, 0.f), 0u, 0u});
      };
      auto epi = [&](int tile, int part) {
        const int f = tile & 1, t = tile >> 1;
#pragma unroll
        for (int g = 2 * part; g < 2 * part + 2; ++g) {
          float v0 = acc[f][t][2 * g] * rr[t]; asm("" : "+v"(v0));
          pw[g] = pk(v0, acc[f][t][2 * g + 1] * rr[t]);
        }
        if (part & 1) {
          const int g = part >> 1;
          auto r0 = __builtin_amdgcn_permlane32_swap(pw[4 * g], pw[4 * g + 2], false, false);
          auto r1 = __builtin_amdgcn_permlane32_swap(pw[4 * g + 1], pw[4 * g + 3], false, false);
          __builtin_amdgcn_raw_buffer_store_b128((u32x4){r0[0], r1[0], r0[1], r1[1]}, rs, qo + t * 122880 + (4 * f + 2 * g) * 512, 0, 16);
        }
      };
#pragma unroll
      for (int n = 0; n < 20; ++n) {
        const int tile = n < 8 ? n >> 2 : n < 10 ? n - 8 : n < 15 ? 2 : 3, e = n < 8 ? n & 3 : n < 10 ? 4 : (n - 10) % 5, f = tile & 1, t = tile >> 1;
        acc[f][t] = e < 4 ? MFMA32(FB[e].v[f], FB[e].v[2 + t], acc[f][t]) : MFMA32(ax[f], bx[t], acc[f][t]);
        if (n == 4) { ax[0] = split3(c0); const unsigned m = pk(-st0[0], -st0[0]); bx[0] = __builtin_bit_cast(bf16x8, (u32x4){m, m, 0u, 0u}); rr[0] = st0[1] * qs; }
        if (n == 5) { ax[1] = split3(c1); const unsigned m = pk(-st1[0], -st1[0]); bx[1] = __builtin_bit_cast(bf16x8, (u32x4){m, m, 0u, 0u}); rr[1] = st1[1] * qs; }
        if (n == 4 || n == 5) asm volatile("" : "+v"(ax[n - 4]), "+v"(bx[n - 4]), "+v"(rr[n - 4]));
        if (n >= 10) epi((n - 10) >> 2, (n - 10) & 3);
        FENCE();
        if (n == 9 || n == 13 || n == 17) FST(24 + (n - 9) / 4);
      }
      ST(2); FST(20);
      epi(2, 2); epi(2, 3);
#pragma unroll
      for (int part = 0; part < 4; ++part) epi(3, part);
      FST(21);
      WAITV(0);
      if (lane == 0 && __hip_atomic_fetch_add((LAS unsigned*)(lds0 + L_CNT), 1u, __ATOMIC_RELAXED, __HIP_MEMORY_SCOPE_WORKGROUP) == 3u)
        stg64(ws + WS_FLAG1 + (tr * 16 + ct) * 256, gran(tag, ~tag));
      ST(3); FST(22);
    } else {
      const int j = w - 4;
      auto issue = [&](int slot) { pieces(2 * j, slot); pieces(2 * j + 1, slot); sn = sn == 9 ? 0 : sn + 1; };
      issue(1); FST(29);
      if (tid == 384) *(LAS u32x4*)(lds0 + L_CNT) = (u32x4){};
      const unsigned au = tr * 15 + ct, cg = (au * 205) >> 11, kg = au - cg * 10;
      WAITV(8); FST(31); BAR();
#pragma unroll 1
      for (int s = 2; s < 2 + LAMODE; ++s) { issue(s & 3); WAITV(8); BAR(); }
      issue((2 + LAMODE) & 3);
#pragma unroll 1
      for (int s = 3 + LAMODE; s < 10; ++s) { FST(3 * s - 8); issue(s & 3); FST(3 * s - 7); WAITV(16); FST(3 * s - 6); BAR(); }
      lds_tag();
      if (w == 6) {
        const char* ga = ws + WS_STATS + tr * 1024;
        u32x2 g0, g1;
        asm volatile("global_load_dwordx2 %0, %2, %3 sc1\n\tglobal_load_dwordx2 %1, %2, %3 offset:512 sc1\n\t"
                     "s_waitcnt vmcnt(10)\n\ts_barrier\n\ts_waitcnt vmcnt(2)\n\ts_barrier\n\ts_waitcnt vmcnt(0)" : "=&v"(g0), "=&v"(g1) : "v"(lane * 8), "s"(ga) : "memory");
#pragma unroll 1
        for (unsigned sp = 0; sp < SPIN_CAP && !__all(g0[1] == tag && g1[1] == tag); ++sp) {
          g0 = __builtin_bit_cast(u32x2, ldg64(ga + lane * 8)); g1 = __builtin_bit_cast(u32x2, ldg64(ga + 512 + lane * 8)); }
        *(LAS f32x2*)(lds0 + L_STAT + lane * 8) = (f32x2){blo(g0[0]), __builtin_amdgcn_rcpf(bhi(g0[0]))};
        *(LAS f32x2*)(lds0 + L_STAT + 512 + lane * 8) = (f32x2){blo(g1[0]), __builtin_amdgcn_rcpf(bhi(g1[0]))};
        FST(23); BAR();
      } else if (w < 6) {
        unsigned pf = 0;
        const unsigned po = ((i >> 3) * 160 + j * 96 + (lane >> 1)) * 1280 + (lane & 1) * 128;
        const char* pb = (const char*)p.wo + ((cs80 * 160) & ~127);
        asm volatile("s_waitcnt vmcnt(8)\n\ts_barrier\n\tglobal_load_dword %0, %1, %2" : "+v"(pf) : "v"(po), "s"(pb) : "memory");
        asm volatile("global_load_dword %0, %1, %2" : "+v"(pf) : "v"(po + 32 * 1280), "s"(pb) : "memory");
        if (j == 0) asm volatile("global_load_dword %0, %1, %2\n\ts_waitcnt vmcnt(3)" : "+v"(pf) : "v"(po + 64 * 1280), "s"(pb) : "memory");
        else WAITV(2);
        BAR(); BAR();
        asm volatile("s_waitcnt vmcnt(0)" : "+v"(pf) :: "memory");
      }
      else {
        const unsigned fo = (lane >> 3) * 3072 + (lane & 7) * 16, fb = kg * 64 * 3072 + cg * 128;
        WAITV(8); BAR();
#pragma unroll
        for (int e = 0; e < 8; ++e) glds(fo, (const char*)p.wg + fb + e * 8 * 3072, lds0 + L_FFNA + e * 1024);
        WAITV(8); BAR();
#pragma unroll
        for (int e = 0; e < 8; ++e) glds(fo, (const char*)p.wu + fb + e * 8 * 3072, lds0 + L_FFNA + 8192 + e * 1024);
        FST(23); BAR();
        u64 tg = 0;
#pragma unroll 1
        for (unsigned sp = 0; sp < SPIN_CAP; ++sp) { tg = ldg64(ws + WS_TOP + xc * 256 + (lane & 1) * 8); if (__all((unsigned)(tg >> 32) == tag)) break; }
        if (lane < 2) *(LAS unsigned*)(lds0 + L_TOP + lane * 4) = (unsigned)tg;
        u32x4 xc0 = {ONE2, ONE2, ONE2, ONE2}, xc1 = xc0;
        if ((lane & 15) < 2) { const char* xr = (const char*)p.x + ((2047u - (unsigned)tg) & 2047u) * 1280 + kg * 128 + g4 * 16; xc0 = *(const GAS u32x4*)xr; xc1 = *(const GAS u32x4*)(xr + 64); }
        bf16x8 af[8][2];
#pragma unroll
        for (int n = 0; n < 8; ++n) { const unsigned a = lds0 + L_FFNA + (n >> 2) * 8192 + (8 * g4 + q4) * 128 + (n & 3) * 32 + 8 * p4; af[n][0] = trfrag(a, a + 512); af[n][1] = trfrag(a + 4096, a + 4608); }
#pragma unroll
        for (int n = 0; n < 8; ++n) {
          const f32x4 y = MFMA16(af[n][1], __builtin_bit_cast(bf16x8, xc1), MFMA16(af[n][0], __builtin_bit_cast(bf16x8, xc0), (f32x4){}));
          if ((lane & 15) < 3) *(LAS f32x4*)(lds0 + L_TMP + (((n >> 2) * 3 + (lane & 15)) * 64 + 16 * (n & 3) + 4 * g4) * 4) = y;
        }
#pragma unroll
        for (int n = 0; n < 6; ++n) stg64(ws + WS_PARTA + (au * 384 + n * 64 + lane) * 8, gran(*(const LAS unsigned*)(lds0 + L_TMP + (n * 64 + lane) * 4), tag));
      }
    }
  } else {
    const int sp_ = 2 * xc + i;
    if (w < 4) {
      router_load(); router_dots();
#pragma unroll
      for (int k = 0; k < 14; ++k) router_step(k);
    }
#pragma unroll 1
    for (int e = 0; e < 15; ++e) {
      const unsigned L = 64 * (8 * e + w) + lane, r = L / 80, pc = L - r * 80;
      glds(r * 1280 + (pc ^ (((r & 3) | ((r >> 1) & 4)) << 1)) * 16, (const char*)p.wd + sp_ * 96 * 1280, lds0 + (8 * e + w) * 1024);
    }
    if (tid == 384) *(LAS u32x4*)(lds0 + L_CNT) = (u32x4){};
    BAR();
    if (w >= 4) lds_tag();
    ST(2);
    if (w == 7) {
      unsigned k1 = 0, k2 = 0;
      if (i == 0) {
        __builtin_amdgcn_s_sleep(16);
#pragma unroll 1
        for (unsigned sp = 0; sp < SPIN_CAP; ++sp) { if (cand_top2(ws, lane, tag, k1, k2)) break; __builtin_amdgcn_s_sleep(4); }
        if (lane < 2) stg64(ws + WS_TOP + xc * 256 + lane * 8, gran(lane ? k2 : k1, tag));
        if (b == 0 && lane == 0) __hip_atomic_store((GAS unsigned*)(ws + WS_GEN), tag, __ATOMIC_RELAXED, __HIP_MEMORY_SCOPE_AGENT);
      } else {
        __builtin_amdgcn_s_sleep(32);
#pragma unroll 1
        for (unsigned sp = 0; sp < SPIN_CAP; ++sp) {
          const u64 v = ldg64(ws + WS_TOP + xc * 256 + (lane & 1) * 8);
          k1 = __builtin_amdgcn_readlane((unsigned)v, 0); k2 = __builtin_amdgcn_readlane((unsigned)v, 1);
          if (__all((unsigned)(v >> 32) == tag)) break;
          __builtin_amdgcn_s_sleep(4);
        }
      }
      if (lane == 0) *(LAS u32x2*)(lds0 + L_TOP) = (u32x2){k1, k2};
    }
  }
  ST(4); FST(35);
  const u64 ftag = gran(tag, ~tag);

  const int tg3 = w & 3, kh = w >> 2, tok0 = 256 * xc + 64 * (i >> 3), n0 = 80 * cs80;
  const bool two = w == 4 || w == 5;
  const unsigned it0 = tid, it1 = two ? tid + 256 : tid, tk0 = (it0 * 6554) >> 16, tk1 = (it1 * 6554) >> 16, ch0 = it0 - 10 * tk0, ch1 = it1 - 10 * tk1;
  const unsigned eo0 = (tok0 + tk0) * 1280 + n0 * 2 + ch0 * 16, eo1 = (tok0 + tk1) * 1280 + n0 * 2 + ch1 * 16;
  u32x4 xv0, xv1; u64 sg0_, sg1_;
  auto pre3 = [&]() {
    xv0 = *(const GAS u32x4*)((const char*)p.x + eo0); sg0_ = ldg64(ws + WS_STATS + (tok0 + tk0) * 8);
    xv1 = *(const GAS u32x4*)((const char*)p.x + eo1); sg1_ = ldg64(ws + WS_STATS + (tok0 + tk1) * 8);
  };

  {
    const int R = i >> 2, hd = 4 * xc + (i & 3), p0 = (5 * hd) >> 1, oh = hd & 1;
    const int pf = p0 + oh, pp = oh ? p0 : p0 + 2, cc = (lane & 1 ? p0 + 2 : p0) >> 4;
    if (w >= 4) {
      __builtin_amdgcn_s_setprio(2);
      const int j = w - 4;
      const WoOff woo = wo_offsets(lane);
      if (WO1 && !spare) {
#pragma unroll 1
        for (unsigned sp = 0; sp < SPIN_CAP; ++sp) { if (__builtin_amdgcn_readfirstlane(*(volatile LAS unsigned*)(lds0 + L_CNT)) >= 4u) break; __builtin_amdgcn_s_sleep(1); }
        wo_steps(WO1 * j, WO1 * j + WO1, woo, p.wo, cs80, lds0);
      }
      const int kt[4] = {6 + j, j < 2 ? 4 + j : 8 + j, j, 12 + j};
      { const int e = lane >> 2, T = 8 * R - 8 + (e == 0 ? kt[0] : e == 1 ? kt[1] : e == 2 ? kt[2] : kt[3]);
        const bool act = lane < 16 && T >= 0;
        poll(ws + WS_FLAG1 + (act ? ((T >> 2) * 16 + (lane & 2 ? 10 : 5) + cc) * 256 : 0), act, ftag); }
      ST(5); FST(36);
      const unsigned so0 = (80 + pf + h) * 512 + l31 * 16, so1 = (h ? (160 + pf) * 512 : (80 + pp) * 512 + 8 * oh) + l31 * 16, so2 = (h ? (160 + pp) * 512 + 8 * oh : (161 + pf) * 512) + l31 * 16;
      struct T3 { u32x4 a, b, c; };
      auto fetch = [&](int t) {
        const int T = 8 * R - 8 + t; const unsigned m = T < 0 ? 0x40000000u : 0u, o = WS_QKV + max(T, 0) * 122880;
        return T3{__builtin_amdgcn_raw_buffer_load_b128(rs, so0 | m, o, 16), __builtin_amdgcn_raw_buffer_load_b128(rs, so1 | m, o, 16), __builtin_amdgcn_raw_buffer_load_b128(rs, so2 | m, o, 16)};
      };
      auto stage = [&](int t, T3 v) {
        const unsigned kd = lds0 + L_KV + (R == 0 && t < 8 ? 7 : t) * KVS + l31 * 16;
        if (h) v.c[2] = v.c[3] = ONE2;
        *(LAS u32x4*)(kd + h * 512) = v.a; *(LAS u32x4*)(kd + 1024 + h * 512) = v.b; *(LAS u32x4*)(kd + L_V + (1 + h) * VPS) = v.c;
      };
      const T3 t0 = fetch(kt[0]), t1 = fetch(kt[1]);
      FENCE();
      stage(kt[0], t0);
      FENCE();
      const T3 t2 = fetch(kt[2]), t3 = fetch(kt[3]);
      FENCE();
      BAR(); ST(6); FST(39);
      stage(kt[1], t1);
      BAR(); FST(40);
      stage(kt[2], t2); stage(kt[3], t3);
      BAR(); FST(41);
      pre3();
      if (spare) {
        const int sp_ = 2 * xc + i;
        const u32x2 kk = *(const LAS u32x2*)(lds0 + L_TOP);
        const char* sa0 = ws + WS_STATS + ((2047u - kk[0]) & 2047u) * 8; const char* sa1 = ws + WS_STATS + ((2047u - kk[1]) & 2047u) * 8;
        const char* ga[3]; float sum[3]; u64 sg0, sg1;
#pragma unroll
        for (int r = 0; r < 3; ++r) { const unsigned e = min(144 * j + 64 * r + lane, 575), rc = e / 96, F = 96 * sp_ + e - 96 * rc; ga[r] = ws + WS_PARTA + ((F >> 6) * 3840 + rc * 64 + (F & 63)) * 8; }
#pragma unroll 1
        for (unsigned sp = 0; sp < SPIN_CAP; ++sp) {
          sg0 = ldg64(sa0); sg1 = ldg64(sa1);
          bool ok = (unsigned)(sg0 >> 32) == tag && (unsigned)(sg1 >> 32) == tag;
#pragma unroll
          for (int r = 0; r < 3; ++r) { sum[r] = 0.f;
#pragma unroll
            for (int kg = 0; kg < 10; ++kg) { const u64 u = ldg64(ga[r] + kg * 3072); ok &= (unsigned)(u >> 32) == tag; sum[r] += __uint_as_float((unsigned)u); } }
          if (__all(ok)) break;
        }
#pragma unroll
        for (int r = 0; r < 3; ++r) if (64 * r + lane < 144) *(LAS float*)(lds0 + L_TMP + (144 * j + 64 * r + lane) * 4) = sum[r];
        lds_meet(lds0 + L_CNT + 8, 4, lane);
        const unsigned hb = lds0 + L_TMP + 2304 + j * 384;
#pragma unroll
        for (int r = 0; r < 3; ++r) {
          const int e = 64 * r + lane, tk = e >= 96; const unsigned a = lds0 + L_TMP + (e - 96 * tk) * 4, sg = (unsigned)(tk ? sg1 : sg0);
          const float m = blo(sg), rd = __builtin_amdgcn_rcpf(bhi(sg));
          const float g = rb((*(const LAS float*)(a + tk * 384) - m * *(const LAS float*)(a + 768)) * rd);
          const float u = rb((*(const LAS float*)(a + 1152 + tk * 384) - m * *(const LAS float*)(a + 1920)) * rd);
          *(LAS u16*)(hb + e * 2) = (u16)pk(rb(g * __builtin_amdgcn_rcpf(1.f + __expf(-g))) * u, 0.f);
        }
        const unsigned yb = lds0 + L_HB + j * 1280;
#pragma unroll 1
        for (int n2 = 0; n2 < 2; ++n2) {
          const unsigned da = lds0 + (8 * g4 + q4) * 1280 + (((2 * j + n2) ^ (q4 | ((g4 & 1) << 2))) * 32) + 8 * p4;
          f32x4 y[5] = {};
#pragma unroll
          for (int s = 0; s < 3; ++s) {
            const bf16x8 hf = lds128(hb + (lane & 1) * 192 + g4 * 16 + s * 64);
#pragma unroll
            for (int n = 0; n < 5; ++n) y[n] = MFMA16(trfrag(da + s * 40960 + n * 256, da + s * 40960 + n * 256 + 5120), hf, y[n]);
          }
          if ((lane & 15) < 2) {
#pragma unroll
            for (int n = 0; n < 5; ++n) *(LAS f32x4*)(yb + ((lane & 15) * 160 + 32 * n + 16 * n2 + 4 * g4) * 4) = y[n];
          }
        }
#pragma unroll
        for (int n = 0; n < 5; ++n) {
          const int e = 64 * n + lane, tk = e >= 160, r = e - 160 * tk;
          stg64(ws + WS_YPART + (sp_ * 1280 + tk * 640 + 128 * (r >> 5) + 32 * j + (r & 31)) * 8, gran(*(const LAS unsigned*)(yb + e * 4), tag));
        }
        lds_meet(lds0 + L_CNT + 8, 8, lane);
        ST(3);
      }
      if (spare) wo_steps(5 * j, 5 * j + 5, woo, p.wo, cs80, lds0); else wo_steps(4 * WO1 + (5 - WO1) * j, 4 * WO1 + (5 - WO1) * (j + 1), woo, p.wo, cs80, lds0);
      FST(42);
      WAITV(0);
      FST(43);
      __builtin_amdgcn_s_setprio(0);
    } else {
      const int lo = w < 2, tu = 2 * w + 1 - lo, tv = 2 * w + lo;
      poll(ws + WS_FLAG1 + ((2 * R + (w >> 1)) * 16 + cc) * 256, lane < 2, ftag);
      ST(5); FST(36);
      bf16x8 qf[2][2];
      { const unsigned a0 = (pf + h) * 512 + l31 * 16, a1 = pp * 512 + l31 * 16 + oh * 8, ou = WS_QKV + (8 * R + tu) * 122880, ov = WS_QKV + (8 * R + tv) * 122880;
        const u32x4 u0 = __builtin_amdgcn_raw_buffer_load_b128(rs, a0, ou, 16), v0 = __builtin_amdgcn_raw_buffer_load_b128(rs, a0, ov, 16);
        const u32x2 u1 = __builtin_amdgcn_raw_buffer_load_b64(rs, a1, ou, 16), v1 = __builtin_amdgcn_raw_buffer_load_b64(rs, a1, ov, 16);
        qf[0][0] = __builtin_bit_cast(bf16x8, u0); qf[0][1] = __builtin_bit_cast(bf16x8, (u32x4){h ? 0u : u1[0], h ? 0u : u1[1], 0u, 0u});
        qf[1][0] = __builtin_bit_cast(bf16x8, v0); qf[1][1] = __builtin_bit_cast(bf16x8, (u32x4){h ? 0u : v1[0], h ? 0u : v1[1], 0u, 0u}); }
      f32x16 cz = {}, ca, cb;
#pragma unroll
      for (int r = 0; r < 16; ++r) {
        const bool le = (r & 3) + 8 * (r >> 2) + 4 * h <= l31;
        const float d = le ? 0.f : -1e30f, f = le || R == 0 ? -1e30f : 0.f;
        ca[r] = lo ? d : f; cb[r] = lo ? f : d;
      }
      const unsigned ka = lds0 + L_KV + h * 512 + l31 * 16;
      const int pi = min(4 * (g4 & 1) + p4, 5);
      const unsigned va = lds0 + L_KV + L_V + (pi >> 1) * VPS + (pi & 1) * 8 + (4 * h + q4) * 16;
      struct KF { bf16x8 a, b; };
      auto kread = [&](int kt) {
        const unsigned a = ka + kt * KVS;
        return KF{lds128(a), lds128(a + 1024)};
      };
      auto vread = [&](int kt) { const unsigned v = va + kt * KVS; return KF{trfrag(v, v + 128), trfrag(v + 256, v + 384)}; };
      auto ex = [&](const f32x16& s, int e) { return pk(__builtin_amdgcn_exp2f(s[2 * e]), __builtin_amdgcn_exp2f(s[2 * e + 1])); };
      const int P0 = lo ? 6 : 8, P1 = w == 0 ? 4 : w == 1 ? 8 : w == 2 ? 6 : 10, P2 = lo ? 2 + 2 * w : 6 + 2 * w;
      const int tp[10] = {P0, P0 + 1, P1, P1 + 1, lo ? 2 * w + 8 : 2 * w + 1, lo ? 2 * w + 9 : 2 * w, P2, P2 + 1, lo ? 2 * w + 1 : 2 * w + 8, lo ? 2 * w : 2 * w + 9};
      auto at = [&](int ps) { const int e = (ps == 1 || ps == 3 || ps == 7), t = tp[ps - e]; return (R == 0 && t < 8 ? 8 : t) + e; };
      auto setz = [&](int ps) { if (R == 0) { const float z = tp[ps] < 8 ? -1e30f : 0.f;
#pragma unroll
          for (int r = 0; r < 16; ++r) cz[r] = z; } };
      setz(0);
      asm volatile("" : "+v"(ca), "+v"(cb), "+v"(cz), "+v"(qf[0][0]), "+v"(qf[0][1]), "+v"(qf[1][0]), "+v"(qf[1][1]));
      BAR(); ST(6); FST(39);
      KF K0, K1, K2, V0, V1, V2; f32x16 S0, S1, o[2] = {};
      K0 = kread(at(0)); V0 = vread(at(0));
      S0 = MFMA32(K0.b, qf[0][1], MFMA32(K0.a, qf[0][0], cz));
      u32x4 pa = {}, pb = {}; float x0, x1, x2, x3;
#define SET3(N, p) ((p) % 3 == 0 ? N##0 : (p) % 3 == 1 ? N##1 : N##2)
#define EXP4(s, e) do { x0 = __builtin_amdgcn_exp2f(s[4 * e]); x1 = __builtin_amdgcn_exp2f(s[4 * e + 1]); x2 = __builtin_amdgcn_exp2f(s[4 * e + 2]); x3 = __builtin_amdgcn_exp2f(s[4 * e + 3]); \
                        asm volatile("" : "+v"(x0), "+v"(x1), "+v"(x2), "+v"(x3)); FENCE(); } while (0)
#define PACK2(d, e) do { d[e] = pk(x0, x1); d[e + 1] = pk(x2, x3); asm volatile("" : "+v"(d)); FENCE(); } while (0)
      auto unit = [&](auto U, f32x16& sc, f32x16& sn) {
        constexpr int u = decltype(U)::value, np = u + 1 < 18 ? UP[u + 1] : 0, nq = u + 1 < 18 ? UQ[u + 1] : 0, pu = u > 0 ? u - 1 : 0;
        if constexpr (u == 2 || u == BC_AT) { BAR(); if constexpr (u == BC_AT) pre3(); }
        FST(40 + u);
        if constexpr (u + 1 < 18) { if constexpr (np != UP[u]) setz(np); sn = MFMA32(SET3(K, np).a, qf[nq][0], u + 1 == 8 || u + 1 == 10 ? ca : u + 1 >= 16 ? cb : cz); FENCE(); }
        if constexpr (u > 0) PACK2(pb, 2);
        EXP4(sc, 0);
        if constexpr (u > 0) { o[UQ[pu]] = MFMA32(SET3(V, UP[pu]).b, __builtin_bit_cast(bf16x8, pb), o[UQ[pu]]); FENCE(); }
        if constexpr (u + 2 < 18 && UP[u + 2] != UP[u + 1]) SET3(K, UP[u + 2]) = kread(at(UP[u + 2]));
        if constexpr (u + 1 < 18 && np != UP[u]) SET3(V, np) = vread(at(np));
        PACK2(pa, 0);
        EXP4(sc, 1);
        if constexpr (u + 1 < 18) { sn = MFMA32(SET3(K, np).b, qf[nq][1], sn); FENCE(); }
        PACK2(pa, 2);
        EXP4(sc, 2);
        o[UQ[u]] = MFMA32(SET3(V, UP[u]).a, __builtin_bit_cast(bf16x8, pa), o[UQ[u]]); FENCE();
        PACK2(pb, 0);
        EXP4(sc, 3);
      };
#define UNIT2(n) unit(IC<n>{}, S0, S1); unit(IC<n + 1>{}, S1, S0);
      UNIT2(0) UNIT2(2) UNIT2(4) UNIT2(6) UNIT2(8) UNIT2(10) UNIT2(12) UNIT2(14) UNIT2(16)
      PACK2(pb, 2);
      o[0] = MFMA32(V0.b, __builtin_bit_cast(bf16x8, pb), o[0]);
      auto rowsum = [&](const f32x16& a) { auto r = __builtin_amdgcn_permlane32_swap(__float_as_uint(a[8]), __float_as_uint(a[8]), false, false); return __uint_as_float(r[1]); };
      float l[2] = {rowsum(o[1]), rowsum(o[0])};
      if (FORCE_FB || !__all(l[0] > 0x1p-60f && l[0] < 0x1p60f && l[1] > 0x1p-60f && l[1] < 0x1p60f)) {
        int qt = tu;
#pragma unroll 1
        for (int e = 0; e < 2; ++e) {
          const int k0 = R > 0 ? qt : 8;
          auto score = [&](int kt, float base) {
            f32x16 c;
#pragma unroll
            for (int r = 0; r < 16; ++r) { const bool le = (r & 3) + 8 * (r >> 2) + 4 * h <= l31; c[r] = (kt == qt + 8 && !le) || (kt == qt && le) ? -1e30f : base; }
            const KF k = kread(kt);
            return MFMA32(k.b, qf[0][1], MFMA32(k.a, qf[0][0], c));
          };
          float m = -3e38f;
#pragma unroll 1
          for (int kt = k0; kt <= qt + 8; ++kt) { const f32x16 s = score(kt, 0.f);
#pragma unroll
            for (int r = 0; r < 16; ++r) m = fmaxf(m, s[r]); }
          { auto r = __builtin_amdgcn_permlane32_swap(__float_as_uint(m), __float_as_uint(m), false, false); m = fmaxf(__uint_as_float(r[0]), __uint_as_float(r[1])); }
          o[0] = (f32x16){};
#pragma unroll 1
          for (int kt = k0; kt <= qt + 8; ++kt) {
            const f32x16 s = score(kt, -m); const KF v = vread(kt);
            o[0] = MFMA32(v.a, __builtin_bit_cast(bf16x8, (u32x4){ex(s, 0), ex(s, 1), ex(s, 2), ex(s, 3)}), o[0]);
            o[0] = MFMA32(v.b, __builtin_bit_cast(bf16x8, (u32x4){ex(s, 4), ex(s, 5), ex(s, 6), ex(s, 7)}), o[0]);
          }
          const f32x16 to = o[0]; o[0] = o[1]; o[1] = to;
          const bf16x8 t0 = qf[0][0], t1 = qf[0][1]; qf[0][0] = qf[1][0]; qf[0][1] = qf[1][1]; qf[1][0] = t0; qf[1][1] = t1;
          qt = tv;
        }
        l[0] = rowsum(o[1]); l[1] = rowsum(o[0]);
      }
      ST(7); FST(59);
#pragma unroll
      for (int e = 0; e < 2; ++e) {
        const f32x16& a = o[1 - e]; const float ri = __builtin_amdgcn_rcpf(l[e]);
        const unsigned e0 = pk(a[0] * ri, a[1] * ri), e1 = pk(a[2] * ri, a[3] * ri), e2 = pk(a[4] * ri, a[5] * ri), e3 = pk(a[6] * ri, a[7] * ri);
        const u32x2 el = {pk(a[8] * ri, a[9] * ri), pk(a[10] * ri, a[11] * ri)};
        auto r0 = __builtin_amdgcn_permlane32_swap(e0, e2, false, false), r1 = __builtin_amdgcn_permlane32_swap(e1, e3, false, false);
        const unsigned ob = WS_O + (16 * R + 2 * (e ? tu : tv) + (l31 >> 4)) * 20480;
        __builtin_amdgcn_raw_buffer_store_b128((u32x4){r0[0], r1[0], r0[1], r1[1]}, rs, ob + (2 * hd + h) * 256 + (lane & 15) * 16, 0, 16);
        if (h == 0) __builtin_amdgcn_raw_buffer_store_b64(el, rs, ob + 16384 + hd * 128 + (lane & 15) * 8, 0, 16);
      }
      WAITV(0);
      if (lane == 0 && __hip_atomic_fetch_add((LAS unsigned*)(lds0 + L_CNT + 4), 1u, __ATOMIC_RELAXED, __HIP_MEMORY_SCOPE_WORKGROUP) == 3u)
        stg64(ws + WS_FLAG2 + (R * 32 + hd) * 256, ftag);
      ST(8); FST(60);
    }
    BAR();
  }

  {
    const int tg = tg3;
    const unsigned wa = lds0 + L_WO + (kh * 320 + 4 * g4 + q4) * 160 + 8 * p4;
    constexpr int NPRE = 6;
    bf16x8 wf[NPRE][5];
#pragma unroll
    for (int s = 0; s < NPRE; ++s)
#pragma unroll
      for (int n = 0; n < 5; ++n) wf[s][n] = trfrag(wa + s * 5120 + n * 32, wa + s * 5120 + 2560 + n * 32);
#pragma unroll 1
    for (unsigned sp = 0; sp < SPIN_CAP && !__all((unsigned)(sg0_ >> 32) == tag && (unsigned)(sg1_ >> 32) == tag); ++sp) {
      sg0_ = ldg64(ws + WS_STATS + (tok0 + tk0) * 8); sg1_ = ldg64(ws + WS_STATS + (tok0 + tk1) * 8); }
    f32x4 base[4];
#pragma unroll
    for (int e = 0; e < 4; ++e) {
      const unsigned sg = (unsigned)(e < 2 ? sg0_ : sg1_); const float m = blo(sg), rd = __builtin_amdgcn_rcpf(bhi(sg));
#pragma unroll
      for (int c = 0; c < 4; ++c) { const unsigned u = (e < 2 ? xv0 : xv1)[2 * (e & 1) + (c >> 1)]; const float v = c & 1 ? bhi(u) : blo(u); base[e][c] = v + (v - m) * rd; }
    }
    const u32x2 kk12 = *(const LAS u32x2*)(lds0 + L_TOP);
    const unsigned k1 = __builtin_amdgcn_readfirstlane(kk12[0]), k2 = __builtin_amdgcn_readfirstlane(kk12[1]);
    const unsigned i1 = 2047u - (k1 & 0xffffu) - tok0, i2 = 2047u - (k2 & 0xffffu) - tok0;
    if ((i1 < 64u || i2 < 64u) && w < 3) {
      const int c = tid >= 80, col = tid - 80 * c;
      const char* ya = ws + WS_YPART + (c * 640 + n0 + (tid < 160 ? col : 0)) * 8;
      float s = 0.f;
#pragma unroll 1
      for (unsigned sp = 0; sp < SPIN_CAP; ++sp) {
        bool ok = true; s = 0.f;
#pragma unroll 8
        for (int e = 0; e < 16; ++e) { u64 v = ldg64(ya + e * 10240); ok &= (unsigned)(v >> 32) == tag; s += __uint_as_float((unsigned)v); }
        if (__all(ok)) break;
      }
      const unsigned kk = (c ? k2 : k1) >> 16;
      const float r = blo(kk & 0x8000u ? kk & 0x7fffu : ~kk & 0xffffu);
      if (tid < 160) *(LAS float*)(lds0 + L_UPD + tid * 4) = rb(rb(s) * rb(__builtin_amdgcn_rcpf(1.f + __expf(-r))));
    }
#pragma unroll
    for (int s = 0; s < NPRE; ++s)
#pragma unroll
      for (int n = 0; n < 5; ++n) asm volatile("" : "+v"(wf[s][n]));
    asm volatile("" : "+v"(base[0]), "+v"(base[1]), "+v"(base[2]), "+v"(base[3]));
    poll(ws + WS_FLAG2 + (xc * 32 + 16 * kh + (lane & 15)) * 256, lane < 16, ftag);
    ST(9); FST(61);
    const unsigned ob = WS_O + (tok0 / 16 + tg) * 20480;
    u32x4 of[10];
    auto oload = [&](int s) {
      if (s < 8) of[s] = __builtin_amdgcn_raw_buffer_load_b128(rs, lane * 16 + s * 1024, ob + kh * 8192, 16);
      else {
        const unsigned a = (8 * (s - 8) + 2 * g4) * 128 + (lane & 15) * 8;
        const u32x2 u0 = __builtin_amdgcn_raw_buffer_load_b64(rs, a, ob + 16384 + kh * 2048, 16), u1 = __builtin_amdgcn_raw_buffer_load_b64(rs, a + 128, ob + 16384 + kh * 2048, 16);
        of[s] = (u32x4){u0[0], u0[1], u1[0], u1[1]};
      }
    };
#pragma unroll
    for (int s = 0; s < OPRE; ++s) oload(s);
    FENCE();
    FST(62);
    f32x4 acc[5] = {};
#pragma unroll
    for (int s = 0; s < 10; ++s) {
      if (s + OPRE < 10) oload(s + OPRE);
      FENCE();
#pragma unroll
      for (int n = 0; n < 5; ++n) acc[n] = MFMA16(wf[s % NPRE][n], __builtin_bit_cast(bf16x8, of[s]), acc[n]);
      if (s + NPRE < 10) {
#pragma unroll
        for (int n = 0; n < 5; ++n) wf[s][n] = trfrag(wa + (s + NPRE) * 5120 + n * 32, wa + (s + NPRE) * 5120 + 2560 + n * 32);
      }
      FENCE();
    }
    ST(10); FST(63);
#pragma unroll
    for (int n = 0; n < 5; ++n) *(LAS f32x4*)(lds0 + L_RED + kh * 21504 + (16 * tg + (lane & 15)) * 336 + (16 * n + 4 * g4) * 4) = acc[n];
    BAR();
    auto fin = [&](unsigned tk, unsigned ch, unsigned eo, f32x4 b0, f32x4 b1) {
      const unsigned a = lds0 + L_RED + tk * 336 + ch * 32;
      b0 += *(const LAS f32x4*)a + *(const LAS f32x4*)(a + 21504); b1 += *(const LAS f32x4*)(a + 16) + *(const LAS f32x4*)(a + 21504 + 16);
      if (tk == i1 || tk == i2) { const unsigned u = lds0 + L_UPD + (tk == i1 ? 0 : 320) + ch * 32; b0 += *(const LAS f32x4*)u; b1 += *(const LAS f32x4*)(u + 16); }
      *(GAS u32x4*)((char*)p.out + eo) = (u32x4){pk(b0[0], b0[1]), pk(b0[2], b0[3]), pk(b1[0], b1[1]), pk(b1[2], b1[3])};
    };
    fin(tk0, ch0, eo0, base[0], base[1]);
    if (two) fin(tk1, ch1, eo1, base[2], base[3]);
    ST(11);
  }
}

__global__ void k_first(unsigned long long* t) { if (threadIdx.x == 0) t[0] = __builtin_amdgcn_s_memrealtime(); }
__global__ void k_spin(const unsigned long long* st, const unsigned long long* tf) {
  unsigned long long tstart = __builtin_amdgcn_s_memrealtime();
  __shared__ unsigned long long a[256], b[256];
  unsigned long long mn = ~0ull, mx = 0;
  for (int w = 0; w < 8; ++w) { unsigned long long s0 = st[((threadIdx.x * 8 + w) * 48 + 0) * 2], e = st[((threadIdx.x * 8 + w) * 48 + 11) * 2]; mn = s0 < mn ? s0 : mn; mx = e > mx ? e : mx; }
  a[threadIdx.x] = mn; b[threadIdx.x] = mx;
  __syncthreads();
  if (threadIdx.x == 0) {
    unsigned long long s = ~0ull, e = 0;
    for (int i = 0; i < 256; ++i) { s = a[i] < s ? a[i] : s; e = b[i] > e ? b[i] : e; }
    unsigned long long span = (2 == 0 ? tstart - e : 2 == 1 ? s - tf[0] : 0) * 100; if (span > 3000000) span = 3000000;
    unsigned long long t0 = __builtin_amdgcn_s_memrealtime();
    while (__builtin_amdgcn_s_memrealtime() - t0 < span) __builtin_amdgcn_s_sleep(8);
  }
}
extern "C" void kernel_launch(void* const* d_in, const int* in_sizes, int n_in, void* d_out, int out_size, void* d_ws, size_t ws_size,
                              hipStream_t stream) {
  Params p;
  p.x = (const u16*)d_in[0]; p.wqkv = (const u16*)d_in[1]; p.wo = (const u16*)d_in[2]; p.wr = (const u16*)d_in[4];
  p.wg = (const u16*)d_in[5]; p.wu = (const u16*)d_in[6]; p.wd = (const u16*)d_in[7]; p.out = (u16*)d_out; p.ws = (char*)d_ws;
  k_first<<<1, 64, 0, stream>>>((unsigned long long*)((char*)d_ws + (60 << 20)));
  fused_block<<<256, 512, 0, stream>>>(p);
  k_spin<<<1, 256, 0, stream>>>((const unsigned long long*)((char*)d_ws + WS_STAMP), (const unsigned long long*)((char*)d_ws + (60 << 20)));
}
```

```cpp
#define STAMP
#include <hip/hip_runtime.h>
#include <stdint.h>

typedef unsigned short u16;
typedef unsigned long long u64;
typedef short bf16x8 __attribute__((ext_vector_type(8)));
typedef short s16x4 __attribute__((ext_vector_type(4)));
typedef float f32x2 __attribute__((ext_vector_type(2)));
typedef float f32x4 __attribute__((ext_vector_type(4)));
typedef float f32x16 __attribute__((ext_vector_type(16)));
typedef unsigned u32x2 __attribute__((ext_vector_type(2)));
typedef unsigned u32x4 __attribute__((ext_vector_type(4)));
typedef __bf16 bf2 __attribute__((ext_vector_type(2)));
#define LAS __attribute__((address_space(3)))
#define GAS __attribute__((address_space(1)))
#define DI __device__ __forceinline__

constexpr unsigned WS_GEN = 0, WS_FLAG1 = 4096, WS_FLAG2 = 131072, WS_CAND = 262144, WS_STATS = 278528, WS_TOP = 294912, WS_PARTA = 524288,
                   WS_YPART = 1572864, WS_QKV = 2097152, WS_O = 10485760, WS_END = 13631488, WS_STAMP = 16777216;
constexpr unsigned L_WO = 0, L_KV = 102400, L_FFNA = 65536;
constexpr unsigned KVS = 3264, L_V = 1536, VPS = 576;
constexpr unsigned L_RED = L_KV, L_UPD = L_KV + 43008;
constexpr unsigned L_KEY = 154624, L_TOP = L_KEY + 32, L_CNT = L_KEY + 48  , L_STAT = L_KEY + 64, L_CS = L_STAT + 1024, L_TMP = L_CS + 1024;
constexpr unsigned L_HB = 122880;
constexpr unsigned SPIN_CAP = 1u << 20;
constexpr unsigned ONE2 = 0x3f803f80u;
#define CODE_KB 30

constexpr int UP[20] = {0, 0, 1, 1, 2, 2, 3, 3, 4, 4, 5, 6, 6, 7, 7, 8, 8, 9, 9, 9}, UQ[18] = {0, 1, 0, 1, 0, 1, 0, 1, 0, 1, 1, 0, 1, 0, 1, 0, 1, 0};
template <int N> struct IC { static constexpr int value = N; };

struct Params { const u16 *x, *wqkv, *wo, *wr, *wg, *wu, *wd; u16* out; char* ws; };

#define ST(i) do { if (lane == 0) { GAS u64* sp_ = (GAS u64*)(p.ws + WS_STAMP) + ((blockIdx.x * 8 + w) * 48 + (i)) * 2; sp_[0] = __builtin_amdgcn_s_memrealtime(); sp_[1] = __builtin_amdgcn_s_memtime(); } } while (0)

#define FST(i) do {} while (0)
#define WAITV(n) asm volatile("s_waitcnt vmcnt(" #n ")" ::: "memory")
#define REQ0 3
#define LA 3
#define LAMODE 1
#define BC_AT 7
#define FORCE_FB 0
#define WO1 2
#define OPRE 4
#define PRIO 0
#define WAITN_(n) WAITV(n)
#define WAITN(n) WAITN_(n)
#define WO_PRE 6
#define BAR() do { __builtin_amdgcn_s_waitcnt(0xC07F); asm volatile("s_barrier" ::: "memory"); } while (0)
#define FENCE() __builtin_amdgcn_sched_barrier(0)
#define MFMA32(a, b, c) __builtin_amdgcn_mfma_f32_32x32x16_bf16((a), (b), (c), 0, 0, 0)
#define MFMA16(a, b, c) __builtin_amdgcn_mfma_f32_16x16x32_bf16((a), (b), (c), 0, 0, 0)

DI void glds(unsigned voff, const void* sbase, unsigned lds) {
  asm volatile("s_mov_b32 m0, %2\n\ts_nop 0\n\tglobal_load_lds_dwordx4 %0, %1" :: "v"(voff), "s"(sbase), "s"(lds) : "memory");
}
DI bf16x8 lds128(unsigned a) { return *(const LAS bf16x8*)a; }
DI s16x4 ldstr(unsigned a) { return __builtin_amdgcn_ds_read_tr16_b64_v4i16((LAS s16x4*)a); }
DI bf16x8 trfrag(unsigned a, unsigned a2) { s16x4 lo = ldstr(a), hi = ldstr(a2); return __builtin_shufflevector(lo, hi, 0, 1, 2, 3, 4, 5, 6, 7); }
DI unsigned pk(float a, float b) { f32x2 v = {a, b}; return __builtin_bit_cast(unsigned, __builtin_convertvector(v, bf2)); }
DI float rb(float a) { return __uint_as_float(pk(a, 0.f) << 16); }
DI float blo(unsigned v) { return __uint_as_float(v << 16); }
DI float bhi(unsigned v) { return __uint_as_float(v & 0xffff0000u); }
DI float dot2(unsigned a, unsigned b, float c) { return __builtin_amdgcn_fdot2_f32_bf16(__builtin_bit_cast(bf2, a), __builtin_bit_cast(bf2, b), c, false); }
DI float xhalf_sum(float v) { auto r = __builtin_amdgcn_permlane32_swap(__float_as_uint(v), __float_as_uint(v), false, false); return __uint_as_float(r[0]) + __uint_as_float(r[1]); }
DI u64 ldg64(const char* a) { return __hip_atomic_load((GAS u64*)a, __ATOMIC_RELAXED, __HIP_MEMORY_SCOPE_AGENT); }
DI void stg64(char* a, u64 v) { __hip_atomic_store((GAS u64*)a, v, __ATOMIC_RELAXED, __HIP_MEMORY_SCOPE_AGENT); }
DI u64 gran(unsigned v, unsigned tag) { return ((u64)tag << 32) | v; }
template <int CTRL> DI unsigned dpp(unsigned v) { return __builtin_amdgcn_update_dpp(0u, v, CTRL, 0xf, 0xf, true); }
DI unsigned wave_umax(unsigned v) {
  v = max(v, dpp<0x128>(v)); v = max(v, dpp<0x124>(v)); v = max(v, dpp<0x122>(v)); v = max(v, dpp<0x121>(v));
  auto r = __builtin_amdgcn_permlane16_swap(v, v, false, false); v = max(r[0], r[1]);
  auto q = __builtin_amdgcn_permlane32_swap(v, v, false, false); return max(q[0], q[1]);
}
DI float sum32(float v) {
  v += __uint_as_float(dpp<0x128>(__float_as_uint(v))); v += __uint_as_float(dpp<0x124>(__float_as_uint(v)));
  v += __uint_as_float(dpp<0x122>(__float_as_uint(v))); v += __uint_as_float(dpp<0x121>(__float_as_uint(v)));
  auto r = __builtin_amdgcn_permlane16_swap(__float_as_uint(v), __float_as_uint(v), false, false);
  return __uint_as_float(r[0]) + __uint_as_float(r[1]);
}
DI void ln_stats(float s1, float s2, float& mb, float& db) {
  float mean = s1 * (1.f / 640.f); float var = fmaxf(s2 * (1.f / 640.f) - mean * mean, 0.f);
  mb = rb(mean); db = rb(__builtin_amdgcn_sqrtf(rb(rb(var) + 1e-5f)));
}
DI void poll(const char* addr, bool act, u64 want) {
#pragma unroll 1
  for (unsigned sp = 0; sp < SPIN_CAP; ++sp) { u64 v = ldg64(addr); if (__all(!act || v == want)) break; __builtin_amdgcn_s_sleep(1); }
  asm volatile("" ::: "memory");
}
DI bool cand_top2(const char* ws, int lane, unsigned tag, unsigned& k1, unsigned& k2) {
  bool ok = true; unsigned a = 0, b = 0;
#pragma unroll 2
  for (int e0 = 0; e0 < 32; e0 += 16) {
    u64 g[16];
#pragma unroll
    for (int e = 0; e < 16; ++e) g[e] = ldg64(ws + WS_CAND + (e0 + e) * 512 + lane * 8);
#pragma unroll
    for (int e = 0; e < 16; ++e) { const unsigned v = (unsigned)g[e]; ok &= (unsigned)(g[e] >> 32) == tag; b = max(b, min(a, v)); a = max(a, v); }
  }
  if (!__all(ok)) return false;
  k1 = wave_umax(a); k2 = wave_umax(a == k1 ? b : a);
  k1 = __builtin_amdgcn_readfirstlane(k1); k2 = __builtin_amdgcn_readfirstlane(k2);
  return true;
}
struct WoOff { unsigned r[5], l[5]; };
DI WoOff wo_offsets(int lane) {
  WoOff o;
#pragma unroll
  for (int m = 0; m < 5; ++m) {
    const unsigned L = 64 * m + lane, rho = (L * 205) >> 11, c = L - rho * 10, q = rho & 3, g = (rho >> 2) & 3, t = rho >> 4;
    o.r[m] = (24 * (g >> 1) + 8 * (g & 1) + 4 * t + q) * 1280 + c * 16; o.l[m] = (40 * g + 16 + 4 * t + q) * 1280 + c * 16;
    asm volatile("" : "+v"(o.r[m]), "+v"(o.l[m]));
  }
  return o;
}
DI void wo_steps(int s0, int s1, const WoOff& o, const u16* wo, int cs80, unsigned lds0) {
#pragma unroll 1
  for (int u = s0; u < s1; ++u) {
    const int kh = u >= 10, st = u - 10 * kh;
    const char* sp = (const char*)wo + cs80 * 160 + (kh * 320 + (st < 8 ? st * 40 : (st - 8) * 160)) * 1280;
    const unsigned d = lds0 + L_WO + u * 5120;
    if (st < 8) {
#pragma unroll
      for (int m = 0; m < 5; ++m) glds(o.r[m], sp, d + m * 1024);
    } else {
#pragma unroll
      for (int m = 0; m < 5; ++m) glds(o.l[m], sp, d + m * 1024);
    }
  }
}
DI void lds_meet(unsigned addr, unsigned want, int lane) {
  __builtin_amdgcn_s_waitcnt(0xC07F);
  if (lane == 0) __hip_atomic_fetch_add((LAS unsigned*)addr, 1u, __ATOMIC_RELAXED, __HIP_MEMORY_SCOPE_WORKGROUP);
#pragma unroll 1
  for (unsigned sp = 0; sp < SPIN_CAP; ++sp) { if (__builtin_amdgcn_readfirstlane(*(volatile LAS unsigned*)addr) >= want) break; __builtin_amdgcn_s_sleep(1); }
  asm volatile("" ::: "memory");
}

__global__ __launch_bounds__(512, 2) void fused_block(Params p) {
  unsigned long long t_entry; asm volatile("s_memrealtime %0" : "=s"(t_entry));
  __shared__ __attribute__((aligned(1024))) char smem[163840];
  const unsigned lds0 = (unsigned)(size_t)smem;
  const int tid = threadIdx.x, lane = tid & 63, w = __builtin_amdgcn_readfirstlane(tid >> 6);
  const int b = blockIdx.x, xc = b & 7, i = b >> 3;
  const bool spare = i < 2;
  const int xp = xc >> 1, odd = xc & 1;
  const int cs80 = i & 7;
  char* const ws = p.ws;
  const auto rs = __builtin_amdgcn_make_buffer_rsrc(ws, 0, WS_END, 0x00020000);
  const int h = lane >> 5, l31 = lane & 31, g4 = lane >> 4, q4 = (lane & 15) >> 2, p4 = lane & 3;
  unsigned tag = 0;
  auto lds_tag = [&]() { tag = __builtin_amdgcn_readfirstlane(*(const LAS unsigned*)(lds0 + L_KEY)); };
  asm volatile("s_waitcnt lgkmcnt(0)" : "+s"(t_entry) : "s"(p.ws), "s"(p.x));
  ST(0);
  if (lane == 0) ((GAS u64*)(p.ws + WS_STAMP))[((blockIdx.x * 8 + w) * 48 + 12) * 2] = t_entry;
  const unsigned j30 = (unsigned)(i - 2) & 31u, j7 = (j30 * 37) >> 8;
  const unsigned trl = j30 < 28 ? j7 : (2 * odd + j30) & 3, ct = j30 < 28 ? j30 - j7 * 7 + odd * 8 : 7;
  unsigned sn = 3 * (trl + ct - 3 * (((trl + ct) * 43) >> 7)) + ((xc * 3) >> 3);
  const unsigned tr = 4 * xp + trl;
  const int rb8 = 64 * xp + 32 * odd + i;
  const char* const xt = (const char*)p.x + tr * (128 * 1280);
  const char* const wt = (const char*)p.wqkv + ct * 256;
  const unsigned x0 = (lane >> 3) * 1280 + (((lane & 7) ^ g4) * 16), x1 = (8 + (lane >> 3)) * 1280 + (((lane & 7) ^ (4 + g4)) * 16);
  const unsigned w0 = g4 * 3840 + (((lane & 15) ^ (g4 << 2)) * 16), w1 = w0 + 4 * 3840;
  auto pieces = [&](unsigned e, unsigned slot) {
    const char* xs = xt + (sn * 128 + e * (16 * 1280)); const char* wsb = wt + (sn * 64 + e * 8) * 3840; const unsigned d = lds0 + slot * 32768 + e * 2048;
    glds(x0, xs, d); glds(w0, wsb, d + 16384); glds(x1, xs, d + 1024); glds(w1, wsb, d + 16384 + 1024);
  };
  if (!spare) { pieces(w, 0); sn = sn == 9 ? 0 : sn + 1; }
  FST(28);

  const int rrow = 8 * rb8 + 2 * w + h;
  u32x4 rx[3], rw[3]; unsigned pfv = 0, genv = 0; float a1 = 0.f, a2 = 0.f, ar = 0.f, sw = 0.f;
  auto router_load = [&]() {
    if (CODE_KB && lane < CODE_KB / 2) asm volatile("global_load_dword %0, %1, %2" : "+v"(pfv) : "v"(((i & 3) * 4 + w) * (CODE_KB * 64) + lane * 128), "s"(__builtin_amdgcn_s_getpc()) : "memory");
    const char* xr = (const char*)p.x + rrow * 1280 + l31 * 16; const char* wr = (const char*)p.wr + l31 * 16;
    const int t3 = l31 < 16 ? 1024 : 0;
    rx[0] = *(const GAS u32x4*)xr; rx[1] = *(const GAS u32x4*)(xr + 512); rw[0] = *(const GAS u32x4*)wr; rw[1] = *(const GAS u32x4*)(wr + 512);
    rx[2] = *(const GAS u32x4*)(xr + t3); rw[2] = *(const GAS u32x4*)(wr + t3);
    genv = __hip_atomic_load((GAS unsigned*)(ws + WS_GEN), __ATOMIC_RELAXED, __HIP_MEMORY_SCOPE_AGENT);
    FENCE();
  };
  auto router_dots = [&]() {
    if (l31 >= 16) rx[2] = rw[2] = (u32x4){};
#pragma unroll
    for (int e = 0; e < 3; ++e)
#pragma unroll
      for (int c = 0; c < 4; ++c) { a1 = dot2(rx[e][c], ONE2, a1); a2 = dot2(rx[e][c], rx[e][c], a2); ar = dot2(rx[e][c], rw[e][c], ar); sw = dot2(rw[e][c], ONE2, sw); }
    tag = __builtin_amdgcn_readfirstlane(genv) + 1u;
    if (tid == 0) *(LAS unsigned*)(lds0 + L_KEY) = tag;
    asm volatile("" : "+v"(a1), "+v"(a2), "+v"(ar), "+v"(sw) : "v"(pfv));
  };
  float rmb = 0.f, rdb = 0.f; unsigned rk = 0, rv = 0;
  auto router_step = [&](int k) {
    auto fdpp = [&](float& v, int st) { const unsigned u = __float_as_uint(v); v += __uint_as_float(st == 0 ? dpp<0x128>(u) : st == 1 ? dpp<0x124>(u) : st == 2 ? dpp<0x122>(u) : dpp<0x121>(u)); };
    auto fswp = [&](float& v) { auto r = __builtin_amdgcn_permlane16_swap(__float_as_uint(v), __float_as_uint(v), false, false); v = __uint_as_float(r[0]) + __uint_as_float(r[1]); };
    if (k < 4) { fdpp(a1, k); fdpp(a2, k); fdpp(ar, k); fdpp(sw, k); }
    if (k == 4) { fswp(a1); fswp(a2); }
    if (k == 5) { fswp(ar); fswp(sw); }
    if (k == 6) { a1 *= 1.f / 640.f; a2 = fmaxf(a2 * (1.f / 640.f) - a1 * a1, 0.f); }
    if (k == 7) { rmb = rb(a1); a2 = rb(a2) + 1e-5f; }
    if (k == 8) { a2 = __builtin_amdgcn_sqrtf(rb(a2)); ar -= rmb * sw; }
    if (k == 9) { rdb = rb(a2); a2 = __builtin_amdgcn_rcpf(rdb); }
    if (k == 10) { rk = pk(ar * a2, 0.f) & 0xffffu; rv = (__float_as_uint(rmb) >> 16) | __float_as_uint(rdb); }
    if (k == 11) rk = (rk & 0x8000u ? (~rk & 0xffffu) : (rk | 0x8000u)) << 16;
    if (k == 12) rv = l31 ? rk | (2047u - rrow) : rv;
    if (k == 13) { if (l31 < 2) __builtin_amdgcn_raw_buffer_store_b64((u32x2){rv, tag}, rs, (l31 ? WS_CAND : WS_STATS) + rrow * 8, 0, 16); }
    asm volatile("" : "+v"(a1), "+v"(a2), "+v"(ar), "+v"(sw), "+v"(rmb), "+v"(rdb), "+v"(rk), "+v"(rv));
  };

  if (!spare) {
    if (w < 4) {
      router_load();
      const int fh = w >> 1, th = w & 1;
      unsigned xa[4], wa[2], ko[4];
#pragma unroll
      for (int e = 0; e < 4; ++e) { const int ks = (2 * th + e) & 3, row = 64 * th + l31; xa[e] = lds0 + row * 128 + (((2 * ks + h) ^ ((row >> 1) & 7)) * 16); ko[e] = ks * 4096; }
#pragma unroll
      for (int f = 0; f < 2; ++f) wa[f] = lds0 + 16384 + (8 * h + q4) * 256 + (((4 * (2 * fh + f) + 2 * (g4 & 1) + (p4 >> 1)) ^ (q4 << 2)) * 16) + 8 * (p4 & 1);
      f32x16 acc[2][2] = {};
      f32x4 cs0 = {}, cs1 = {};
      const unsigned selw = (lane & 15) == (g4 & 1) ? ONE2 : 0u;
      const bf16x8 sel = __builtin_bit_cast(bf16x8, (u32x4){selw, selw, selw, selw});
      struct Fr { bf16x8 v[4]; };
      Fr FA[4], FB[4];
      auto rd = [&](Fr* F, int n, unsigned so) {
        const int e = n >> 2, j = n & 3;
        if (j < 2) { const unsigned a = wa[j] + (so + ko[e]); F[e].v[j] = trfrag(a, a + 1024); } else F[e].v[j] = lds128(xa[e] + so + (j - 2) * 4096);
      };
      auto colsum = [&](const Fr* C, int e) { cs0 = MFMA16(C[e].v[0], sel, cs0); cs1 = MFMA16(C[e].v[1], sel, cs1); };
      auto body = [&](Fr* N, const Fr* C, unsigned so, int r0 = 99) {
#pragma unroll
        for (int n = 0; n < 16; ++n) {
          const int e = n >> 2, f = (n >> 1) & 1, t = n & 1;
          acc[f][t] = MFMA32(C[e].v[f], C[e].v[2 + t], acc[f][t]);
          if (n < 4) { rd(N, 2 * n, so); rd(N, 2 * n + 1, so); } else if (n < 12) rd(N, n + 4, so);
          if (n >= 4 && r0 + n - 4 < 14) router_step(r0 + n - 4);
          FENCE();
        }
        colsum(C, 0); colsum(C, 1);
        FENCE();
      };
      if (CODE_KB) WAITV(8); else WAITV(7);
      BAR(); ST(1);
#pragma unroll
      for (int n = 0; n < 16; ++n) rd(FA, n, 0);
      router_dots();
      BAR();
#pragma unroll
      for (int s = 1; s <= 7; s += 2) { FST(2 * s); body(FB, FA, (s & 3) * 32768, s == 1 ? 0 : 99); FST(2 * s + 1); BAR(); FST(2 * s + 2); body(FA, FB, ((s + 1) & 3) * 32768, s == 1 ? 12 : 99); FST(2 * s + 3); BAR(); }
      FST(18);
      body(FB, FA, 32768);
      colsum(FB, 0); colsum(FB, 1);
      if ((lane & 15) < 2) { const unsigned d = lds0 + L_CS + w * 256 + ((lane & 15) * 16 + 4 * g4) * 4; *(LAS f32x4*)d = cs0; *(LAS f32x4*)(d + 128) = cs1; }
      FST(23); BAR(); FST(19);
      const unsigned ca = lds0 + L_CS + fh * 512 + l31 * 4;
      const float c0 = *(const LAS float*)ca + *(const LAS float*)(ca + 256), c1 = *(const LAS float*)(ca + 128) + *(const LAS float*)(ca + 384);
      const f32x2 st0 = *(const LAS f32x2*)(lds0 + L_STAT + (64 * th + l31) * 8), st1 = *(const LAS f32x2*)(lds0 + L_STAT + (64 * th + 32 + l31) * 8);
      const float qs = ct < 5 ? 0.22360679774997896f * 1.4426950408889634f : 1.f;
      const unsigned qo = WS_QKV + (4 * tr + 2 * th) * 122880 + (16 * ct + 8 * fh + h) * 512 + l31 * 16;
      bf16x8 ax[2], bx[2]; float rr[2]; unsigned pw[8];
      auto split3 = [&](float c) {
        const float hi = rb(c), r1 = c - hi, mid = rb(r1);
        return __builtin_bit_cast(bf16x8, (u32x4){h ? 0u : pk(hi, mid), h ? 0u : pk(r1 - mid, 0.f), 0u, 0u});
      };
      auto epi = [&](int tile, int part) {
        const int f = tile & 1, t = tile >> 1;
#pragma unroll
        for (int g = 2 * part; g < 2 * part + 2; ++g) {
          float v0 = acc[f][t][2 * g] * rr[t]; asm("" : "+v"(v0));
          pw[g] = pk(v0, acc[f][t][2 * g + 1] * rr[t]);
        }
        if (part & 1) {
          const int g = part >> 1;
          auto r0 = __builtin_amdgcn_permlane32_swap(pw[4 * g], pw[4 * g + 2], false, false);
          auto r1 = __builtin_amdgcn_permlane32_swap(pw[4 * g + 1], pw[4 * g + 3], false, false);
          __builtin_amdgcn_raw_buffer_store_b128((u32x4){r0[0], r1[0], r0[1], r1[1]}, rs, qo + t * 122880 + (4 * f + 2 * g) * 512, 0, 16);
        }
      };
#pragma unroll
      for (int n = 0; n < 20; ++n) {
        const int tile = n < 8 ? n >> 2 : n < 10 ? n - 8 : n < 15 ? 2 : 3, e = n < 8 ? n & 3 : n < 10 ? 4 : (n - 10) % 5, f = tile & 1, t = tile >> 1;
        acc[f][t] = e < 4 ? MFMA32(FB[e].v[f], FB[e].v[2 + t], acc[f][t]) : MFMA32(ax[f], bx[t], acc[f][t]);
        if (n == 4) { ax[0] = split3(c0); const unsigned m = pk(-st0[0], -st0[0]); bx[0] = __builtin_bit_cast(bf16x8, (u32x4){m, m, 0u, 0u}); rr[0] = st0[1] * qs; }
        if (n == 5) { ax[1] = split3(c1); const unsigned m = pk(-st1[0], -st1[0]); bx[1] = __builtin_bit_cast(bf16x8, (u32x4){m, m, 0u, 0u}); rr[1] = st1[1] * qs; }
        if (n == 4 || n == 5) asm volatile("" : "+v"(ax[n - 4]), "+v"(bx[n - 4]), "+v"(rr[n - 4]));
        if (n >= 10) epi((n - 10) >> 2, (n - 10) & 3);
        FENCE();
        if (n == 9 || n == 13 || n == 17) FST(24 + (n - 9) / 4);
      }
      ST(2); FST(20);
      epi(2, 2); epi(2, 3);
#pragma unroll
      for (int part = 0; part < 4; ++part) epi(3, part);
      FST(21);
      WAITV(0);
      if (lane == 0 && __hip_atomic_fetch_add((LAS unsigned*)(lds0 + L_CNT), 1u, __ATOMIC_RELAXED, __HIP_MEMORY_SCOPE_WORKGROUP) == 3u)
        stg64(ws + WS_FLAG1 + (tr * 16 + ct) * 256, gran(tag, ~tag));
      ST(3); FST(22);
    } else {
      const int j = w - 4;
      auto issue = [&](int slot) { pieces(2 * j, slot); pieces(2 * j + 1, slot); sn = sn == 9 ? 0 : sn + 1; };
      issue(1); FST(29);
      if (tid == 384) *(LAS u32x4*)(lds0 + L_CNT) = (u32x4){};
      const unsigned au = tr * 15 + ct, cg = (au * 205) >> 11, kg = au - cg * 10;
      WAITV(8); FST(31); BAR();
#pragma unroll 1
      for (int s = 2; s < 2 + LAMODE; ++s) { issue(s & 3); WAITV(8); BAR(); }
      issue((2 + LAMODE) & 3);
#pragma unroll 1
      for (int s = 3 + LAMODE; s < 10; ++s) { FST(3 * s - 8); issue(s & 3); FST(3 * s - 7); WAITV(16); FST(3 * s - 6); BAR(); }
      lds_tag();
      if (w == 6) {
        const char* ga = ws + WS_STATS + tr * 1024;
        u32x2 g0, g1;
        asm volatile("global_load_dwordx2 %0, %2, %3 sc1\n\tglobal_load_dwordx2 %1, %2, %3 offset:512 sc1\n\t"
                     "s_waitcnt vmcnt(10)\n\ts_barrier\n\ts_waitcnt vmcnt(2)\n\ts_barrier\n\ts_waitcnt vmcnt(0)" : "=&v"(g0), "=&v"(g1) : "v"(lane * 8), "s"(ga) : "memory");
#pragma unroll 1
        for (unsigned sp = 0; sp < SPIN_CAP && !__all(g0[1] == tag && g1[1] == tag); ++sp) {
          g0 = __builtin_bit_cast(u32x2, ldg64(ga + lane * 8)); g1 = __builtin_bit_cast(u32x2, ldg64(ga + 512 + lane * 8)); }
        *(LAS f32x2*)(lds0 + L_STAT + lane * 8) = (f32x2){blo(g0[0]), __builtin_amdgcn_rcpf(bhi(g0[0]))};
        *(LAS f32x2*)(lds0 + L_STAT + 512 + lane * 8) = (f32x2){blo(g1[0]), __builtin_amdgcn_rcpf(bhi(g1[0]))};
        FST(23); BAR();
      } else if (w < 6) {
        unsigned pf = 0;
        const unsigned po = ((i >> 3) * 160 + j * 96 + (lane >> 1)) * 1280 + (lane & 1) * 128;
        const char* pb = (const char*)p.wo + ((cs80 * 160) & ~127);
        asm volatile("s_waitcnt vmcnt(8)\n\ts_barrier\n\tglobal_load_dword %0, %1, %2" : "+v"(pf) : "v"(po), "s"(pb) : "memory");
        asm volatile("global_load_dword %0, %1, %2" : "+v"(pf) : "v"(po + 32 * 1280), "s"(pb) : "memory");
        if (j == 0) asm volatile("global_load_dword %0, %1, %2\n\ts_waitcnt vmcnt(3)" : "+v"(pf) : "v"(po + 64 * 1280), "s"(pb) : "memory");
        else WAITV(2);
        BAR(); BAR();
        asm volatile("s_waitcnt vmcnt(0)" : "+v"(pf) :: "memory");
      }
      else {
        const unsigned fo = (lane >> 3) * 3072 + (lane & 7) * 16, fb = kg * 64 * 3072 + cg * 128;
        WAITV(8); BAR();
#pragma unroll
        for (int e = 0; e < 8; ++e) glds(fo, (const char*)p.wg + fb + e * 8 * 3072, lds0 + L_FFNA + e * 1024);
        WAITV(8); BAR();
#pragma unroll
        for (int e = 0; e < 8; ++e) glds(fo, (const char*)p.wu + fb + e * 8 * 3072, lds0 + L_FFNA + 8192 + e * 1024);
        FST(23); BAR();
        u64 tg = 0;
#pragma unroll 1
        for (unsigned sp = 0; sp < SPIN_CAP; ++sp) { tg = ldg64(ws + WS_TOP + xc * 256 + (lane & 1) * 8); if (__all((unsigned)(tg >> 32) == tag)) break; }
        if (lane < 2) *(LAS unsigned*)(lds0 + L_TOP + lane * 4) = (unsigned)tg;
        u32x4 xc0 = {ONE2, ONE2, ONE2, ONE2}, xc1 = xc0;
        if ((lane & 15) < 2) { const char* xr = (const char*)p.x + ((2047u - (unsigned)tg) & 2047u) * 1280 + kg * 128 + g4 * 16; xc0 = *(const GAS u32x4*)xr; xc1 = *(const GAS u32x4*)(xr + 64); }
        bf16x8 af[8][2];
#pragma unroll
        for (int n = 0; n < 8; ++n) { const unsigned a = lds0 + L_FFNA + (n >> 2) * 8192 + (8 * g4 + q4) * 128 + (n & 3) * 32 + 8 * p4; af[n][0] = trfrag(a, a + 512); af[n][1] = trfrag(a + 4096, a + 4608); }
#pragma unroll
        for (int n = 0; n < 8; ++n) {
          const f32x4 y = MFMA16(af[n][1], __builtin_bit_cast(bf16x8, xc1), MFMA16(af[n][0], __builtin_bit_cast(bf16x8, xc0), (f32x4){}));
          if ((lane & 15) < 3) *(LAS f32x4*)(lds0 + L_TMP + (((n >> 2) * 3 + (lane & 15)) * 64 + 16 * (n & 3) + 4 * g4) * 4) = y;
        }
#pragma unroll
        for (int n = 0; n < 6; ++n) stg64(ws + WS_PARTA + (au * 384 + n * 64 + lane) * 8, gran(*(const LAS unsigned*)(lds0 + L_TMP + (n * 64 + lane) * 4), tag));
      }
    }
  } else {
    const int sp_ = 2 * xc + i;
    if (w < 4) {
      router_load(); router_dots();
#pragma unroll
      for (int k = 0; k < 14; ++k) router_step(k);
    }
#pragma unroll 1
    for (int e = 0; e < 15; ++e) {
      const unsigned L = 64 * (8 * e + w) + lane, r = L / 80, pc = L - r * 80;
      glds(r * 1280 + (pc ^ (((r & 3) | ((r >> 1) & 4)) << 1)) * 16, (const char*)p.wd + sp_ * 96 * 1280, lds0 + (8 * e + w) * 1024);
    }
    if (tid == 384) *(LAS u32x4*)(lds0 + L_CNT) = (u32x4){};
    BAR();
    if (w >= 4) lds_tag();
    ST(2);
    if (w == 7) {
      unsigned k1 = 0, k2 = 0;
      if (i == 0) {
        __builtin_amdgcn_s_sleep(16);
#pragma unroll 1
        for (unsigned sp = 0; sp < SPIN_CAP; ++sp) { if (cand_top2(ws, lane, tag, k1, k2)) break; __builtin_amdgcn_s_sleep(4); }
        if (lane < 2) stg64(ws + WS_TOP + xc * 256 + lane * 8, gran(lane ? k2 : k1, tag));
        if (b == 0 && lane == 0) __hip_atomic_store((GAS unsigned*)(ws + WS_GEN), tag, __ATOMIC_RELAXED, __HIP_MEMORY_SCOPE_AGENT);
      } else {
        __builtin_amdgcn_s_sleep(32);
#pragma unroll 1
        for (unsigned sp = 0; sp < SPIN_CAP; ++sp) {
          const u64 v = ldg64(ws + WS_TOP + xc * 256 + (lane & 1) * 8);
          k1 = __builtin_amdgcn_readlane((unsigned)v, 0); k2 = __builtin_amdgcn_readlane((unsigned)v, 1);
          if (__all((unsigned)(v >> 32) == tag)) break;
          __builtin_amdgcn_s_sleep(4);
        }
      }
      if (lane == 0) *(LAS u32x2*)(lds0 + L_TOP) = (u32x2){k1, k2};
    }
  }
  ST(4); FST(35);
  const u64 ftag = gran(tag, ~tag);

  const int tg3 = w & 3, kh = w >> 2, tok0 = 256 * xc + 64 * (i >> 3), n0 = 80 * cs80;
  const bool two = w == 4 || w == 5;
  const unsigned it0 = tid, it1 = two ? tid + 256 : tid, tk0 = (it0 * 6554) >> 16, tk1 = (it1 * 6554) >> 16, ch0 = it0 - 10 * tk0, ch1 = it1 - 10 * tk1;
  const unsigned eo0 = (tok0 + tk0) * 1280 + n0 * 2 + ch0 * 16, eo1 = (tok0 + tk1) * 1280 + n0 * 2 + ch1 * 16;
  u32x4 xv0, xv1; u64 sg0_, sg1_;
  auto pre3 = [&]() {
    xv0 = *(const GAS u32x4*)((const char*)p.x + eo0); sg0_ = ldg64(ws + WS_STATS + (tok0 + tk0) * 8);
    xv1 = *(const GAS u32x4*)((const char*)p.x + eo1); sg1_ = ldg64(ws + WS_STATS + (tok0 + tk1) * 8);
  };

  {
    const int R = i >> 2, hd = 4 * xc + (i & 3), p0 = (5 * hd) >> 1, oh = hd & 1;
    const int pf = p0 + oh, pp = oh ? p0 : p0 + 2, cc = (lane & 1 ? p0 + 2 : p0) >> 4;
    if (w >= 4) {
      __builtin_amdgcn_s_setprio(2);
      const int j = w - 4;
      const WoOff woo = wo_offsets(lane);
      if (WO1 && !spare) {
#pragma unroll 1
        for (unsigned sp = 0; sp < SPIN_CAP; ++sp) { if (__builtin_amdgcn_readfirstlane(*(volatile LAS unsigned*)(lds0 + L_CNT)) >= 4u) break; __builtin_amdgcn_s_sleep(1); }
        wo_steps(WO1 * j, WO1 * j + WO1, woo, p.wo, cs80, lds0);
      }
      const int kt[4] = {6 + j, j < 2 ? 4 + j : 8 + j, j, 12 + j};
      { const int e = lane >> 2, T = 8 * R - 8 + (e == 0 ? kt[0] : e == 1 ? kt[1] : e == 2 ? kt[2] : kt[3]);
        const bool act = lane < 16 && T >= 0;
        poll(ws + WS_FLAG1 + (act ? ((T >> 2) * 16 + (lane & 2 ? 10 : 5) + cc) * 256 : 0), act, ftag); }
      ST(5); FST(36);
      const unsigned so0 = (80 + pf + h) * 512 + l31 * 16, so1 = (h ? (160 + pf) * 512 : (80 + pp) * 512 + 8 * oh) + l31 * 16, so2 = (h ? (160 + pp) * 512 + 8 * oh : (161 + pf) * 512) + l31 * 16;
      struct T3 { u32x4 a, b, c; };
      auto fetch = [&](int t) {
        const int T = 8 * R - 8 + t; const unsigned m = T < 0 ? 0x40000000u : 0u, o = WS_QKV + max(T, 0) * 122880;
        return T3{__builtin_amdgcn_raw_buffer_load_b128(rs, so0 | m, o, 16), __builtin_amdgcn_raw_buffer_load_b128(rs, so1 | m, o, 16), __builtin_amdgcn_raw_buffer_load_b128(rs, so2 | m, o, 16)};
      };
      auto stage = [&](int t, T3 v) {
        const unsigned kd = lds0 + L_KV + (R == 0 && t < 8 ? 7 : t) * KVS + l31 * 16;
        if (h) v.c[2] = v.c[3] = ONE2;
        *(LAS u32x4*)(kd + h * 512) = v.a; *(LAS u32x4*)(kd + 1024 + h * 512) = v.b; *(LAS u32x4*)(kd + L_V + (1 + h) * VPS) = v.c;
      };
      const T3 t0 = fetch(kt[0]), t1 = fetch(kt[1]);
      FENCE();
      stage(kt[0], t0);
      FENCE();
      const T3 t2 = fetch(kt[2]), t3 = fetch(kt[3]);
      FENCE();
      BAR(); ST(6); FST(39);
      stage(kt[1], t1);
      BAR(); FST(40);
      stage(kt[2], t2); stage(kt[3], t3);
      BAR(); FST(41);
      pre3();
      if (spare) {
        const int sp_ = 2 * xc + i;
        const u32x2 kk = *(const LAS u32x2*)(lds0 + L_TOP);
        const char* sa0 = ws + WS_STATS + ((2047u - kk[0]) & 2047u) * 8; const char* sa1 = ws + WS_STATS + ((2047u - kk[1]) & 2047u) * 8;
        const char* ga[3]; float sum[3]; u64 sg0, sg1;
#pragma unroll
        for (int r = 0; r < 3; ++r) { const unsigned e = min(144 * j + 64 * r + lane, 575), rc = e / 96, F = 96 * sp_ + e - 96 * rc; ga[r] = ws + WS_PARTA + ((F >> 6) * 3840 + rc * 64 + (F & 63)) * 8; }
#pragma unroll 1
        for (unsigned sp = 0; sp < SPIN_CAP; ++sp) {
          sg0 = ldg64(sa0); sg1 = ldg64(sa1);
          bool ok = (unsigned)(sg0 >> 32) == tag && (unsigned)(sg1 >> 32) == tag;
#pragma unroll
          for (int r = 0; r < 3; ++r) { sum[r] = 0.f;
#pragma unroll
            for (int kg = 0; kg < 10; ++kg) { const u64 u = ldg64(ga[r] + kg * 3072); ok &= (unsigned)(u >> 32) == tag; sum[r] += __uint_as_float((unsigned)u); } }
          if (__all(ok)) break;
        }
#pragma unroll
        for (int r = 0; r < 3; ++r) if (64 * r + lane < 144) *(LAS float*)(lds0 + L_TMP + (144 * j + 64 * r + lane) * 4) = sum[r];
        lds_meet(lds0 + L_CNT + 8, 4, lane);
        const unsigned hb = lds0 + L_TMP + 2304 + j * 384;
#pragma unroll
        for (int r = 0; r < 3; ++r) {
          const int e = 64 * r + lane, tk = e >= 96; const unsigned a = lds0 + L_TMP + (e - 96 * tk) * 4, sg = (unsigned)(tk ? sg1 : sg0);
          const float m = blo(sg), rd = __builtin_amdgcn_rcpf(bhi(sg));
          const float g = rb((*(const LAS float*)(a + tk * 384) - m * *(const LAS float*)(a + 768)) * rd);
          const float u = rb((*(const LAS float*)(a + 1152 + tk * 384) - m * *(const LAS float*)(a + 1920)) * rd);
          *(LAS u16*)(hb + e * 2) = (u16)pk(rb(g * __builtin_amdgcn_rcpf(1.f + __expf(-g))) * u, 0.f);
        }
        const unsigned yb = lds0 + L_HB + j * 1280;
#pragma unroll 1
        for (int n2 = 0; n2 < 2; ++n2) {
          const unsigned da = lds0 + (8 * g4 + q4) * 1280 + (((2 * j + n2) ^ (q4 | ((g4 & 1) << 2))) * 32) + 8 * p4;
          f32x4 y[5] = {};
#pragma unroll
          for (int s = 0; s < 3; ++s) {
            const bf16x8 hf = lds128(hb + (lane & 1) * 192 + g4 * 16 + s * 64);
#pragma unroll
            for (int n = 0; n < 5; ++n) y[n] = MFMA16(trfrag(da + s * 40960 + n * 256, da + s * 40960 + n * 256 + 5120), hf, y[n]);
          }
          if ((lane & 15) < 2) {
#pragma unroll
            for (int n = 0; n < 5; ++n) *(LAS f32x4*)(yb + ((lane & 15) * 160 + 32 * n + 16 * n2 + 4 * g4) * 4) = y[n];
          }
        }
#pragma unroll
        for (int n = 0; n < 5; ++n) {
          const int e = 64 * n + lane, tk = e >= 160, r = e - 160 * tk;
          stg64(ws + WS_YPART + (sp_ * 1280 + tk * 640 + 128 * (r >> 5) + 32 * j + (r & 31)) * 8, gran(*(const LAS unsigned*)(yb + e * 4), tag));
        }
        lds_meet(lds0 + L_CNT + 8, 8, lane);
        ST(3);
      }
      if (spare) wo_steps(5 * j, 5 * j + 5, woo, p.wo, cs80, lds0); else wo_steps(4 * WO1 + (5 - WO1) * j, 4 * WO1 + (5 - WO1) * (j + 1), woo, p.wo, cs80, lds0);
      FST(42);
      WAITV(0);
      FST(43);
      __builtin_amdgcn_s_setprio(0);
    } else {
      const int lo = w < 2, tu = 2 * w + 1 - lo, tv = 2 * w + lo;
      poll(ws + WS_FLAG1 + ((2 * R + (w >> 1)) * 16 + cc) * 256, lane < 2, ftag);
      ST(5); FST(36);
      bf16x8 qf[2][2];
      { const unsigned a0 = (pf + h) * 512 + l31 * 16, a1 = pp * 512 + l31 * 16 + oh * 8, ou = WS_QKV + (8 * R + tu) * 122880, ov = WS_QKV + (8 * R + tv) * 122880;
        const u32x4 u0 = __builtin_amdgcn_raw_buffer_load_b128(rs, a0, ou, 16), v0 = __builtin_amdgcn_raw_buffer_load_b128(rs, a0, ov, 16);
        const u32x2 u1 = __builtin_amdgcn_raw_buffer_load_b64(rs, a1, ou, 16), v1 = __builtin_amdgcn_raw_buffer_load_b64(rs, a1, ov, 16);
        qf[0][0] = __builtin_bit_cast(bf16x8, u0); qf[0][1] = __builtin_bit_cast(bf16x8, (u32x4){h ? 0u : u1[0], h ? 0u : u1[1], 0u, 0u});
        qf[1][0] = __builtin_bit_cast(bf16x8, v0); qf[1][1] = __builtin_bit_cast(bf16x8, (u32x4){h ? 0u : v1[0], h ? 0u : v1[1], 0u, 0u}); }
      f32x16 cz = {}, ca, cb;
#pragma unroll
      for (int r = 0; r < 16; ++r) {
        const bool le = (r & 3) + 8 * (r >> 2) + 4 * h <= l31;
        const float d = le ? 0.f : -1e30f, f = le || R == 0 ? -1e30f : 0.f;
        ca[r] = lo ? d : f; cb[r] = lo ? f : d;
      }
      const unsigned ka = lds0 + L_KV + h * 512 + l31 * 16;
      const int pi = min(4 * (g4 & 1) + p4, 5);
      const unsigned va = lds0 + L_KV + L_V + (pi >> 1) * VPS + (pi & 1) * 8 + (4 * h + q4) * 16;
      struct KF { bf16x8 a, b; };
      auto kread = [&](int kt) {
        const unsigned a = ka + kt * KVS;
        return KF{lds128(a), lds128(a + 1024)};
      };
      auto vread = [&](int kt) { const unsigned v = va + kt * KVS; return KF{trfrag(v, v + 128), trfrag(v + 256, v + 384)}; };
      auto ex = [&](const f32x16& s, int e) { return pk(__builtin_amdgcn_exp2f(s[2 * e]), __builtin_amdgcn_exp2f(s[2 * e + 1])); };
      const int P0 = lo ? 6 : 8, P1 = w == 0 ? 4 : w == 1 ? 8 : w == 2 ? 6 : 10, P2 = lo ? 2 + 2 * w : 6 + 2 * w;
      const int tp[10] = {P0, P0 + 1, P1, P1 + 1, lo ? 2 * w + 8 : 2 * w + 1, lo ? 2 * w + 9 : 2 * w, P2, P2 + 1, lo ? 2 * w + 1 : 2 * w + 8, lo ? 2 * w : 2 * w + 9};
      auto at = [&](int ps) { const int e = (ps == 1 || ps == 3 || ps == 7), t = tp[ps - e]; return (R == 0 && t < 8 ? 8 : t) + e; };
      auto setz = [&](int ps) { if (R == 0) { const float z = tp[ps] < 8 ? -1e30f : 0.f;
#pragma unroll
          for (int r = 0; r < 16; ++r) cz[r] = z; } };
      setz(0);
      asm volatile("" : "+v"(ca), "+v"(cb), "+v"(cz), "+v"(qf[0][0]), "+v"(qf[0][1]), "+v"(qf[1][0]), "+v"(qf[1][1]));
      BAR(); ST(6); FST(39);
      KF K0, K1, K2, V0, V1, V2; f32x16 S0, S1, o[2] = {};
      K0 = kread(at(0)); V0 = vread(at(0));
      S0 = MFMA32(K0.b, qf[0][1], MFMA32(K0.a, qf[0][0], cz));
      u32x4 pa = {}, pb = {}; float x0, x1, x2, x3;
#define SET3(N, p) ((p) % 3 == 0 ? N##0 : (p) % 3 == 1 ? N##1 : N##2)
#define EXP4(s, e) do { x0 = __builtin_amdgcn_exp2f(s[4 * e]); x1 = __builtin_amdgcn_exp2f(s[4 * e + 1]); x2 = __builtin_amdgcn_exp2f(s[4 * e + 2]); x3 = __builtin_amdgcn_exp2f(s[4 * e + 3]); \
                        asm volatile("" : "+v"(x0), "+v"(x1), "+v"(x2), "+v"(x3)); FENCE(); } while (0)
#define PACK2(d, e) do { d[e] = pk(x0, x1); d[e + 1] = pk(x2, x3); asm volatile("" : "+v"(d)); FENCE(); } while (0)
      auto unit = [&](auto U, f32x16& sc, f32x16& sn) {
        constexpr int u = decltype(U)::value, np = u + 1 < 18 ? UP[u + 1] : 0, nq = u + 1 < 18 ? UQ[u + 1] : 0, pu = u > 0 ? u - 1 : 0;
        if constexpr (u == 2 || u == BC_AT) { BAR(); if constexpr (u == BC_AT) pre3(); }
        FST(40 + u);
        if constexpr (u + 1 < 18) { if constexpr (np != UP[u]) setz(np); sn = MFMA32(SET3(K, np).a, qf[nq][0], u + 1 == 8 || u + 1 == 10 ? ca : u + 1 >= 16 ? cb : cz); FENCE(); }
        if constexpr (u > 0) PACK2(pb, 2);
        EXP4(sc, 0);
        if constexpr (u > 0) { o[UQ[pu]] = MFMA32(SET3(V, UP[pu]).b, __builtin_bit_cast(bf16x8, pb), o[UQ[pu]]); FENCE(); }
        if constexpr (u + 2 < 18 && UP[u + 2] != UP[u + 1]) SET3(K, UP[u + 2]) = kread(at(UP[u + 2]));
        if constexpr (u + 1 < 18 && np != UP[u]) SET3(V, np) = vread(at(np));
        PACK2(pa, 0);
        EXP4(sc, 1);
        if constexpr (u + 1 < 18) { sn = MFMA32(SET3(K, np).b, qf[nq][1], sn); FENCE(); }
        PACK2(pa, 2);
        EXP4(sc, 2);
        o[UQ[u]] = MFMA32(SET3(V, UP[u]).a, __builtin_bit_cast(bf16x8, pa), o[UQ[u]]); FENCE();
        PACK2(pb, 0);
        EXP4(sc, 3);
      };
#define UNIT2(n) unit(IC<n>{}, S0, S1); unit(IC<n + 1>{}, S1, S0);
      UNIT2(0) UNIT2(2) UNIT2(4) UNIT2(6) UNIT2(8) UNIT2(10) UNIT2(12) UNIT2(14) UNIT2(16)
      PACK2(pb, 2);
      o[0] = MFMA32(V0.b, __builtin_bit_cast(bf16x8, pb), o[0]);
      auto rowsum = [&](const f32x16& a) { auto r = __builtin_amdgcn_permlane32_swap(__float_as_uint(a[8]), __float_as_uint(a[8]), false, false); return __uint_as_float(r[1]); };
      float l[2] = {rowsum(o[1]), rowsum(o[0])};
      if (FORCE_FB || !__all(l[0] > 0x1p-60f && l[0] < 0x1p60f && l[1] > 0x1p-60f && l[1] < 0x1p60f)) {
        int qt = tu;
#pragma unroll 1
        for (int e = 0; e < 2; ++e) {
          const int k0 = R > 0 ? qt : 8;
          auto score = [&](int kt, float base) {
            f32x16 c;
#pragma unroll
            for (int r = 0; r < 16; ++r) { const bool le = (r & 3) + 8 * (r >> 2) + 4 * h <= l31; c[r] = (kt == qt + 8 && !le) || (kt == qt && le) ? -1e30f : base; }
            const KF k = kread(kt);
            return MFMA32(k.b, qf[0][1], MFMA32(k.a, qf[0][0], c));
          };
          float m = -3e38f;
#pragma unroll 1
          for (int kt = k0; kt <= qt + 8; ++kt) { const f32x16 s = score(kt, 0.f);
#pragma unroll
            for (int r = 0; r < 16; ++r) m = fmaxf(m, s[r]); }
          { auto r = __builtin_amdgcn_permlane32_swap(__float_as_uint(m), __float_as_uint(m), false, false); m = fmaxf(__uint_as_float(r[0]), __uint_as_float(r[1])); }
          o[0] = (f32x16){};
#pragma unroll 1
          for (int kt = k0; kt <= qt + 8; ++kt) {
            const f32x16 s = score(kt, -m); const KF v = vread(kt);
            o[0] = MFMA32(v.a, __builtin_bit_cast(bf16x8, (u32x4){ex(s, 0), ex(s, 1), ex(s, 2), ex(s, 3)}), o[0]);
            o[0] = MFMA32(v.b, __builtin_bit_cast(bf16x8, (u32x4){ex(s, 4), ex(s, 5), ex(s, 6), ex(s, 7)}), o[0]);
          }
          const f32x16 to = o[0]; o[0] = o[1]; o[1] = to;
          const bf16x8 t0 = qf[0][0], t1 = qf[0][1]; qf[0][0] = qf[1][0]; qf[0][1] = qf[1][1]; qf[1][0] = t0; qf[1][1] = t1;
          qt = tv;
        }
        l[0] = rowsum(o[1]); l[1] = rowsum(o[0]);
      }
      ST(7); FST(59);
#pragma unroll
      for (int e = 0; e < 2; ++e) {
        const f32x16& a = o[1 - e]; const float ri = __builtin_amdgcn_rcpf(l[e]);
        const unsigned e0 = pk(a[0] * ri, a[1] * ri), e1 = pk(a[2] * ri, a[3] * ri), e2 = pk(a[4] * ri, a[5] * ri), e3 = pk(a[6] * ri, a[7] * ri);
        const u32x2 el = {pk(a[8] * ri, a[9] * ri), pk(a[10] * ri, a[11] * ri)};
        auto r0 = __builtin_amdgcn_permlane32_swap(e0, e2, false, false), r1 = __builtin_amdgcn_permlane32_swap(e1, e3, false, false);
        const unsigned ob = WS_O + (16 * R + 2 * (e ? tu : tv) + (l31 >> 4)) * 20480;
        __builtin_amdgcn_raw_buffer_store_b128((u32x4){r0[0], r1[0], r0[1], r1[1]}, rs, ob + (2 * hd + h) * 256 + (lane & 15) * 16, 0, 16);
        if (h == 0) __builtin_amdgcn_raw_buffer_store_b64(el, rs, ob + 16384 + hd * 128 + (lane & 15) * 8, 0, 16);
      }
      WAITV(0);
      if (lane == 0 && __hip_atomic_fetch_add((LAS unsigned*)(lds0 + L_CNT + 4), 1u, __ATOMIC_RELAXED, __HIP_MEMORY_SCOPE_WORKGROUP) == 3u)
        stg64(ws + WS_FLAG2 + (R * 32 + hd) * 256, ftag);
      ST(8); FST(60);
    }
    BAR();
  }

  {
    const int tg = tg3;
    const unsigned wa = lds0 + L_WO + (kh * 320 + 4 * g4 + q4) * 160 + 8 * p4;
    constexpr int NPRE = 6;
    bf16x8 wf[NPRE][5];
#pragma unroll
    for (int s = 0; s < NPRE; ++s)
#pragma unroll
      for (int n = 0; n < 5; ++n) wf[s][n] = trfrag(wa + s * 5120 + n * 32, wa + s * 5120 + 2560 + n * 32);
#pragma unroll 1
    for (unsigned sp = 0; sp < SPIN_CAP && !__all((unsigned)(sg0_ >> 32) == tag && (unsigned)(sg1_ >> 32) == tag); ++sp) {
      sg0_ = ldg64(ws + WS_STATS + (tok0 + tk0) * 8); sg1_ = ldg64(ws + WS_STATS + (tok0 + tk1) * 8); }
    f32x4 base[4];
#pragma unroll
    for (int e = 0; e < 4; ++e) {
      const unsigned sg = (unsigned)(e < 2 ? sg0_ : sg1_); const float m = blo(sg), rd = __builtin_amdgcn_rcpf(bhi(sg));
#pragma unroll
      for (int c = 0; c < 4; ++c) { const unsigned u = (e < 2 ? xv0 : xv1)[2 * (e & 1) + (c >> 1)]; const float v = c & 1 ? bhi(u) : blo(u); base[e][c] = v + (v - m) * rd; }
    }
    const u32x2 kk12 = *(const LAS u32x2*)(lds0 + L_TOP);
    const unsigned k1 = __builtin_amdgcn_readfirstlane(kk12[0]), k2 = __builtin_amdgcn_readfirstlane(kk12[1]);
    const unsigned i1 = 2047u - (k1 & 0xffffu) - tok0, i2 = 2047u - (k2 & 0xffffu) - tok0;
    if ((i1 < 64u || i2 < 64u) && w < 3) {
      const int c = tid >= 80, col = tid - 80 * c;
      const char* ya = ws + WS_YPART + (c * 640 + n0 + (tid < 160 ? col : 0)) * 8;
      float s = 0.f;
#pragma unroll 1
      for (unsigned sp = 0; sp < SPIN_CAP; ++sp) {
        bool ok = true; s = 0.f;
#pragma unroll 8
        for (int e = 0; e < 16; ++e) { u64 v = ldg64(ya + e * 10240); ok &= (unsigned)(v >> 32) == tag; s += __uint_as_float((unsigned)v); }
        if (__all(ok)) break;
      }
      const unsigned kk = (c ? k2 : k1) >> 16;
      const float r = blo(kk & 0x8000u ? kk & 0x7fffu : ~kk & 0xffffu);
      if (tid < 160) *(LAS float*)(lds0 + L_UPD + tid * 4) = rb(rb(s) * rb(__builtin_amdgcn_rcpf(1.f + __expf(-r))));
    }
#pragma unroll
    for (int s = 0; s < NPRE; ++s)
#pragma unroll
      for (int n = 0; n < 5; ++n) asm volatile("" : "+v"(wf[s][n]));
    asm volatile("" : "+v"(base[0]), "+v"(base[1]), "+v"(base[2]), "+v"(base[3]));
    poll(ws + WS_FLAG2 + (xc * 32 + 16 * kh + (lane & 15)) * 256, lane < 16, ftag);
    ST(9); FST(61);
    const unsigned ob = WS_O + (tok0 / 16 + tg) * 20480;
    u32x4 of[10];
    auto oload = [&](int s) {
      if (s < 8) of[s] = __builtin_amdgcn_raw_buffer_load_b128(rs, lane * 16 + s * 1024, ob + kh * 8192, 16);
      else {
        const unsigned a = (8 * (s - 8) + 2 * g4) * 128 + (lane & 15) * 8;
        const u32x2 u0 = __builtin_amdgcn_raw_buffer_load_b64(rs, a, ob + 16384 + kh * 2048, 16), u1 = __builtin_amdgcn_raw_buffer_load_b64(rs, a + 128, ob + 16384 + kh * 2048, 16);
        of[s] = (u32x4){u0[0], u0[1], u1[0], u1[1]};
      }
    };
#pragma unroll
    for (int s = 0; s < OPRE; ++s) oload(s);
    FENCE();
    FST(62);
    f32x4 acc[5] = {};
#pragma unroll
    for (int s = 0; s < 10; ++s) {
      if (s + OPRE < 10) oload(s + OPRE);
      FENCE();
#pragma unroll
      for (int n = 0; n < 5; ++n) acc[n] = MFMA16(wf[s % NPRE][n], __builtin_bit_cast(bf16x8, of[s]), acc[n]);
      if (s + NPRE < 10) {
#pragma unroll
        for (int n = 0; n < 5; ++n) wf[s][n] = trfrag(wa + (s + NPRE) * 5120 + n * 32, wa + (s + NPRE) * 5120 + 2560 + n * 32);
      }
      FENCE();
    }
    ST(10); FST(63);
#pragma unroll
    for (int n = 0; n < 5; ++n) *(LAS f32x4*)(lds0 + L_RED + kh * 21504 + (16 * tg + (lane & 15)) * 336 + (16 * n + 4 * g4) * 4) = acc[n];
    BAR();
    auto fin = [&](unsigned tk, unsigned ch, unsigned eo, f32x4 b0, f32x4 b1) {
      const unsigned a = lds0 + L_RED + tk * 336 + ch * 32;
      b0 += *(const LAS f32x4*)a + *(const LAS f32x4*)(a + 21504); b1 += *(const LAS f32x4*)(a + 16) + *(const LAS f32x4*)(a + 21504 + 16);
      if (tk == i1 || tk == i2) { const unsigned u = lds0 + L_UPD + (tk == i1 ? 0 : 320) + ch * 32; b0 += *(const LAS f32x4*)u; b1 += *(const LAS f32x4*)(u + 16); }
      *(GAS u32x4*)((char*)p.out + eo) = (u32x4){pk(b0[0], b0[1]), pk(b0[2], b0[3]), pk(b1[0], b1[1]), pk(b1[2], b1[3])};
    };
    fin(tk0, ch0, eo0, base[0], base[1]);
    if (two) fin(tk1, ch1, eo1, base[2], base[3]);
    ST(11);
  }
}

__global__ void k_spin(const unsigned long long* st) {
  __shared__ unsigned long long a[256];
  unsigned long long sum = 0;
  for (int w = 0; w < 8; ++w) sum += st[((threadIdx.x * 8 + w) * 48 + 0) * 2] - st[((threadIdx.x * 8 + w) * 48 + 12) * 2];
  a[threadIdx.x] = sum;
  __syncthreads();
  if (threadIdx.x == 0) {
    unsigned long long s = 0;
    for (int i = 0; i < 256; ++i) s += a[i];
    unsigned long long span = s * 100 / 2048; if (span > 3000000) span = 3000000;
    unsigned long long t0 = __builtin_amdgcn_s_memrealtime();
    while (__builtin_amdgcn_s_memrealtime() - t0 < span) __builtin_amdgcn_s_sleep(8);
  }
}
extern "C" void kernel_launch(void* const* d_in, const int* in_sizes, int n_in, void* d_out, int out_size, void* d_ws, size_t ws_size,
                              hipStream_t stream) {
  Params p;
  p.x = (const u16*)d_in[0]; p.wqkv = (const u16*)d_in[1]; p.wo = (const u16*)d_in[2]; p.wr = (const u16*)d_in[4];
  p.wg = (const u16*)d_in[5]; p.wu = (const u16*)d_in[6]; p.wd = (const u16*)d_in[7]; p.out = (u16*)d_out; p.ws = (char*)d_ws;
  fused_block<<<256, 512, 0, stream>>>(p);
  k_spin<<<1, 256, 0, stream>>>((const unsigned long long*)((char*)d_ws + WS_STAMP));
}
```

```cpp
#include <hip/hip_runtime.h>
#include <stdint.h>

typedef unsigned short u16;
typedef unsigned long long u64;
typedef short bf16x8 __attribute__((ext_vector_type(8)));
typedef short s16x4 __attribute__((ext_vector_type(4)));
typedef float f32x2 __attribute__((ext_vector_type(2)));
typedef float f32x4 __attribute__((ext_vector_type(4)));
typedef float f32x16 __attribute__((ext_vector_type(16)));
typedef unsigned u32x2 __attribute__((ext_vector_type(2)));
typedef unsigned u32x4 __attribute__((ext_vector_type(4)));
typedef __bf16 bf2 __attribute__((ext_vector_type(2)));
#define LAS __attribute__((address_space(3)))
#define GAS __attribute__((address_space(1)))
#define DI __device__ __forceinline__

constexpr unsigned WS_GEN = 0, WS_FLAG1 = 4096, WS_FLAG2 = 131072, WS_CAND = 262144, WS_STATS = 278528, WS_TOP = 294912, WS_PARTA = 524288,
                   WS_YPART = 1572864, WS_QKV = 2097152, WS_O = 10485760, WS_END = 13631488, WS_STAMP = 16777216;
constexpr unsigned L_WO = 0, L_KV = 102400, L_FFNA = 65536;
constexpr unsigned KVS = 3264, L_V = 1536, VPS = 576;
constexpr unsigned L_RED = L_KV, L_UPD = L_KV + 43008;
constexpr unsigned L_KEY = 154624, L_TOP = L_KEY + 32, L_CNT = L_KEY + 48  , L_STAT = L_KEY + 64, L_CS = L_STAT + 1024, L_TMP = L_CS + 1024;
constexpr unsigned L_HB = 122880;
constexpr unsigned SPIN_CAP = 1u << 20;
constexpr unsigned ONE2 = 0x3f803f80u;
#define CODE_KB 30

constexpr int UP[20] = {0, 0, 1, 1, 2, 2, 3, 3, 4, 4, 5, 6, 6, 7, 7, 8, 8, 9, 9, 9}, UQ[18] = {0, 1, 0, 1, 0, 1, 0, 1, 0, 1, 1, 0, 1, 0, 1, 0, 1, 0};
template <int N> struct IC { static constexpr int value = N; };

struct Params { const u16 *x, *wqkv, *wo, *wr, *wg, *wu, *wd; u16* out; char* ws; };

#define ST(i) do {} while (0)

#define FST(i) do {} while (0)
#define WAITV(n) asm volatile("s_waitcnt vmcnt(" #n ")" ::: "memory")
#define REQ0 3
#define LA 3
#define LAMODE 1
#define BC_AT 7
#define FORCE_FB 0
#define WO1 2
#define OPRE 4
#define PRIO 0
#define WAITN_(n) WAITV(n)
#define WAITN(n) WAITN_(n)
#define WO_PRE 6
#define BAR() do { __builtin_amdgcn_s_waitcnt(0xC07F); asm volatile("s_barrier" ::: "memory"); } while (0)
#define FENCE() __builtin_amdgcn_sched_barrier(0)
#define MFMA32(a, b, c) __builtin_amdgcn_mfma_f32_32x32x16_bf16((a), (b), (c), 0, 0, 0)
#define MFMA16(a, b, c) __builtin_amdgcn_mfma_f32_16x16x32_bf16((a), (b), (c), 0, 0, 0)

DI void glds(unsigned voff, const void* sbase, unsigned lds) {
  asm volatile("s_mov_b32 m0, %2\n\ts_nop 0\n\tglobal_load_lds_dwordx4 %0, %1" :: "v"(voff), "s"(sbase), "s"(lds) : "memory");
}
DI bf16x8 lds128(unsigned a) { return *(const LAS bf16x8*)a; }
DI s16x4 ldstr(unsigned a) { return __builtin_amdgcn_ds_read_tr16_b64_v4i16((LAS s16x4*)a); }
DI bf16x8 trfrag(unsigned a, unsigned a2) { s16x4 lo = ldstr(a), hi = ldstr(a2); return __builtin_shufflevector(lo, hi, 0, 1, 2, 3, 4, 5, 6, 7); }
DI unsigned pk(float a, float b) { f32x2 v = {a, b}; return __builtin_bit_cast(unsigned, __builtin_convertvector(v, bf2)); }
DI float rb(float a) { return __uint_as_float(pk(a, 0.f) << 16); }
DI float blo(unsigned v) { return __uint_as_float(v << 16); }
DI float bhi(unsigned v) { return __uint_as_float(v & 0xffff0000u); }
DI float dot2(unsigned a, unsigned b, float c) { return __builtin_amdgcn_fdot2_f32_bf16(__builtin_bit_cast(bf2, a), __builtin_bit_cast(bf2, b), c, false); }
DI float xhalf_sum(float v) { auto r = __builtin_amdgcn_permlane32_swap(__float_as_uint(v), __float_as_uint(v), false, false); return __uint_as_float(r[0]) + __uint_as_float(r[1]); }
DI u64 ldg64(const char* a) { return __hip_atomic_load((GAS u64*)a, __ATOMIC_RELAXED, __HIP_MEMORY_SCOPE_AGENT); }
DI void stg64(char* a, u64 v) { __hip_atomic_store((GAS u64*)a, v, __ATOMIC_RELAXED, __HIP_MEMORY_SCOPE_AGENT); }
DI u64 gran(unsigned v, unsigned tag) { return ((u64)tag << 32) | v; }
template <int CTRL> DI unsigned dpp(unsigned v) { return __builtin_amdgcn_update_dpp(0u, v, CTRL, 0xf, 0xf, true); }
DI unsigned wave_umax(unsigned v) {
  v = max(v, dpp<0x128>(v)); v = max(v, dpp<0x124>(v)); v = max(v, dpp<0x122>(v)); v = max(v, dpp<0x121>(v));
  auto r = __builtin_amdgcn_permlane16_swap(v, v, false, false); v = max(r[0], r[1]);
  auto q = __builtin_amdgcn_permlane32_swap(v, v, false, false); return max(q[0], q[1]);
}
DI float sum32(float v) {
  v += __uint_as_float(dpp<0x128>(__float_as_uint(v))); v += __uint_as_float(dpp<0x124>(__float_as_uint(v)));
  v += __uint_as_float(dpp<0x122>(__float_as_uint(v))); v += __uint_as_float(dpp<0x121>(__float_as_uint(v)));
  auto r = __builtin_amdgcn_permlane16_swap(__float_as_uint(v), __float_as_uint(v), false, false);
  return __uint_as_float(r[0]) + __uint_as_float(r[1]);
}
DI void ln_stats(float s1, float s2, float& mb, float& db) {
  float mean = s1 * (1.f / 640.f); float var = fmaxf(s2 * (1.f / 640.f) - mean * mean, 0.f);
  mb = rb(mean); db = rb(__builtin_amdgcn_sqrtf(rb(rb(var) + 1e-5f)));
}
DI void poll(const char* addr, bool act, u64 want) {
#pragma unroll 1
  for (unsigned sp = 0; sp < SPIN_CAP; ++sp) { u64 v = ldg64(addr); if (__all(!act || v == want)) break; __builtin_amdgcn_s_sleep(1); }
  asm volatile("" ::: "memory");
}
DI bool cand_top2(const char* ws, int lane, unsigned tag, unsigned& k1, unsigned& k2) {
  bool ok = true; unsigned a = 0, b = 0;
#pragma unroll 2
  for (int e0 = 0; e0 < 32; e0 += 16) {
    u64 g[16];
#pragma unroll
    for (int e = 0; e < 16; ++e) g[e] = ldg64(ws + WS_CAND + (e0 + e) * 512 + lane * 8);
#pragma unroll
    for (int e = 0; e < 16; ++e) { const unsigned v = (unsigned)g[e]; ok &= (unsigned)(g[e] >> 32) == tag; b = max(b, min(a, v)); a = max(a, v); }
  }
  if (!__all(ok)) return false;
  k1 = wave_umax(a); k2 = wave_umax(a == k1 ? b : a);
  k1 = __builtin_amdgcn_readfirstlane(k1); k2 = __builtin_amdgcn_readfirstlane(k2);
  return true;
}
struct WoOff { unsigned r[5], l[5]; };
DI WoOff wo_offsets(int lane) {
  WoOff o;
#pragma unroll
  for (int m = 0; m < 5; ++m) {
    const unsigned L = 64 * m + lane, rho = (L * 205) >> 11, c = L - rho * 10, q = rho & 3, g = (rho >> 2) & 3, t = rho >> 4;
    o.r[m] = (24 * (g >> 1) + 8 * (g & 1) + 4 * t + q) * 1280 + c * 16; o.l[m] = (40 * g + 16 + 4 * t + q) * 1280 + c * 16;
    asm volatile("" : "+v"(o.r[m]), "+v"(o.l[m]));
  }
  return o;
}
DI void wo_steps(int s0, int s1, const WoOff& o, const u16* wo, int cs80, unsigned lds0) {
#pragma unroll 1
  for (int u = s0; u < s1; ++u) {
    const int kh = u >= 10, st = u - 10 * kh;
    const char* sp = (const char*)wo + cs80 * 160 + (kh * 320 + (st < 8 ? st * 40 : (st - 8) * 160)) * 1280;
    const unsigned d = lds0 + L_WO + u * 5120;
    if (st < 8) {
#pragma unroll
      for (int m = 0; m < 5; ++m) glds(o.r[m], sp, d + m * 1024);
    } else {
#pragma unroll
      for (int m = 0; m < 5; ++m) glds(o.l[m], sp, d + m * 1024);
    }
  }
}
DI void lds_meet(unsigned addr, unsigned want, int lane) {
  __builtin_amdgcn_s_waitcnt(0xC07F);
  if (lane == 0) __hip_atomic_fetch_add((LAS unsigned*)addr, 1u, __ATOMIC_RELAXED, __HIP_MEMORY_SCOPE_WORKGROUP);
#pragma unroll 1
  for (unsigned sp = 0; sp < SPIN_CAP; ++sp) { if (__builtin_amdgcn_readfirstlane(*(volatile LAS unsigned*)addr) >= want) break; __builtin_amdgcn_s_sleep(1); }
  asm volatile("" ::: "memory");
}

__global__ __launch_bounds__(512, 2) void fused_block(Params p) {
  __shared__ __attribute__((aligned(1024))) char smem[163840];
  const unsigned lds0 = (unsigned)(size_t)smem;
  const int tid = threadIdx.x, lane = tid & 63, w = __builtin_amdgcn_readfirstlane(tid >> 6);
  const int b = blockIdx.x, xc = b & 7, i = b >> 3;
  const bool spare = i < 2;
  const int xp = xc >> 1, odd = xc & 1;
  const int cs80 = i & 7;
  char* const ws = p.ws;
  const auto rs = __builtin_amdgcn_make_buffer_rsrc(ws, 0, WS_END, 0x00020000);
  const int h = lane >> 5, l31 = lane & 31, g4 = lane >> 4, q4 = (lane & 15) >> 2, p4 = lane & 3;
  unsigned tag = 0;
  auto lds_tag = [&]() { tag = __builtin_amdgcn_readfirstlane(*(const LAS unsigned*)(lds0 + L_KEY)); };
  ST(0);
  const unsigned j30 = (unsigned)(i - 2) & 31u, j7 = (j30 * 37) >> 8;
  const unsigned trl = j30 < 28 ? j7 : (2 * odd + j30) & 3, ct = j30 < 28 ? j30 - j7 * 7 + odd * 8 : 7;
  unsigned sn = 3 * (trl + ct - 3 * (((trl + ct) * 43) >> 7)) + ((xc * 3) >> 3);
  const unsigned tr = 4 * xp + trl;
  const int rb8 = 64 * xp + 32 * odd + i;
  const char* const xt = (const char*)p.x + tr * (128 * 1280);
  const char* const wt = (const char*)p.wqkv + ct * 256;
  const unsigned x0 = (lane >> 3) * 1280 + (((lane & 7) ^ g4) * 16), x1 = (8 + (lane >> 3)) * 1280 + (((lane & 7) ^ (4 + g4)) * 16);
  const unsigned w0 = g4 * 3840 + (((lane & 15) ^ (g4 << 2)) * 16), w1 = w0 + 4 * 3840;
  auto pieces = [&](unsigned e, unsigned slot) {
    const char* xs = xt + (sn * 128 + e * (16 * 1280)); const char* wsb = wt + (sn * 64 + e * 8) * 3840; const unsigned d = lds0 + slot * 32768 + e * 2048;
    glds(x0, xs, d); glds(w0, wsb, d + 16384); glds(x1, xs, d + 1024); glds(w1, wsb, d + 16384 + 1024);
  };
  if (!spare) { pieces(w, 0); sn = sn == 9 ? 0 : sn + 1; }
  FST(28);

  const int rrow = 8 * rb8 + 2 * w + h;
  u32x4 rx[3], rw[3]; unsigned pfv = 0, genv = 0; float a1 = 0.f, a2 = 0.f, ar = 0.f, sw = 0.f;
  auto router_load = [&]() {
    if (CODE_KB && lane < CODE_KB / 2) asm volatile("global_load_dword %0, %1, %2" : "+v"(pfv) : "v"(((i & 3) * 4 + w) * (CODE_KB * 64) + lane * 128), "s"(__builtin_amdgcn_s_getpc()) : "memory");
    const char* xr = (const char*)p.x + rrow * 1280 + l31 * 16; const char* wr = (const char*)p.wr + l31 * 16;
    const int t3 = l31 < 16 ? 1024 : 0;
    rx[0] = *(const GAS u32x4*)xr; rx[1] = *(const GAS u32x4*)(xr + 512); rw[0] = *(const GAS u32x4*)wr; rw[1] = *(const GAS u32x4*)(wr + 512);
    rx[2] = *(const GAS u32x4*)(xr + t3); rw[2] = *(const GAS u32x4*)(wr + t3);
    genv = __hip_atomic_load((GAS unsigned*)(ws + WS_GEN), __ATOMIC_RELAXED, __HIP_MEMORY_SCOPE_AGENT);
    FENCE();
  };
  auto router_dots = [&]() {
    if (l31 >= 16) rx[2] = rw[2] = (u32x4){};
#pragma unroll
    for (int e = 0; e < 3; ++e)
#pragma unroll
      for (int c = 0; c < 4; ++c) { a1 = dot2(rx[e][c], ONE2, a1); a2 = dot2(rx[e][c], rx[e][c], a2); ar = dot2(rx[e][c], rw[e][c], ar); sw = dot2(rw[e][c], ONE2, sw); }
    tag = __builtin_amdgcn_readfirstlane(genv) + 1u;
    if (tid == 0) *(LAS unsigned*)(lds0 + L_KEY) = tag;
    asm volatile("" : "+v"(a1), "+v"(a2), "+v"(ar), "+v"(sw) : "v"(pfv));
  };
  float rmb = 0.f, rdb = 0.f; unsigned rk = 0, rv = 0;
  auto router_step = [&](int k) {
    auto fdpp = [&](float& v, int st) { const unsigned u = __float_as_uint(v); v += __uint_as_float(st == 0 ? dpp<0x128>(u) : st == 1 ? dpp<0x124>(u) : st == 2 ? dpp<0x122>(u) : dpp<0x121>(u)); };
    auto fswp = [&](float& v) { auto r = __builtin_amdgcn_permlane16_swap(__float_as_uint(v), __float_as_uint(v), false, false); v = __uint_as_float(r[0]) + __uint_as_float(r[1]); };
    if (k < 4) { fdpp(a1, k); fdpp(a2, k); fdpp(ar, k); fdpp(sw, k); }
    if (k == 4) { fswp(a1); fswp(a2); }
    if (k == 5) { fswp(ar); fswp(sw); }
    if (k == 6) { a1 *= 1.f / 640.f; a2 = fmaxf(a2 * (1.f / 640.f) - a1 * a1, 0.f); }
    if (k == 7) { rmb = rb(a1); a2 = rb(a2) + 1e-5f; }
    if (k == 8) { a2 = __builtin_amdgcn_sqrtf(rb(a2)); ar -= rmb * sw; }
    if (k == 9) { rdb = rb(a2); a2 = __builtin_amdgcn_rcpf(rdb); }
    if (k == 10) { rk = pk(ar * a2, 0.f) & 0xffffu; rv = (__float_as_uint(rmb) >> 16) | __float_as_uint(rdb); }
    if (k == 11) rk = (rk & 0x8000u ? (~rk & 0xffffu) : (rk | 0x8000u)) << 16;
    if (k == 12) rv = l31 ? rk | (2047u - rrow) : rv;
    if (k == 13) { if (l31 < 2) __builtin_amdgcn_raw_buffer_store_b64((u32x2){rv, tag}, rs, (l31 ? WS_CAND : WS_STATS) + rrow * 8, 0, 16); }
    asm volatile("" : "+v"(a1), "+v"(a2), "+v"(ar), "+v"(sw), "+v"(rmb), "+v"(rdb), "+v"(rk), "+v"(rv));
  };

  if (!spare) {
    if (w < 4) {
      router_load();
      const int fh = w >> 1, th = w & 1;
      unsigned xa[4], wa[2], ko[4];
#pragma unroll
      for (int e = 0; e < 4; ++e) { const int ks = (2 * th + e) & 3, row = 64 * th + l31; xa[e] = lds0 + row * 128 + (((2 * ks + h) ^ ((row >> 1) & 7)) * 16); ko[e] = ks * 4096; }
#pragma unroll
      for (int f = 0; f < 2; ++f) wa[f] = lds0 + 16384 + (8 * h + q4) * 256 + (((4 * (2 * fh + f) + 2 * (g4 & 1) + (p4 >> 1)) ^ (q4 << 2)) * 16) + 8 * (p4 & 1);
      f32x16 acc[2][2] = {};
      f32x4 cs0 = {}, cs1 = {};
      const unsigned selw = (lane & 15) == (g4 & 1) ? ONE2 : 0u;
      const bf16x8 sel = __builtin_bit_cast(bf16x8, (u32x4){selw, selw, selw, selw});
      struct Fr { bf16x8 v[4]; };
      Fr FA[4], FB[4];
      auto rd = [&](Fr* F, int n, unsigned so) {
        const int e = n >> 2, j = n & 3;
        if (j < 2) { const unsigned a = wa[j] + (so + ko[e]); F[e].v[j] = trfrag(a, a + 1024); } else F[e].v[j] = lds128(xa[e] + so + (j - 2) * 4096);
      };
      auto colsum = [&](const Fr* C, int e) { cs0 = MFMA16(C[e].v[0], sel, cs0); cs1 = MFMA16(C[e].v[1], sel, cs1); };
      auto body = [&](Fr* N, const Fr* C, unsigned so, int r0 = 99) {
#pragma unroll
        for (int n = 0; n < 16; ++n) {
          const int e = n >> 2, f = (n >> 1) & 1, t = n & 1;
          acc[f][t] = MFMA32(C[e].v[f], C[e].v[2 + t], acc[f][t]);
          if (n < 4) { rd(N, 2 * n, so); rd(N, 2 * n + 1, so); } else if (n < 12) rd(N, n + 4, so);
          if (n >= 4 && r0 + n - 4 < 14) router_step(r0 + n - 4);
          FENCE();
        }
        colsum(C, 0); colsum(C, 1);
        FENCE();
      };
      if (CODE_KB) WAITV(8); else WAITV(7);
      BAR(); ST(1);
#pragma unroll
      for (int n = 0; n < 16; ++n) rd(FA, n, 0);
      router_dots();
      BAR();
#pragma unroll
      for (int s = 1; s <= 7; s += 2) { FST(2 * s); body(FB, FA, (s & 3) * 32768, s == 1 ? 0 : 99); FST(2 * s + 1); BAR(); FST(2 * s + 2); body(FA, FB, ((s + 1) & 3) * 32768, s == 1 ? 12 : 99); FST(2 * s + 3); BAR(); }
      FST(18);
      body(FB, FA, 32768);
      colsum(FB, 0); colsum(FB, 1);
      if ((lane & 15) < 2) { const unsigned d = lds0 + L_CS + w * 256 + ((lane & 15) * 16 + 4 * g4) * 4; *(LAS f32x4*)d = cs0; *(LAS f32x4*)(d + 128) = cs1; }
      FST(23); BAR(); FST(19);
      const unsigned ca = lds0 + L_CS + fh * 512 + l31 * 4;
      const float c0 = *(const LAS float*)ca + *(const LAS float*)(ca + 256), c1 = *(const LAS float*)(ca + 128) + *(const LAS float*)(ca + 384);
      const f32x2 st0 = *(const LAS f32x2*)(lds0 + L_STAT + (64 * th + l31) * 8), st1 = *(const LAS f32x2*)(lds0 + L_STAT + (64 * th + 32 + l31) * 8);
      const float qs = ct < 5 ? 0.22360679774997896f * 1.4426950408889634f : 1.f;
      const unsigned qo = WS_QKV + (4 * tr + 2 * th) * 122880 + (16 * ct + 8 * fh + h) * 512 + l31 * 16;
      bf16x8 ax[2], bx[2]; float rr[2]; unsigned pw[8];
      auto split3 = [&](float c) {
        const float hi = rb(c), r1 = c - hi, mid = rb(r1);
        return __builtin_bit_cast(bf16x8, (u32x4){h ? 0u : pk(hi, mid), h ? 0u : pk(r1 - mid, 0.f), 0u, 0u});
      };
      auto epi = [&](int tile, int part) {
        const int f = tile & 1, t = tile >> 1;
#pragma unroll
        for (int g = 2 * part; g < 2 * part + 2; ++g) {
          float v0 = acc[f][t][2 * g] * rr[t]; asm("" : "+v"(v0));
          pw[g] = pk(v0, acc[f][t][2 * g + 1] * rr[t]);
        }
        if (part & 1) {
          const int g = part >> 1;
          auto r0 = __builtin_amdgcn_permlane32_swap(pw[4 * g], pw[4 * g + 2], false, false);
          auto r1 = __builtin_amdgcn_permlane32_swap(pw[4 * g + 1], pw[4 * g + 3], false, false);
          __builtin_amdgcn_raw_buffer_store_b128((u32x4){r0[0], r1[0], r0[1], r1[1]}, rs, qo + t * 122880 + (4 * f + 2 * g) * 512, 0, 16);
        }
      };
#pragma unroll
      for (int n = 0; n < 20; ++n) {
        const int tile = n < 8 ? n >> 2 : n < 10 ? n - 8 : n < 15 ? 2 : 3, e = n < 8 ? n & 3 : n < 10 ? 4 : (n - 10) % 5, f = tile & 1, t = tile >> 1;
        acc[f][t] = e < 4 ? MFMA32(FB[e].v[f], FB[e].v[2 + t], acc[f][t]) : MFMA32(ax[f], bx[t], acc[f][t]);
        if (n == 4) { ax[0] = split3(c0); const unsigned m = pk(-st0[0], -st0[0]); bx[0] = __builtin_bit_cast(bf16x8, (u32x4){m, m, 0u, 0u}); rr[0] = st0[1] * qs; }
        if (n == 5) { ax[1] = split3(c1); const unsigned m = pk(-st1[0], -st1[0]); bx[1] = __builtin_bit_cast(bf16x8, (u32x4){m, m, 0u, 0u}); rr[1] = st1[1] * qs; }
        if (n == 4 || n == 5) asm volatile("" : "+v"(ax[n - 4]), "+v"(bx[n - 4]), "+v"(rr[n - 4]));
        if (n >= 10) epi((n - 10) >> 2, (n - 10) & 3);
        FENCE();
        if (n == 9 || n == 13 || n == 17) FST(24 + (n - 9) / 4);
      }
      ST(2); FST(20);
      epi(2, 2); epi(2, 3);
#pragma unroll
      for (int part = 0; part < 4; ++part) epi(3, part);
      FST(21);
      WAITV(0);
      if (lane == 0 && __hip_atomic_fetch_add((LAS unsigned*)(lds0 + L_CNT), 1u, __ATOMIC_RELAXED, __HIP_MEMORY_SCOPE_WORKGROUP) == 3u)
        stg64(ws + WS_FLAG1 + (tr * 16 + ct) * 256, gran(tag, ~tag));
      ST(3); FST(22);
    } else {
      const int j = w - 4;
      auto issue = [&](int slot) { pieces(2 * j, slot); pieces(2 * j + 1, slot); sn = sn == 9 ? 0 : sn + 1; };
      issue(1); FST(29);
      if (tid == 384) *(LAS u32x4*)(lds0 + L_CNT) = (u32x4){};
      const unsigned au = tr * 15 + ct, cg = (au * 205) >> 11, kg = au - cg * 10;
      WAITV(8); FST(31); BAR();
#pragma unroll 1
      for (int s = 2; s < 2 + LAMODE; ++s) { issue(s & 3); WAITV(8); BAR(); }
      issue((2 + LAMODE) & 3);
#pragma unroll 1
      for (int s = 3 + LAMODE; s < 10; ++s) { FST(3 * s - 8); issue(s & 3); FST(3 * s - 7); WAITV(16); FST(3 * s - 6); BAR(); }
      lds_tag();
      if (w == 6) {
        const char* ga = ws + WS_STATS + tr * 1024;
        u32x2 g0, g1;
        asm volatile("global_load_dwordx2 %0, %2, %3 sc1\n\tglobal_load_dwordx2 %1, %2, %3 offset:512 sc1\n\t"
                     "s_waitcnt vmcnt(10)\n\ts_barrier\n\ts_waitcnt vmcnt(2)\n\ts_barrier\n\ts_waitcnt vmcnt(0)" : "=&v"(g0), "=&v"(g1) : "v"(lane * 8), "s"(ga) : "memory");
#pragma unroll 1
        for (unsigned sp = 0; sp < SPIN_CAP && !__all(g0[1] == tag && g1[1] == tag); ++sp) {
          g0 = __builtin_bit_cast(u32x2, ldg64(ga + lane * 8)); g1 = __builtin_bit_cast(u32x2, ldg64(ga + 512 + lane * 8)); }
        *(LAS f32x2*)(lds0 + L_STAT + lane * 8) = (f32x2){blo(g0[0]), __builtin_amdgcn_rcpf(bhi(g0[0]))};
        *(LAS f32x2*)(lds0 + L_STAT + 512 + lane * 8) = (f32x2){blo(g1[0]), __builtin_amdgcn_rcpf(bhi(g1[0]))};
        FST(23); BAR();
      } else if (w < 6) {
        unsigned pf = 0;
        const unsigned po = ((i >> 3) * 160 + j * 96 + (lane >> 1)) * 1280 + (lane & 1) * 128;
        const char* pb = (const char*)p.wo + ((cs80 * 160) & ~127);
        asm volatile("s_waitcnt vmcnt(8)\n\ts_barrier\n\tglobal_load_dword %0, %1, %2" : "+v"(pf) : "v"(po), "s"(pb) : "memory");
        asm volatile("global_load_dword %0, %1, %2" : "+v"(pf) : "v"(po + 32 * 1280), "s"(pb) : "memory");
        if (j == 0) asm volatile("global_load_dword %0, %1, %2\n\ts_waitcnt vmcnt(3)" : "+v"(pf) : "v"(po + 64 * 1280), "s"(pb) : "memory");
        else WAITV(2);
        BAR(); BAR();
        asm volatile("s_waitcnt vmcnt(0)" : "+v"(pf) :: "memory");
      }
      else {
        const unsigned fo = (lane >> 3) * 3072 + (lane & 7) * 16, fb = kg * 64 * 3072 + cg * 128;
        WAITV(8); BAR();
#pragma unroll
        for (int e = 0; e < 8; ++e) glds(fo, (const char*)p.wg + fb + e * 8 * 3072, lds0 + L_FFNA + e * 1024);
        WAITV(8); BAR();
#pragma unroll
        for (int e = 0; e < 8; ++e) glds(fo, (const char*)p.wu + fb + e * 8 * 3072, lds0 + L_FFNA + 8192 + e * 1024);
        FST(23); BAR();
        u64 tg = 0;
#pragma unroll 1
        for (unsigned sp = 0; sp < SPIN_CAP; ++sp) { tg = ldg64(ws + WS_TOP + xc * 256 + (lane & 1) * 8); if (__all((unsigned)(tg >> 32) == tag)) break; }
        if (lane < 2) *(LAS unsigned*)(lds0 + L_TOP + lane * 4) = (unsigned)tg;
        u32x4 xc0 = {ONE2, ONE2, ONE2, ONE2}, xc1 = xc0;
        if ((lane & 15) < 2) { const char* xr = (const char*)p.x + ((2047u - (unsigned)tg) & 2047u) * 1280 + kg * 128 + g4 * 16; xc0 = *(const GAS u32x4*)xr; xc1 = *(const GAS u32x4*)(xr + 64); }
        bf16x8 af[8][2];
#pragma unroll
        for (int n = 0; n < 8; ++n) { const unsigned a = lds0 + L_FFNA + (n >> 2) * 8192 + (8 * g4 + q4) * 128 + (n & 3) * 32 + 8 * p4; af[n][0] = trfrag(a, a + 512); af[n][1] = trfrag(a + 4096, a + 4608); }
#pragma unroll
        for (int n = 0; n < 8; ++n) {
          const f32x4 y = MFMA16(af[n][1], __builtin_bit_cast(bf16x8, xc1), MFMA16(af[n][0], __builtin_bit_cast(bf16x8, xc0), (f32x4){}));
          if ((lane & 15) < 3) *(LAS f32x4*)(lds0 + L_TMP + (((n >> 2) * 3 + (lane & 15)) * 64 + 16 * (n & 3) + 4 * g4) * 4) = y;
        }
#pragma unroll
        for (int n = 0; n < 6; ++n) stg64(ws + WS_PARTA + (au * 384 + n * 64 + lane) * 8, gran(*(const LAS unsigned*)(lds0 + L_TMP + (n * 64 + lane) * 4), tag));
      }
    }
  } else {
    const int sp_ = 2 * xc + i;
    if (w < 4) {
      router_load(); router_dots();
#pragma unroll
      for (int k = 0; k < 14; ++k) router_step(k);
    }
#pragma unroll 1
    for (int e = 0; e < 15; ++e) {
      const unsigned L = 64 * (8 * e + w) + lane, r = L / 80, pc = L - r * 80;
      glds(r * 1280 + (pc ^ (((r & 3) | ((r >> 1) & 4)) << 1)) * 16, (const char*)p.wd + sp_ * 96 * 1280, lds0 + (8 * e + w) * 1024);
    }
    if (tid == 384) *(LAS u32x4*)(lds0 + L_CNT) = (u32x4){};
    BAR();
    if (w >= 4) lds_tag();
    ST(2);
    if (w == 7) {
      unsigned k1 = 0, k2 = 0;
      if (i == 0) {
        __builtin_amdgcn_s_sleep(16);
#pragma unroll 1
        for (unsigned sp = 0; sp < SPIN_CAP; ++sp) { if (cand_top2(ws, lane, tag, k1, k2)) break; __builtin_amdgcn_s_sleep(4); }
        if (lane < 2) stg64(ws + WS_TOP + xc * 256 + lane * 8, gran(lane ? k2 : k1, tag));
        if (b == 0 && lane == 0) __hip_atomic_store((GAS unsigned*)(ws + WS_GEN), tag, __ATOMIC_RELAXED, __HIP_MEMORY_SCOPE_AGENT);
      } else {
        __builtin_amdgcn_s_sleep(32);
#pragma unroll 1
        for (unsigned sp = 0; sp < SPIN_CAP; ++sp) {
          const u64 v = ldg64(ws + WS_TOP + xc * 256 + (lane & 1) * 8);
          k1 = __builtin_amdgcn_readlane((unsigned)v, 0); k2 = __builtin_amdgcn_readlane((unsigned)v, 1);
          if (__all((unsigned)(v >> 32) == tag)) break;
          __builtin_amdgcn_s_sleep(4);
        }
      }
      if (lane == 0) *(LAS u32x2*)(lds0 + L_TOP) = (u32x2){k1, k2};
    }
  }
  ST(4); FST(35);
  const u64 ftag = gran(tag, ~tag);

  const int tg3 = w & 3, kh = w >> 2, tok0 = 256 * xc + 64 * (i >> 3), n0 = 80 * cs80;
  const bool two = w == 4 || w == 5;
  const unsigned it0 = tid, it1 = two ? tid + 256 : tid, tk0 = (it0 * 6554) >> 16, tk1 = (it1 * 6554) >> 16, ch0 = it0 - 10 * tk0, ch1 = it1 - 10 * tk1;
  const unsigned eo0 = (tok0 + tk0) * 1280 + n0 * 2 + ch0 * 16, eo1 = (tok0 + tk1) * 1280 + n0 * 2 + ch1 * 16;
  u32x4 xv0, xv1; u64 sg0_, sg1_;
  auto pre3 = [&]() {
    xv0 = *(const GAS u32x4*)((const char*)p.x + eo0); sg0_ = ldg64(ws + WS_STATS + (tok0 + tk0) * 8);
    xv1 = *(const GAS u32x4*)((const char*)p.x + eo1); sg1_ = ldg64(ws + WS_STATS + (tok0 + tk1) * 8);
  };

  {
    const int R = i >> 2, hd = 4 * xc + (i & 3), p0 = (5 * hd) >> 1, oh = hd & 1;
    const int pf = p0 + oh, pp = oh ? p0 : p0 + 2, cc = (lane & 1 ? p0 + 2 : p0) >> 4;
    if (w >= 4) {
      __builtin_amdgcn_s_setprio(2);
      const int j = w - 4;
      const WoOff woo = wo_offsets(lane);
      if (WO1 && !spare) {
#pragma unroll 1
        for (unsigned sp = 0; sp < SPIN_CAP; ++sp) { if (__builtin_amdgcn_readfirstlane(*(volatile LAS unsigned*)(lds0 + L_CNT)) >= 4u) break; __builtin_amdgcn_s_sleep(1); }
        wo_steps(WO1 * j, WO1 * j + WO1, woo, p.wo, cs80, lds0);
      }
      const int kt[4] = {6 + j, j < 2 ? 4 + j : 8 + j, j, 12 + j};
      { const int e = lane >> 2, T = 8 * R - 8 + (e == 0 ? kt[0] : e == 1 ? kt[1] : e == 2 ? kt[2] : kt[3]);
        const bool act = lane < 16 && T >= 0;
        poll(ws + WS_FLAG1 + (act ? ((T >> 2) * 16 + (lane & 2 ? 10 : 5) + cc) * 256 : 0), act, ftag); }
      ST(5); FST(36);
      const unsigned so0 = (80 + pf + h) * 512 + l31 * 16, so1 = (h ? (160 + pf) * 512 : (80 + pp) * 512 + 8 * oh) + l31 * 16, so2 = (h ? (160 + pp) * 512 + 8 * oh : (161 + pf) * 512) + l31 * 16;
      struct T3 { u32x4 a, b, c; };
      auto fetch = [&](int t) {
        const int T = 8 * R - 8 + t; const unsigned m = T < 0 ? 0x40000000u : 0u, o = WS_QKV + max(T, 0) * 122880;
        return T3{__builtin_amdgcn_raw_buffer_load_b128(rs, so0 | m, o, 16), __builtin_amdgcn_raw_buffer_load_b128(rs, so1 | m, o, 16), __builtin_amdgcn_raw_buffer_load_b128(rs, so2 | m, o, 16)};
      };
      auto stage = [&](int t, T3 v) {
        const unsigned kd = lds0 + L_KV + (R == 0 && t < 8 ? 7 : t) * KVS + l31 * 16;
        if (h) v.c[2] = v.c[3] = ONE2;
        *(LAS u32x4*)(kd + h * 512) = v.a; *(LAS u32x4*)(kd + 1024 + h * 512) = v.b; *(LAS u32x4*)(kd + L_V + (1 + h) * VPS) = v.c;
      };
      const T3 t0 = fetch(kt[0]), t1 = fetch(kt[1]);
      FENCE();
      stage(kt[0], t0);
      FENCE();
      const T3 t2 = fetch(kt[2]), t3 = fetch(kt[3]);
      FENCE();
      BAR(); ST(6); FST(39);
      stage(kt[1], t1);
      BAR(); FST(40);
      stage(kt[2], t2); stage(kt[3], t3);
      BAR(); FST(41);
      pre3();
      if (spare) {
        const int sp_ = 2 * xc + i;
        const u32x2 kk = *(const LAS u32x2*)(lds0 + L_TOP);
        const char* sa0 = ws + WS_STATS + ((2047u - kk[0]) & 2047u) * 8; const char* sa1 = ws + WS_STATS + ((2047u - kk[1]) & 2047u) * 8;
        const char* ga[3]; float sum[3]; u64 sg0, sg1;
#pragma unroll
        for (int r = 0; r < 3; ++r) { const unsigned e = min(144 * j + 64 * r + lane, 575), rc = e / 96, F = 96 * sp_ + e - 96 * rc; ga[r] = ws + WS_PARTA + ((F >> 6) * 3840 + rc * 64 + (F & 63)) * 8; }
#pragma unroll 1
        for (unsigned sp = 0; sp < SPIN_CAP; ++sp) {
          sg0 = ldg64(sa0); sg1 = ldg64(sa1);
          bool ok = (unsigned)(sg0 >> 32) == tag && (unsigned)(sg1 >> 32) == tag;
#pragma unroll
          for (int r = 0; r < 3; ++r) { sum[r] = 0.f;
#pragma unroll
            for (int kg = 0; kg < 10; ++kg) { const u64 u = ldg64(ga[r] + kg * 3072); ok &= (unsigned)(u >> 32) == tag; sum[r] += __uint_as_float((unsigned)u); } }
          if (__all(ok)) break;
        }
#pragma unroll
        for (int r = 0; r < 3; ++r) if (64 * r + lane < 144) *(LAS float*)(lds0 + L_TMP + (144 * j + 64 * r + lane) * 4) = sum[r];
        lds_meet(lds0 + L_CNT + 8, 4, lane);
        const unsigned hb = lds0 + L_TMP + 2304 + j * 384;
#pragma unroll
        for (int r = 0; r < 3; ++r) {
          const int e = 64 * r + lane, tk = e >= 96; const unsigned a = lds0 + L_TMP + (e - 96 * tk) * 4, sg = (unsigned)(tk ? sg1 : sg0);
          const float m = blo(sg), rd = __builtin_amdgcn_rcpf(bhi(sg));
          const float g = rb((*(const LAS float*)(a + tk * 384) - m * *(const LAS float*)(a + 768)) * rd);
          const float u = rb((*(const LAS float*)(a + 1152 + tk * 384) - m * *(const LAS float*)(a + 1920)) * rd);
          *(LAS u16*)(hb + e * 2) = (u16)pk(rb(g * __builtin_amdgcn_rcpf(1.f + __expf(-g))) * u, 0.f);
        }
        const unsigned yb = lds0 + L_HB + j * 1280;
#pragma unroll 1
        for (int n2 = 0; n2 < 2; ++n2) {
          const unsigned da = lds0 + (8 * g4 + q4) * 1280 + (((2 * j + n2) ^ (q4 | ((g4 & 1) << 2))) * 32) + 8 * p4;
          f32x4 y[5] = {};
#pragma unroll
          for (int s = 0; s < 3; ++s) {
            const bf16x8 hf = lds128(hb + (lane & 1) * 192 + g4 * 16 + s * 64);
#pragma unroll
            for (int n = 0; n < 5; ++n) y[n] = MFMA16(trfrag(da + s * 40960 + n * 256, da + s * 40960 + n * 256 + 5120), hf, y[n]);
          }
          if ((lane & 15) < 2) {
#pragma unroll
            for (int n = 0; n < 5; ++n) *(LAS f32x4*)(yb + ((lane & 15) * 160 + 32 * n + 16 * n2 + 4 * g4) * 4) = y[n];
          }
        }
#pragma unroll
        for (int n = 0; n < 5; ++n) {
          const int e = 64 * n + lane, tk = e >= 160, r = e - 160 * tk;
          stg64(ws + WS_YPART + (sp_ * 1280 + tk * 640 + 128 * (r >> 5) + 32 * j + (r & 31)) * 8, gran(*(const LAS unsigned*)(yb + e * 4), tag));
        }
        lds_meet(lds0 + L_CNT + 8, 8, lane);
        ST(3);
      }
      if (spare) wo_steps(5 * j, 5 * j + 5, woo, p.wo, cs80, lds0); else wo_steps(4 * WO1 + (5 - WO1) * j, 4 * WO1 + (5 - WO1) * (j + 1), woo, p.wo, cs80, lds0);
      FST(42);
      WAITV(0);
      FST(43);
      __builtin_amdgcn_s_setprio(0);
    } else {
      const int lo = w < 2, tu = 2 * w + 1 - lo, tv = 2 * w + lo;
      poll(ws + WS_FLAG1 + ((2 * R + (w >> 1)) * 16 + cc) * 256, lane < 2, ftag);
      ST(5); FST(36);
      bf16x8 qf[2][2];
      { const unsigned a0 = (pf + h) * 512 + l31 * 16, a1 = pp * 512 + l31 * 16 + oh * 8, ou = WS_QKV + (8 * R + tu) * 122880, ov = WS_QKV + (8 * R + tv) * 122880;
        const u32x4 u0 = __builtin_amdgcn_raw_buffer_load_b128(rs, a0, ou, 16), v0 = __builtin_amdgcn_raw_buffer_load_b128(rs, a0, ov, 16);
        const u32x2 u1 = __builtin_amdgcn_raw_buffer_load_b64(rs, a1, ou, 16), v1 = __builtin_amdgcn_raw_buffer_load_b64(rs, a1, ov, 16);
        qf[0][0] = __builtin_bit_cast(bf16x8, u0); qf[0][1] = __builtin_bit_cast(bf16x8, (u32x4){h ? 0u : u1[0], h ? 0u : u1[1], 0u, 0u});
        qf[1][0] = __builtin_bit_cast(bf16x8, v0); qf[1][1] = __builtin_bit_cast(bf16x8, (u32x4){h ? 0u : v1[0], h ? 0u : v1[1], 0u, 0u}); }
      f32x16 cz = {}, ca, cb;
#pragma unroll
      for (int r = 0; r < 16; ++r) {
        const bool le = (r & 3) + 8 * (r >> 2) + 4 * h <= l31;
        const float d = le ? 0.f : -1e30f, f = le || R == 0 ? -1e30f : 0.f;
        ca[r] = lo ? d : f; cb[r] = lo ? f : d;
      }
      const unsigned ka = lds0 + L_KV + h * 512 + l31 * 16;
      const int pi = min(4 * (g4 & 1) + p4, 5);
      const unsigned va = lds0 + L_KV + L_V + (pi >> 1) * VPS + (pi & 1) * 8 + (4 * h + q4) * 16;
      struct KF { bf16x8 a, b; };
      auto kread = [&](int kt) {
        const unsigned a = ka + kt * KVS;
        return KF{lds128(a), lds128(a + 1024)};
      };
      auto vread = [&](int kt) { const unsigned v = va + kt * KVS; return KF{trfrag(v, v + 128), trfrag(v + 256, v + 384)}; };
      auto ex = [&](const f32x16& s, int e) { return pk(__builtin_amdgcn_exp2f(s[2 * e]), __builtin_amdgcn_exp2f(s[2 * e + 1])); };
      const int P0 = lo ? 6 : 8, P1 = w == 0 ? 4 : w == 1 ? 8 : w == 2 ? 6 : 10, P2 = lo ? 2 + 2 * w : 6 + 2 * w;
      const int tp[10] = {P0, P0 + 1, P1, P1 + 1, lo ? 2 * w + 8 : 2 * w + 1, lo ? 2 * w + 9 : 2 * w, P2, P2 + 1, lo ? 2 * w + 1 : 2 * w + 8, lo ? 2 * w : 2 * w + 9};
      auto at = [&](int ps) { const int e = (ps == 1 || ps == 3 || ps == 7), t = tp[ps - e]; return (R == 0 && t < 8 ? 8 : t) + e; };
      auto setz = [&](int ps) { if (R == 0) { const float z = tp[ps] < 8 ? -1e30f : 0.f;
#pragma unroll
          for (int r = 0; r < 16; ++r) cz[r] = z; } };
      setz(0);
      asm volatile("" : "+v"(ca), "+v"(cb), "+v"(cz), "+v"(qf[0][0]), "+v"(qf[0][1]), "+v"(qf[1][0]), "+v"(qf[1][1]));
      BAR(); ST(6); FST(39);
      KF K0, K1, K2, V0, V1, V2; f32x16 S0, S1, o[2] = {};
      K0 = kread(at(0)); V0 = vread(at(0));
      S0 = MFMA32(K0.b, qf[0][1], MFMA32(K0.a, qf[0][0], cz));
      u32x4 pa = {}, pb = {}; float x0, x1, x2, x3;
#define SET3(N, p) ((p) % 3 == 0 ? N##0 : (p) % 3 == 1 ? N##1 : N##2)
#define EXP4(s, e) do { x0 = __builtin_amdgcn_exp2f(s[4 * e]); x1 = __builtin_amdgcn_exp2f(s[4 * e + 1]); x2 = __builtin_amdgcn_exp2f(s[4 * e + 2]); x3 = __builtin_amdgcn_exp2f(s[4 * e + 3]); \
                        asm volatile("" : "+v"(x0), "+v"(x1), "+v"(x2), "+v"(x3)); FENCE(); } while (0)
#define PACK2(d, e) do { d[e] = pk(x0, x1); d[e + 1] = pk(x2, x3); asm volatile("" : "+v"(d)); FENCE(); } while (0)
      auto unit = [&](auto U, f32x16& sc, f32x16& sn) {
        constexpr int u = decltype(U)::value, np = u + 1 < 18 ? UP[u + 1] : 0, nq = u + 1 < 18 ? UQ[u + 1] : 0, pu = u > 0 ? u - 1 : 0;
        if constexpr (u == 2 || u == BC_AT) { BAR(); if constexpr (u == BC_AT) pre3(); }
        FST(40 + u);
        if constexpr (u + 1 < 18) { if constexpr (np != UP[u]) setz(np); sn = MFMA32(SET3(K, np).a, qf[nq][0], u + 1 == 8 || u + 1 == 10 ? ca : u + 1 >= 16 ? cb : cz); FENCE(); }
        if constexpr (u > 0) PACK2(pb, 2);
        EXP4(sc, 0);
        if constexpr (u > 0) { o[UQ[pu]] = MFMA32(SET3(V, UP[pu]).b, __builtin_bit_cast(bf16x8, pb), o[UQ[pu]]); FENCE(); }
        if constexpr (u + 2 < 18 && UP[u + 2] != UP[u + 1]) SET3(K, UP[u + 2]) = kread(at(UP[u + 2]));
        if constexpr (u + 1 < 18 && np != UP[u]) SET3(V, np) = vread(at(np));
        PACK2(pa, 0);
        EXP4(sc, 1);
        if constexpr (u + 1 < 18) { sn = MFMA32(SET3(K, np).b, qf[nq][1], sn); FENCE(); }
        PACK2(pa, 2);
        EXP4(sc, 2);
        o[UQ[u]] = MFMA32(SET3(V, UP[u]).a, __builtin_bit_cast(bf16x8, pa), o[UQ[u]]); FENCE();
        PACK2(pb, 0);
        EXP4(sc, 3);
      };
#define UNIT2(n) unit(IC<n>{}, S0, S1); unit(IC<n + 1>{}, S1, S0);
      UNIT2(0) UNIT2(2) UNIT2(4) UNIT2(6) UNIT2(8) UNIT2(10) UNIT2(12) UNIT2(14) UNIT2(16)
      PACK2(pb, 2);
      o[0] = MFMA32(V0.b, __builtin_bit_cast(bf16x8, pb), o[0]);
      auto rowsum = [&](const f32x16& a) { auto r = __builtin_amdgcn_permlane32_swap(__float_as_uint(a[8]), __float_as_uint(a[8]), false, false); return __uint_as_float(r[1]); };
      float l[2] = {rowsum(o[1]), rowsum(o[0])};
      if (FORCE_FB || !__all(l[0] > 0x1p-60f && l[0] < 0x1p60f && l[1] > 0x1p-60f && l[1] < 0x1p60f)) {
        int qt = tu;
#pragma unroll 1
        for (int e = 0; e < 2; ++e) {
          const int k0 = R > 0 ? qt : 8;
          auto score = [&](int kt, float base) {
            f32x16 c;
#pragma unroll
            for (int r = 0; r < 16; ++r) { const bool le = (r & 3) + 8 * (r >> 2) + 4 * h <= l31; c[r] = (kt == qt + 8 && !le) || (kt == qt && le) ? -1e30f : base; }
            const KF k = kread(kt);
            return MFMA32(k.b, qf[0][1], MFMA32(k.a, qf[0][0], c));
          };
          float m = -3e38f;
#pragma unroll 1
          for (int kt = k0; kt <= qt + 8; ++kt) { const f32x16 s = score(kt, 0.f);
#pragma unroll
            for (int r = 0; r < 16; ++r) m = fmaxf(m, s[r]); }
          { auto r = __builtin_amdgcn_permlane32_swap(__float_as_uint(m), __float_as_uint(m), false, false); m = fmaxf(__uint_as_float(r[0]), __uint_as_float(r[1])); }
          o[0] = (f32x16){};
#pragma unroll 1
          for (int kt = k0; kt <= qt + 8; ++kt) {
            const f32x16 s = score(kt, -m); const KF v = vread(kt);
            o[0] = MFMA32(v.a, __builtin_bit_cast(bf16x8, (u32x4){ex(s, 0), ex(s, 1), ex(s, 2), ex(s, 3)}), o[0]);
            o[0] = MFMA32(v.b, __builtin_bit_cast(bf16x8, (u32x4){ex(s, 4), ex(s, 5), ex(s, 6), ex(s, 7)}), o[0]);
          }
          const f32x16 to = o[0]; o[0] = o[1]; o[1] = to;
          const bf16x8 t0 = qf[0][0], t1 = qf[0][1]; qf[0][0] = qf[1][0]; qf[0][1] = qf[1][1]; qf[1][0] = t0; qf[1][1] = t1;
          qt = tv;
        }
        l[0] = rowsum(o[1]); l[1] = rowsum(o[0]);
      }
      ST(7); FST(59);
#pragma unroll
      for (int e = 0; e < 2; ++e) {
        const f32x16& a = o[1 - e]; const float ri = __builtin_amdgcn_rcpf(l[e]);
        const unsigned e0 = pk(a[0] * ri, a[1] * ri), e1 = pk(a[2] * ri, a[3] * ri), e2 = pk(a[4] * ri, a[5] * ri), e3 = pk(a[6] * ri, a[7] * ri);
        const u32x2 el = {pk(a[8] * ri, a[9] * ri), pk(a[10] * ri, a[11] * ri)};
        auto r0 = __builtin_amdgcn_permlane32_swap(e0, e2, false, false), r1 = __builtin_amdgcn_permlane32_swap(e1, e3, false, false);
        const unsigned ob = WS_O + (16 * R + 2 * (e ? tu : tv) + (l31 >> 4)) * 20480;
        __builtin_amdgcn_raw_buffer_store_b128((u32x4){r0[0], r1[0], r0[1], r1[1]}, rs, ob + (2 * hd + h) * 256 + (lane & 15) * 16, 0, 16);
        if (h == 0) __builtin_amdgcn_raw_buffer_store_b64(el, rs, ob + 16384 + hd * 128 + (lane & 15) * 8, 0, 16);
      }
      WAITV(0);
      if (lane == 0 && __hip_atomic_fetch_add((LAS unsigned*)(lds0 + L_CNT + 4), 1u, __ATOMIC_RELAXED, __HIP_MEMORY_SCOPE_WORKGROUP) == 3u)
        stg64(ws + WS_FLAG2 + (R * 32 + hd) * 256, ftag);
      ST(8); FST(60);
    }
    BAR();
  }

  {
    const int tg = tg3;
    const unsigned wa = lds0 + L_WO + (kh * 320 + 4 * g4 + q4) * 160 + 8 * p4;
    constexpr int NPRE = 6;
    bf16x8 wf[NPRE][5];
#pragma unroll
    for (int s = 0; s < NPRE; ++s)
#pragma unroll
      for (int n = 0; n < 5; ++n) wf[s][n] = trfrag(wa + s * 5120 + n * 32, wa + s * 5120 + 2560 + n * 32);
#pragma unroll 1
    for (unsigned sp = 0; sp < SPIN_CAP && !__all((unsigned)(sg0_ >> 32) == tag && (unsigned)(sg1_ >> 32) == tag); ++sp) {
      sg0_ = ldg64(ws + WS_STATS + (tok0 + tk0) * 8); sg1_ = ldg64(ws + WS_STATS + (tok0 + tk1) * 8); }
    f32x4 base[4];
#pragma unroll
    for (int e = 0; e < 4; ++e) {
      const unsigned sg = (unsigned)(e < 2 ? sg0_ : sg1_); const float m = blo(sg), rd = __builtin_amdgcn_rcpf(bhi(sg));
#pragma unroll
      for (int c = 0; c < 4; ++c) { const unsigned u = (e < 2 ? xv0 : xv1)[2 * (e & 1) + (c >> 1)]; const float v = c & 1 ? bhi(u) : blo(u); base[e][c] = v + (v - m) * rd; }
    }
    const u32x2 kk12 = *(const LAS u32x2*)(lds0 + L_TOP);
    const unsigned k1 = __builtin_amdgcn_readfirstlane(kk12[0]), k2 = __builtin_amdgcn_readfirstlane(kk12[1]);
    const unsigned i1 = 2047u - (k1 & 0xffffu) - tok0, i2 = 2047u - (k2 & 0xffffu) - tok0;
    if ((i1 < 64u || i2 < 64u) && w < 3) {
      const int c = tid >= 80, col = tid - 80 * c;
      const char* ya = ws + WS_YPART + (c * 640 + n0 + (tid < 160 ? col : 0)) * 8;
      float s = 0.f;
#pragma unroll 1
      for (unsigned sp = 0; sp < SPIN_CAP; ++sp) {
        bool ok = true; s = 0.f;
#pragma unroll 8
        for (int e = 0; e < 16; ++e) { u64 v = ldg64(ya + e * 10240); ok &= (unsigned)(v >> 32) == tag; s += __uint_as_float((unsigned)v); }
        if (__all(ok)) break;
      }
      const unsigned kk = (c ? k2 : k1) >> 16;
      const float r = blo(kk & 0x8000u ? kk & 0x7fffu : ~kk & 0xffffu);
      if (tid < 160) *(LAS float*)(lds0 + L_UPD + tid * 4) = rb(rb(s) * rb(__builtin_amdgcn_rcpf(1.f + __expf(-r))));
    }
#pragma unroll
    for (int s = 0; s < NPRE; ++s)
#pragma unroll
      for (int n = 0; n < 5; ++n) asm volatile("" : "+v"(wf[s][n]));
    asm volatile("" : "+v"(base[0]), "+v"(base[1]), "+v"(base[2]), "+v"(base[3]));
    poll(ws + WS_FLAG2 + (xc * 32 + 16 * kh + (lane & 15)) * 256, lane < 16, ftag);
    ST(9); FST(61);
    const unsigned ob = WS_O + (tok0 / 16 + tg) * 20480;
    u32x4 of[10];
    auto oload = [&](int s) {
      if (s < 8) of[s] = __builtin_amdgcn_raw_buffer_load_b128(rs, lane * 16 + s * 1024, ob + kh * 8192, 16);
      else {
        const unsigned a = (8 * (s - 8) + 2 * g4) * 128 + (lane & 15) * 8;
        const u32x2 u0 = __builtin_amdgcn_raw_buffer_load_b64(rs, a, ob + 16384 + kh * 2048, 16), u1 = __builtin_amdgcn_raw_buffer_load_b64(rs, a + 128, ob + 16384 + kh * 2048, 16);
        of[s] = (u32x4){u0[0], u0[1], u1[0], u1[1]};
      }
    };
#pragma unroll
    for (int s = 0; s < OPRE; ++s) oload(s);
    FENCE();
    FST(62);
    f32x4 acc[5] = {};
#pragma unroll
    for (int s = 0; s < 10; ++s) {
      if (s + OPRE < 10) oload(s + OPRE);
      FENCE();
#pragma unroll
      for (int n = 0; n < 5; ++n) acc[n] = MFMA16(wf[s % NPRE][n], __builtin_bit_cast(bf16x8, of[s]), acc[n]);
      if (s + NPRE < 10) {
#pragma unroll
        for (int n = 0; n < 5; ++n) wf[s][n] = trfrag(wa + (s + NPRE) * 5120 + n * 32, wa + (s + NPRE) * 5120 + 2560 + n * 32);
      }
      FENCE();
    }
    ST(10); FST(63);
#pragma unroll
    for (int n = 0; n < 5; ++n) *(LAS f32x4*)(lds0 + L_RED + kh * 21504 + (16 * tg + (lane & 15)) * 336 + (16 * n + 4 * g4) * 4) = acc[n];
    BAR();
    auto fin = [&](unsigned tk, unsigned ch, unsigned eo, f32x4 b0, f32x4 b1) {
      const unsigned a = lds0 + L_RED + tk * 336 + ch * 32;
      b0 += *(const LAS f32x4*)a + *(const LAS f32x4*)(a + 21504); b1 += *(const LAS f32x4*)(a + 16) + *(const LAS f32x4*)(a + 21504 + 16);
      if (tk == i1 || tk == i2) { const unsigned u = lds0 + L_UPD + (tk == i1 ? 0 : 320) + ch * 32; b0 += *(const LAS f32x4*)u; b1 += *(const LAS f32x4*)(u + 16); }
      *(GAS u32x4*)((char*)p.out + eo) = (u32x4){pk(b0[0], b0[1]), pk(b0[2], b0[3]), pk(b1[0], b1[1]), pk(b1[2], b1[3])};
    };
    fin(tk0, ch0, eo0, base[0], base[1]);
    if (two) fin(tk1, ch1, eo1, base[2], base[3]);
    ST(11);
  }
}

__global__ void k_first(unsigned long long* t) { if (threadIdx.x == 0) t[0] = __builtin_amdgcn_s_memrealtime(); }
#define R4(x) x x x x
#define R16(x) R4(R4(x))
#define R256(x) R16(R16(x))
__global__ __launch_bounds__(512) void k_test(unsigned long long* t, float* sink) {
  unsigned long long t0; asm volatile("s_memrealtime %0\n\ts_waitcnt lgkmcnt(0)" : "=s"(t0));
  __shared__ char sm[163840 ? 163840 : 16];
  if (0) { asm volatile("v_mov_b32 v250, 0" ::: "v250"); }
  sm[threadIdx.x] = 1;
  __syncthreads();
  if (threadIdx.x == 0) t[16 + blockIdx.x] = t0;
  if (sm[threadIdx.x ^ 1] == 7) { sink[0] = 1; if (0) { float a = 1.f; asm volatile(R16(R256("v_fma_f32 %0, %0, %0, %0\n")) : "+v"(a)); sink[1] = a; } }
}
__global__ void k_spin(const unsigned long long* t) {
  if (threadIdx.x == 0) {
    unsigned long long s = ~0ull;
    for (int i = 0; i < 256; ++i) s = t[16 + i] < s ? t[16 + i] : s;
    unsigned long long span = (s - t[0]) * 100; if (span > 3000000) span = 3000000;
    unsigned long long t0 = __builtin_amdgcn_s_memrealtime();
    while (__builtin_amdgcn_s_memrealtime() - t0 < span) __builtin_amdgcn_s_sleep(8);
  }
}
extern "C" void kernel_launch(void* const* d_in, const int* in_sizes, int n_in, void* d_out, int out_size, void* d_ws, size_t ws_size,
                              hipStream_t stream) {
  Params p;
  p.x = (const u16*)d_in[0]; p.wqkv = (const u16*)d_in[1]; p.wo = (const u16*)d_in[2]; p.wr = (const u16*)d_in[4];
  p.wg = (const u16*)d_in[5]; p.wu = (const u16*)d_in[6]; p.wd = (const u16*)d_in[7]; p.out = (u16*)d_out; p.ws = (char*)d_ws;
  fused_block<<<256, 512, 0, stream>>>(p);
  unsigned long long* t = (unsigned long long*)((char*)d_ws + 1024 * 1024 + 900000);
  k_first<<<1, 64, 0, stream>>>(t);
  k_test<<<256, 512, 0, stream>>>(t, (float*)((char*)d_ws + (20 << 20)));
  k_spin<<<1, 64, 0, stream>>>(t);
}
```

```cpp
#include <hip/hip_runtime.h>
#include <stdint.h>

typedef unsigned short u16;
typedef unsigned long long u64;
typedef short bf16x8 __attribute__((ext_vector_type(8)));
typedef short s16x4 __attribute__((ext_vector_type(4)));
typedef float f32x2 __attribute__((ext_vector_type(2)));
typedef float f32x4 __attribute__((ext_vector_type(4)));
typedef float f32x16 __attribute__((ext_vector_type(16)));
typedef unsigned u32x2 __attribute__((ext_vector_type(2)));
typedef unsigned u32x4 __attribute__((ext_vector_type(4)));
typedef __bf16 bf2 __attribute__((ext_vector_type(2)));
#define LAS __attribute__((address_space(3)))
#define GAS __attribute__((address_space(1)))
#define DI __device__ __forceinline__

constexpr unsigned WS_GEN = 0, WS_FLAG1 = 4096, WS_FLAG2 = 131072, WS_CAND = 262144, WS_STATS = 278528, WS_PARTA = 524288,
                   WS_YPART = 1572864, WS_QKV = 2097152, WS_O = 10485760, WS_END = 13631488, WS_STAMP = 16777216;
constexpr unsigned L_WO = 0, L_KV = 102400, L_FFNA = 65536;
constexpr unsigned KVS = 3264, L_V = 1536, VPS = 576;
constexpr unsigned L_RED = L_KV, L_UPD = L_KV + 43008;
constexpr unsigned L_KEY = 154624, L_TOP = L_KEY + 32, L_CNT = L_KEY + 48  , L_KEYS = L_KEY + 64, L_STAT = L_KEY + 96, L_CS = L_STAT + 1024, L_TMP = L_CS + 1024;
constexpr unsigned L_Y = L_KV + 44032;
constexpr unsigned SPIN_CAP = 1u << 20;
constexpr unsigned ONE2 = 0x3f803f80u;
#define CODE_KB 36

constexpr int UP[20] = {0, 0, 1, 1, 2, 2, 3, 3, 4, 4, 5, 6, 6, 7, 7, 8, 8, 9, 9, 9}, UQ[18] = {0, 1, 0, 1, 0, 1, 0, 1, 0, 1, 1, 0, 1, 0, 1, 0, 1, 0};
template <int N> struct IC { static constexpr int value = N; };

struct Params { const u16 *x, *wqkv, *wo, *wr, *wg, *wu, *wd; u16* out; char* ws; };

#define ST(i) do {} while (0)

#define FST(i) do {} while (0)
#define WAITV(n) asm volatile("s_waitcnt vmcnt(" #n ")" ::: "memory")
#define REQ0 3
#define LA 3
#define LAMODE 1
#define BC_AT 7
#define FORCE_FB 0
#define WO1 0
#define OPRE 4
#define TLBW 0
#define HPRIO2 1
#define ROT 3
#define PRIO 0
#define WAITN_(n) WAITV(n)
#define WAITN(n) WAITN_(n)
#define WO_PRE 6
#define BAR() do { __builtin_amdgcn_s_waitcnt(0xC07F); asm volatile("s_barrier" ::: "memory"); } while (0)
#define FENCE() __builtin_amdgcn_sched_barrier(0)
#define MFMA32(a, b, c) __builtin_amdgcn_mfma_f32_32x32x16_bf16((a), (b), (c), 0, 0, 0)
#define MFMA16(a, b, c) __builtin_amdgcn_mfma_f32_16x16x32_bf16((a), (b), (c), 0, 0, 0)

DI void glds(unsigned voff, const void* sbase, unsigned lds) {
  asm volatile("s_mov_b32 m0, %2\n\ts_nop 0\n\tglobal_load_lds_dwordx4 %0, %1" :: "v"(voff), "s"(sbase), "s"(lds) : "memory");
}
DI bf16x8 lds128(unsigned a) { return *(const LAS bf16x8*)a; }
DI s16x4 ldstr(unsigned a) { return __builtin_amdgcn_ds_read_tr16_b64_v4i16((LAS s16x4*)a); }
DI bf16x8 trfrag(unsigned a, unsigned a2) { s16x4 lo = ldstr(a), hi = ldstr(a2); return __builtin_shufflevector(lo, hi, 0, 1, 2, 3, 4, 5, 6, 7); }
DI unsigned pk(float a, float b) { f32x2 v = {a, b}; return __builtin_bit_cast(unsigned, __builtin_convertvector(v, bf2)); }
DI float rb(float a) { return __uint_as_float(pk(a, 0.f) << 16); }
DI float blo(unsigned v) { return __uint_as_float(v << 16); }
DI float bhi(unsigned v) { return __uint_as_float(v & 0xffff0000u); }
DI float dot2(unsigned a, unsigned b, float c) { return __builtin_amdgcn_fdot2_f32_bf16(__builtin_bit_cast(bf2, a), __builtin_bit_cast(bf2, b), c, false); }
DI float xhalf_sum(float v) { auto r = __builtin_amdgcn_permlane32_swap(__float_as_uint(v), __float_as_uint(v), false, false); return __uint_as_float(r[0]) + __uint_as_float(r[1]); }
DI u64 ldg64(const char* a) { return __hip_atomic_load((GAS u64*)a, __ATOMIC_RELAXED, __HIP_MEMORY_SCOPE_AGENT); }
DI void stg64(char* a, u64 v) { __hip_atomic_store((GAS u64*)a, v, __ATOMIC_RELAXED, __HIP_MEMORY_SCOPE_AGENT); }
DI u64 gran(unsigned v, unsigned tag) { return ((u64)tag << 32) | v; }
template <int CTRL> DI unsigned dpp(unsigned v) { return __builtin_amdgcn_update_dpp(0u, v, CTRL, 0xf, 0xf, true); }
DI unsigned wave_umax(unsigned v) {
  v = max(v, dpp<0x128>(v)); v = max(v, dpp<0x124>(v)); v = max(v, dpp<0x122>(v)); v = max(v, dpp<0x121>(v));
  auto r = __builtin_amdgcn_permlane16_swap(v, v, false, false); v = max(r[0], r[1]);
  auto q = __builtin_amdgcn_permlane32_swap(v, v, false, false); return max(q[0], q[1]);
}
DI float sum32(float v) {
  v += __uint_as_float(dpp<0x128>(__float_as_uint(v))); v += __uint_as_float(dpp<0x124>(__float_as_uint(v)));
  v += __uint_as_float(dpp<0x122>(__float_as_uint(v))); v += __uint_as_float(dpp<0x121>(__float_as_uint(v)));
  auto r = __builtin_amdgcn_permlane16_swap(__float_as_uint(v), __float_as_uint(v), false, false);
  return __uint_as_float(r[0]) + __uint_as_float(r[1]);
}
DI void ln_stats(float s1, float s2, float& mb, float& db) {
  float mean = s1 * (1.f / 640.f); float var = fmaxf(s2 * (1.f / 640.f) - mean * mean, 0.f);
  mb = rb(mean); db = rb(__builtin_amdgcn_sqrtf(rb(rb(var) + 1e-5f)));
}
DI void poll(const char* addr, bool act, u64 want) {
#pragma unroll 1
  for (unsigned sp = 0; sp < SPIN_CAP; ++sp) { u64 v = ldg64(addr); if (__all(!act || v == want)) break; __builtin_amdgcn_s_sleep(1); }
  asm volatile("" ::: "memory");
}
struct Keys { u32x4 g[4]; };
DI bool keys_top2(const Keys& k, unsigned tag, unsigned& k1, unsigned& k2) {
  unsigned bad = 0, a = 0, b = 0;
#pragma unroll
  for (int e = 0; e < 4; ++e) {
    bad |= (k.g[e][1] ^ tag) | (k.g[e][3] ^ tag);
    const unsigned hi = max(k.g[e][0], k.g[e][2]), lo = min(k.g[e][0], k.g[e][2]);
    b = max(max(b, lo), min(a, hi)); a = max(a, hi);
  }
  if (!__all(bad == 0)) return false;
  k1 = wave_umax(a); k2 = wave_umax(a == k1 ? b : a);
  k1 = __builtin_amdgcn_readfirstlane(k1); k2 = __builtin_amdgcn_readfirstlane(k2);
  return true;
}
template <class RS> DI void keys_wait(RS rs, int lane, unsigned tag, unsigned& k1, unsigned& k2) {
#pragma unroll 1
  for (unsigned sp = 0; sp < SPIN_CAP; ++sp) {
    Keys k;
    asm volatile("" ::: "memory");
#pragma unroll
    for (int e = 0; e < 4; ++e) k.g[e] = __builtin_amdgcn_raw_buffer_load_b128(rs, lane * 16 + e * 1024, WS_CAND, 16);
    if (keys_top2(k, tag, k1, k2)) break;
    __builtin_amdgcn_s_sleep(8);
  }
}
struct WoOff { unsigned r[5], l[5]; };
DI WoOff wo_offsets(int lane) {
  WoOff o;
#pragma unroll
  for (int m = 0; m < 5; ++m) {
    const unsigned L = 64 * m + lane, rho = (L * 205) >> 11, c = L - rho * 10, q = rho & 3, g = (rho >> 2) & 3, t = rho >> 4;
    o.r[m] = (24 * (g >> 1) + 8 * (g & 1) + 4 * t + q) * 1280 + c * 16; o.l[m] = (40 * g + 16 + 4 * t + q) * 1280 + c * 16;
    asm volatile("" : "+v"(o.r[m]), "+v"(o.l[m]));
  }
  return o;
}
DI void wo_steps(int s0, int s1, const WoOff& o, const u16* wo, int cs80, unsigned lds0) {
#pragma unroll 1
  for (int u = s0; u < s1; ++u) {
    const int kh = u >= 10, st = u - 10 * kh;
    const char* sp = (const char*)wo + cs80 * 160 + (kh * 320 + (st < 8 ? st * 40 : (st - 8) * 160)) * 1280;
    const unsigned d = lds0 + L_WO + u * 5120;
    if (st < 8) {
#pragma unroll
      for (int m = 0; m < 5; ++m) glds(o.r[m], sp, d + m * 1024);
    } else {
#pragma unroll
      for (int m = 0; m < 5; ++m) glds(o.l[m], sp, d + m * 1024);
    }
  }
}

__global__ __launch_bounds__(512, 2) void fused_block(Params p) {
  __shared__ __attribute__((aligned(1024))) char smem[163840];
  const unsigned lds0 = (unsigned)(size_t)smem;
  u64 pc0; asm volatile("s_getpc_b64 %0" : "=s"(pc0));
  const int tid = threadIdx.x, lane = tid & 63, w = __builtin_amdgcn_readfirstlane(tid >> 6);
  const int b = blockIdx.x, xc = b & 7, i = b >> 3;
  const bool spare = i < 2;
  const int xp = xc >> 1, odd = xc & 1;
  const int cs80 = i & 7;
  char* const ws = p.ws;
  const auto rs = __builtin_amdgcn_make_buffer_rsrc(ws, 0, WS_END, 0x00020000);
  const int h = lane >> 5, l31 = lane & 31, g4 = lane >> 4, q4 = (lane & 15) >> 2, p4 = lane & 3;
  unsigned tag = 0;
  auto lds_tag = [&]() { tag = __builtin_amdgcn_readfirstlane(*(const LAS unsigned*)(lds0 + L_KEY)); };
  ST(0);
  const unsigned j30 = (unsigned)(i - 2) & 31u, j7 = (j30 * 37) >> 8;
  const unsigned trl = j30 < 28 ? j7 : (2 * odd + j30) & 3, ct = j30 < 28 ? j30 - j7 * 7 + odd * 8 : 7;
  unsigned sn = ROT == 3 ? 3 * (trl + ct - 3 * (((trl + ct) * 43) >> 7)) + ((xc * 3) >> 3) : ROT == 2 ? 5 * ((trl + ct) & 1) + ((xc * 5) >> 3) : ROT == 1 ? (xc * 5) >> 2 : ROT == 4 ? (5 * ((trl + ct) & 3)) / 2 + (xc & 1) : 0;
  const unsigned tr = 4 * xp + trl;
  const int rb8 = 64 * xp + 32 * odd + i;
  const char* const xt = (const char*)p.x + tr * (128 * 1280);
  const char* const wt = (const char*)p.wqkv + ct * 256;
  const unsigned x0 = (lane >> 3) * 1280 + (((lane & 7) ^ g4) * 16), x1 = (8 + (lane >> 3)) * 1280 + (((lane & 7) ^ (4 + g4)) * 16);
  const unsigned w0 = g4 * 3840 + (((lane & 15) ^ (g4 << 2)) * 16), w1 = w0 + 4 * 3840;
  auto pieces = [&](unsigned e, unsigned slot) {
    const char* xs = xt + (sn * 128 + e * (16 * 1280)); const char* wsb = wt + (sn * 64 + e * 8) * 3840; const unsigned d = lds0 + slot * 32768 + e * 2048;
    glds(x0, xs, d); glds(w0, wsb, d + 16384); glds(x1, xs, d + 1024); glds(w1, wsb, d + 16384 + 1024);
  };

  const int rrow = 8 * rb8 + 2 * w + h;
  u32x4 rx[3], rw[3]; unsigned pfv = 0, genv; float a1 = 0.f, a2 = 0.f, ar = 0.f, sw = 0.f;
  auto router_load = [&]() {
    if (CODE_KB && lane < CODE_KB / 2) asm volatile("global_load_dword %0, %1, %2" : "+v"(pfv) : "v"(((i & 3) * 4 + w) * (CODE_KB * 64) + lane * 128), "s"(pc0) : "memory");
    const char* xr = (const char*)p.x + rrow * 1280 + l31 * 16; const char* wr = (const char*)p.wr + l31 * 16;
    const int t3 = l31 < 16 ? 1024 : 0;
    rx[0] = *(const GAS u32x4*)xr; rx[1] = *(const GAS u32x4*)(xr + 512); rw[0] = *(const GAS u32x4*)wr; rw[1] = *(const GAS u32x4*)(wr + 512);
    rx[2] = *(const GAS u32x4*)(xr + t3); rw[2] = *(const GAS u32x4*)(wr + t3);
    genv = __hip_atomic_load((GAS unsigned*)(ws + WS_GEN), __ATOMIC_RELAXED, __HIP_MEMORY_SCOPE_AGENT);
    FENCE();
  };
  auto router_dots = [&]() {
    if (l31 >= 16) rx[2] = rw[2] = (u32x4){};
#pragma unroll
    for (int e = 0; e < 3; ++e)
#pragma unroll
      for (int c = 0; c < 4; ++c) { a1 = dot2(rx[e][c], ONE2, a1); a2 = dot2(rx[e][c], rx[e][c], a2); ar = dot2(rx[e][c], rw[e][c], ar); sw = dot2(rw[e][c], ONE2, sw); }
    tag = __builtin_amdgcn_readfirstlane(genv) + 1u;
    if (tid == 0) *(LAS unsigned*)(lds0 + L_KEY) = tag;
    asm volatile("" : "+v"(a1), "+v"(a2), "+v"(ar), "+v"(sw) : "v"(pfv));
  };
  float rmb = 0.f, rdb = 0.f; unsigned rk = 0, rv = 0;
  auto router_step = [&](int k) {
    auto fdpp = [&](float& v, int st) { const unsigned u = __float_as_uint(v); v += __uint_as_float(st == 0 ? dpp<0x128>(u) : st == 1 ? dpp<0x124>(u) : st == 2 ? dpp<0x122>(u) : dpp<0x121>(u)); };
    auto fswp = [&](float& v) { auto r = __builtin_amdgcn_permlane16_swap(__float_as_uint(v), __float_as_uint(v), false, false); v = __uint_as_float(r[0]) + __uint_as_float(r[1]); };
    if (k < 4) { fdpp(a1, k); fdpp(a2, k); fdpp(ar, k); fdpp(sw, k); }
    if (k == 4) { fswp(a1); fswp(a2); }
    if (k == 5) { fswp(ar); fswp(sw); }
    if (k == 6) { a1 *= 1.f / 640.f; a2 = fmaxf(a2 * (1.f / 640.f) - a1 * a1, 0.f); }
    if (k == 7) { rmb = rb(a1); a2 = rb(a2) + 1e-5f; }
    if (k == 8) { a2 = __builtin_amdgcn_sqrtf(rb(a2)); ar -= rmb * sw; }
    if (k == 9) { rdb = rb(a2); a2 = __builtin_amdgcn_rcpf(rdb); }
    if (k == 10) { rk = pk(ar * a2, 0.f) & 0xffffu; rv = (__float_as_uint(rmb) >> 16) | __float_as_uint(rdb); }
    if (k == 11) rk = (rk & 0x8000u ? (~rk & 0xffffu) : (rk | 0x8000u)) << 16;
    if (k == 12) { if (l31 == 0) *(LAS unsigned*)(lds0 + L_KEYS + (2 * w + h) * 4) = rk | (2047u - rrow); }
    if (k == 13) { if (l31 == 0) __builtin_amdgcn_raw_buffer_store_b64((u32x2){rv, tag}, rs, WS_STATS + rrow * 8, 0, 16); }
    auto max8 = [&](unsigned& v, int st) { v = max(v, st == 0 ? dpp<0xB1>(v) : st == 1 ? dpp<0x4E>(v) : dpp<0x141>(v)); };
    if (k == 14) rk = rv = *(const LAS unsigned*)(lds0 + L_KEYS + (lane & 7) * 4);
    if (k >= 16 && k < 19) max8(rv, k - 16);
    if (k == 19) rk = rk == rv ? 0u : rk;
    if (k >= 20 && k < 23) max8(rk, k - 20);
    if (k == 23) { if (w == 0 && lane < 2) __builtin_amdgcn_raw_buffer_store_b64((u32x2){lane ? rk : rv, tag}, rs, WS_CAND + (2 * rb8 + lane) * 8, 0, 16); }
    asm volatile("" : "+v"(a1), "+v"(a2), "+v"(ar), "+v"(sw), "+v"(rmb), "+v"(rdb), "+v"(rk), "+v"(rv));
  };

  bf16x8 wdf[30];
  if (!spare) {
    int wq = w; asm volatile("" : "+s"(wq));
    if (wq < 4) router_load();
    pieces(w, 0); sn = sn == 9 ? 0 : sn + 1;
    if (wq >= 4) { pieces(2 * w - 8, 1); pieces(2 * w - 7, 1); }
    sn = sn == 9 ? 0 : sn + 1;
    FST(28);
    if (w >= 4) {
      const int j = w - 4;
      auto issue = [&](int slot) { pieces(2 * j, slot); pieces(2 * j + 1, slot); sn = sn == 9 ? 0 : sn + 1; };
      if (tid == 384) *(LAS u32x4*)(lds0 + L_CNT) = (u32x4){};
      const unsigned au = tr * 15 + ct, cg = (au * 205) >> 11, kg = au - cg * 10;
      WAITV(8); FST(31); BAR();
      FST(1); issue(2); FST(2);
      WAITV(8); FST(3); BAR();
      issue(3);
#pragma unroll 1
      for (int s = 4; s < 9; ++s) { FST(3 * s - 8); issue(s & 3); FST(3 * s - 7); WAITV(16); FST(3 * s - 6); BAR(); }
      lds_tag();
      auto ffn_dma = [&]() {
        const unsigned fo = (lane >> 3) * 3072 + (lane & 7) * 16;
        const char* fb = (const char*)(j < 2 ? p.wg : p.wu) + (kg * 64 + 32 * (j & 1)) * 3072 + cg * 128;
#pragma unroll
        for (int e = 0; e < 4; ++e) glds(fo, fb + e * 8 * 3072, lds0 + L_FFNA + (4 * j + e) * 1024);
      };
      auto ffn_landed = [&]() { WAITV(0); if (lane == 0) __hip_atomic_fetch_add((LAS unsigned*)(lds0 + L_CNT + 12), 1u, __ATOMIC_RELAXED, __HIP_MEMORY_SCOPE_WORKGROUP); };
      if (w == 6) {
        const char* ga = ws + WS_STATS + tr * 1024;
        u32x2 g0, g1;
        asm volatile("global_load_dwordx2 %0, %2, %3 sc1\n\tglobal_load_dwordx2 %1, %2, %3 offset:512 sc1" : "=&v"(g0), "=&v"(g1) : "v"(lane * 8), "s"(ga) : "memory");
        issue(1); WAITV(16); BAR();
        ffn_dma();
        asm volatile("s_waitcnt vmcnt(12)\n\ts_barrier\n\ts_waitcnt vmcnt(4)\n\ts_barrier" : "+v"(g0), "+v"(g1) :: "memory");
        if (!__all(g0[1] == tag && g1[1] == tag)) {
#pragma unroll 1
          for (unsigned sp = 0; sp < SPIN_CAP; ++sp) {
            g0 = __builtin_bit_cast(u32x2, ldg64(ga + lane * 8)); g1 = __builtin_bit_cast(u32x2, ldg64(ga + 512 + lane * 8));
            if (__all(g0[1] == tag && g1[1] == tag)) break; }
        }
        *(LAS f32x2*)(lds0 + L_STAT + lane * 8) = (f32x2){blo(g0[0]), __builtin_amdgcn_rcpf(bhi(g0[0]))};
        *(LAS f32x2*)(lds0 + L_STAT + 512 + lane * 8) = (f32x2){blo(g1[0]), __builtin_amdgcn_rcpf(bhi(g1[0]))};
        FST(23); BAR();
        ffn_landed();
      } else if (w < 6) { issue(1); WAITV(16); BAR(); ffn_dma(); WAITV(12); BAR(); WAITV(4); BAR(); BAR(); ffn_landed(); }
      else {
        Keys kk;
        asm volatile("buffer_load_dwordx4 %0, %4, %5, %6 offen sc1\n\tbuffer_load_dwordx4 %1, %4, %5, %6 offen offset:1024 sc1\n\t"
                     "buffer_load_dwordx4 %2, %4, %5, %6 offen offset:2048 sc1\n\tbuffer_load_dwordx4 %3, %4, %5, %6 offen offset:3072 sc1"
                     : "=&v"(kk.g[0]), "=&v"(kk.g[1]), "=&v"(kk.g[2]), "=&v"(kk.g[3]) : "v"(lane * 16), "s"(rs), "s"(WS_CAND) : "memory");
        issue(1); WAITV(16); BAR();
        ffn_dma();
        asm volatile("s_waitcnt vmcnt(12)\n\ts_barrier" : "+v"(kk.g[0]), "+v"(kk.g[1]), "+v"(kk.g[2]), "+v"(kk.g[3]) :: "memory");
        unsigned k1 = 0, k2 = 0;
        const bool early = keys_top2(kk, tag, k1, k2);
        u32x4 xc0 = {ONE2, ONE2, ONE2, ONE2}, xc1 = xc0;
        { const char* xq = (const char*)p.x + ((2047u - (lane & 1 ? k2 : k1)) & 2047u) * 1280 + kg * 128 + g4 * 16;
          if ((lane & 15) < 2) asm volatile("global_load_dwordx4 %0, %2, off\n\tglobal_load_dwordx4 %1, %2, off offset:64" : "+v"(xc0), "+v"(xc1) : "v"(xq) : "memory"); }
        asm volatile("s_waitcnt vmcnt(6)\n\ts_barrier\n\ts_barrier\n\ts_waitcnt vmcnt(0)" : "+v"(xc0), "+v"(xc1) :: "memory");
        FST(23);
        if (!early) {
          keys_wait(rs, lane, tag, k1, k2);
          if ((lane & 15) < 2) { const char* xq = (const char*)p.x + ((2047u - (lane & 1 ? k2 : k1)) & 2047u) * 1280 + kg * 128 + g4 * 16; xc0 = *(const GAS u32x4*)xq; xc1 = *(const GAS u32x4*)(xq + 64); }
        }
        if (b == 16 && lane == 0) __hip_atomic_store((GAS unsigned*)(ws + WS_GEN), tag, __ATOMIC_RELAXED, __HIP_MEMORY_SCOPE_AGENT);
        if (lane == 0) *(LAS u32x2*)(lds0 + L_TOP) = (u32x2){k1, k2};
#pragma unroll 1
        for (unsigned sp = 0; sp < SPIN_CAP; ++sp) { if (__builtin_amdgcn_readfirstlane(*(volatile LAS unsigned*)(lds0 + L_CNT + 12)) >= 3u) break; __builtin_amdgcn_s_sleep(1); }
        asm volatile("" ::: "memory");
#pragma unroll
        for (int n = 0; n < 8; ++n) {
          const unsigned a = lds0 + L_FFNA + (n >> 2) * 8192 + (8 * g4 + q4) * 128 + (n & 3) * 32 + 8 * p4;
          const f32x4 y = MFMA16(trfrag(a + 4096, a + 4608), __builtin_bit_cast(bf16x8, xc1), MFMA16(trfrag(a, a + 512), __builtin_bit_cast(bf16x8, xc0), (f32x4){}));
          if ((lane & 15) < 3) *(LAS f32x4*)(lds0 + L_TMP + (((n >> 2) * 3 + (lane & 15)) * 64 + 16 * (n & 3) + 4 * g4) * 4) = y;
        }
#pragma unroll
        for (int n = 0; n < 6; ++n) stg64(ws + WS_PARTA + (au * 384 + n * 64 + lane) * 8, gran(*(const LAS unsigned*)(lds0 + L_TMP + (n * 64 + lane) * 4), tag));
        FST(25);
      }
    } else {
      const int fh = w >> 1, th = w & 1;
      unsigned xa[4], wa[2], ko[4];
#pragma unroll
      for (int e = 0; e < 4; ++e) { const int ks = (2 * th + e) & 3, row = 64 * th + l31; xa[e] = lds0 + row * 128 + (((2 * ks + h) ^ ((row >> 1) & 7)) * 16); ko[e] = ks * 4096; }
#pragma unroll
      for (int f = 0; f < 2; ++f) wa[f] = lds0 + 16384 + (8 * h + q4) * 256 + (((4 * (2 * fh + f) + 2 * (g4 & 1) + (p4 >> 1)) ^ (q4 << 2)) * 16) + 8 * (p4 & 1);
      f32x16 acc[2][2] = {};
      f32x4 cs0 = {}, cs1 = {};
      const unsigned selw = (lane & 15) == (g4 & 1) ? ONE2 : 0u;
      const bf16x8 sel = __builtin_bit_cast(bf16x8, (u32x4){selw, selw, selw, selw});
      struct Fr { bf16x8 v[4]; };
      Fr FA[4], FB[4];
      auto rd = [&](Fr* F, int n, unsigned so) {
        const int e = n >> 2, j = n & 3;
        if (j < 2) { const unsigned a = wa[j] + (so + ko[e]); F[e].v[j] = trfrag(a, a + 1024); } else F[e].v[j] = lds128(xa[e] + so + (j - 2) * 4096);
      };
      auto colsum = [&](const Fr* C, int e) { cs0 = MFMA16(C[e].v[0], sel, cs0); cs1 = MFMA16(C[e].v[1], sel, cs1); };
      auto body = [&](Fr* N, const Fr* C, unsigned so, int r0 = 99) {
#pragma unroll
        for (int n = 0; n < 16; ++n) {
          const int e = n >> 2, f = (n >> 1) & 1, t = n & 1;
          acc[f][t] = MFMA32(C[e].v[f], C[e].v[2 + t], acc[f][t]);
          if (n < 4) { rd(N, 2 * n, so); rd(N, 2 * n + 1, so); } else if (n < 12) rd(N, n + 4, so);
          if (n >= 2 && r0 + n - 2 < 24) router_step(r0 + n - 2);
          FENCE();
        }
        colsum(C, 0); colsum(C, 1);
        FENCE();
      };
      FST(29);
      WAITV(0);
      FST(30); BAR(); ST(1); FST(31);
#pragma unroll
      for (int n = 0; n < 16; ++n) rd(FA, n, 0);
      FST(32); router_dots(); FST(33);
      BAR();
#pragma unroll
      for (int s = 1; s <= 7; s += 2) { FST(2 * s); body(FB, FA, (s & 3) * 32768, s == 1 ? 0 : 99); FST(2 * s + 1); BAR(); FST(2 * s + 2); body(FA, FB, ((s + 1) & 3) * 32768, s == 1 ? 14 : 99); FST(2 * s + 3); BAR(); }
      FST(18);
      body(FB, FA, 32768);
      colsum(FB, 0); colsum(FB, 1);
      if ((lane & 15) < 2) { const unsigned d = lds0 + L_CS + w * 256 + ((lane & 15) * 16 + 4 * g4) * 4; *(LAS f32x4*)d = cs0; *(LAS f32x4*)(d + 128) = cs1; }
      FST(23); BAR(); FST(19);
      const unsigned ca = lds0 + L_CS + fh * 512 + l31 * 4;
      const float c0 = *(const LAS float*)ca + *(const LAS float*)(ca + 256), c1 = *(const LAS float*)(ca + 128) + *(const LAS float*)(ca + 384);
      const f32x2 st0 = *(const LAS f32x2*)(lds0 + L_STAT + (64 * th + l31) * 8), st1 = *(const LAS f32x2*)(lds0 + L_STAT + (64 * th + 32 + l31) * 8);
      const float qs = ct < 5 ? 0.22360679774997896f * 1.4426950408889634f : 1.f;
      const unsigned qo = WS_QKV + (4 * tr + 2 * th) * 122880 + (16 * ct + 8 * fh + h) * 512 + l31 * 16;
      bf16x8 ax[2], bx[2]; float rr[2]; unsigned pw[8];
      auto split3 = [&](float c) {
        const float hi = rb(c), r1 = c - hi, mid = rb(r1);
        return __builtin_bit_cast(bf16x8, (u32x4){h ? 0u : pk(hi, mid), h ? 0u : pk(r1 - mid, 0.f), 0u, 0u});
      };
      auto epi = [&](int tile, int part) {
        const int f = tile & 1, t = tile >> 1;
#pragma unroll
        for (int g = 2 * part; g < 2 * part + 2; ++g) {
          float v0 = acc[f][t][2 * g] * rr[t]; asm("" : "+v"(v0));
          pw[g] = pk(v0, acc[f][t][2 * g + 1] * rr[t]);
        }
        if (part & 1) {
          const int g = part >> 1;
          auto r0 = __builtin_amdgcn_permlane32_swap(pw[4 * g], pw[4 * g + 2], false, false);
          auto r1 = __builtin_amdgcn_permlane32_swap(pw[4 * g + 1], pw[4 * g + 3], false, false);
          __builtin_amdgcn_raw_buffer_store_b128((u32x4){r0[0], r1[0], r0[1], r1[1]}, rs, qo + t * 122880 + (4 * f + 2 * g) * 512, 0, 16);
        }
      };
#pragma unroll
      for (int n = 0; n < 20; ++n) {
        const int tile = n < 8 ? n >> 2 : n < 10 ? n - 8 : n < 15 ? 2 : 3, e = n < 8 ? n & 3 : n < 10 ? 4 : (n - 10) % 5, f = tile & 1, t = tile >> 1;
        acc[f][t] = e < 4 ? MFMA32(FB[e].v[f], FB[e].v[2 + t], acc[f][t]) : MFMA32(ax[f], bx[t], acc[f][t]);
        if (n == 4) { ax[0] = split3(c0); const unsigned m = pk(-st0[0], -st0[0]); bx[0] = __builtin_bit_cast(bf16x8, (u32x4){m, m, 0u, 0u}); rr[0] = st0[1] * qs; }
        if (n == 5) { ax[1] = split3(c1); const unsigned m = pk(-st1[0], -st1[0]); bx[1] = __builtin_bit_cast(bf16x8, (u32x4){m, m, 0u, 0u}); rr[1] = st1[1] * qs; }
        if (n == 4 || n == 5) asm volatile("" : "+v"(ax[n - 4]), "+v"(bx[n - 4]), "+v"(rr[n - 4]));
        if (n >= 10) epi((n - 10) >> 2, (n - 10) & 3);
        FENCE();
        if (n == 9 || n == 13 || n == 17) FST(24 + (n - 9) / 4);
      }
      ST(2); FST(20);
      epi(2, 2); epi(2, 3);
#pragma unroll
      for (int part = 0; part < 4; ++part) epi(3, part);
      FST(21);
      WAITV(0);
      if (lane == 0 && __hip_atomic_fetch_add((LAS unsigned*)(lds0 + L_CNT), 1u, __ATOMIC_RELAXED, __HIP_MEMORY_SCOPE_WORKGROUP) == 3u)
        stg64(ws + WS_FLAG1 + (tr * 16 + ct) * 256, gran(tag, ~tag));
      ST(3); FST(22);
    }
  } else {
    if (w < 4) router_load();
#pragma unroll 1
    for (int e = 0; e < 15; ++e) {
      const unsigned L = 512 * e + tid, r = (L * 52429) >> 22, pc = L - r * 80;
      glds(r * 1280 + (pc ^ (((r & 3) | ((r >> 1) & 4)) << 1)) * 16, (const char*)p.wd + (2 * xc + i) * 96 * 1280, lds0 + (8 * e + w) * 1024);
    }
    if (w < 4) {
      router_dots();
#pragma unroll
      for (int k = 0; k < 14; ++k) router_step(k);
    }
    if (tid == 384) *(LAS u32x4*)(lds0 + L_CNT) = (u32x4){};
    BAR();
    if (w == 0) {
#pragma unroll
      for (int k = 14; k < 24; ++k) router_step(k);
    }
    WAITV(0); BAR(); ST(1);
    if (w >= 4) {
      lds_tag();
#pragma unroll
      for (int n = 0; n < 30; ++n) {
        const unsigned a = lds0 + (8 * g4 + q4) * 1280 + (((2 * (w - 4) + n / 15) ^ (q4 | ((g4 & 1) << 2))) * 32) + 8 * p4 + (n % 15 / 5) * 40960 + (n % 5) * 256;
        wdf[n] = trfrag(a, a + 5120);
      }
    } else if (w == 0) {
      unsigned k1 = 0, k2 = 0;
      keys_wait(rs, lane, tag, k1, k2);
      const char* sa = ws + WS_STATS + ((2047u - (lane & 1 ? k2 : k1)) & 2047u) * 8;
      u64 sg = ldg64(sa);
#pragma unroll 1
      for (unsigned sp = 0; sp < SPIN_CAP && !__all((unsigned)(sg >> 32) == tag); ++sp) sg = ldg64(sa);
      if (lane < 2) { *(LAS unsigned*)(lds0 + L_TOP + lane * 4) = lane ? k2 : k1; *(LAS unsigned*)(lds0 + L_TOP + 8 + lane * 4) = (unsigned)sg; }
    }
    BAR(); ST(2);
  }
  ST(4); FST(35);
  const u64 ftag = gran(tag, ~tag);

  const int tg3 = w & 3, kh = w >> 2, tok0 = 256 * xc + 64 * (i >> 3), n0 = 80 * cs80;
  const bool two = w == 4 || w == 5;
  const unsigned it0 = tid, it1 = two ? tid + 256 : tid, tk0 = (it0 * 6554) >> 16, tk1 = (it1 * 6554) >> 16, ch0 = it0 - 10 * tk0, ch1 = it1 - 10 * tk1;
  const unsigned eo0 = (tok0 + tk0) * 1280 + n0 * 2 + ch0 * 16, eo1 = (tok0 + tk1) * 1280 + n0 * 2 + ch1 * 16;
  u32x4 xv0, xv1; u64 sg0_, sg1_;
  auto pre3 = [&]() {
    xv0 = *(const GAS u32x4*)((const char*)p.x + eo0); sg0_ = ldg64(ws + WS_STATS + (tok0 + tk0) * 8);
    xv1 = *(const GAS u32x4*)((const char*)p.x + eo1); sg1_ = ldg64(ws + WS_STATS + (tok0 + tk1) * 8);
  };

  {
    const int R = i >> 2, hd = 4 * xc + (i & 3), p0 = (5 * hd) >> 1, oh = hd & 1;
    const int pf = p0 + oh, pp = oh ? p0 : p0 + 2, cc = (lane & 1 ? p0 + 2 : p0) >> 4;
    if (w < 4) {
      const int lo = w < 2, tu = 2 * w + 1 - lo, tv = 2 * w + lo;
      poll(ws + WS_FLAG1 + ((2 * R + (w >> 1)) * 16 + cc) * 256, lane < 2, ftag);
      ST(5); FST(36);
      bf16x8 qf[2][2];
      { const unsigned a0 = (pf + h) * 512 + l31 * 16, a1 = pp * 512 + l31 * 16 + oh * 8, ou = WS_QKV + (8 * R + tu) * 122880, ov = WS_QKV + (8 * R + tv) * 122880;
        const u32x4 u0 = __builtin_amdgcn_raw_buffer_load_b128(rs, a0, ou, 16), v0 = __builtin_amdgcn_raw_buffer_load_b128(rs, a0, ov, 16);
        const u32x2 u1 = __builtin_amdgcn_raw_buffer_load_b64(rs, a1, ou, 16), v1 = __builtin_amdgcn_raw_buffer_load_b64(rs, a1, ov, 16);
        qf[0][0] = __builtin_bit_cast(bf16x8, u0); qf[0][1] = __builtin_bit_cast(bf16x8, (u32x4){h ? 0u : u1[0], h ? 0u : u1[1], 0u, 0u});
        qf[1][0] = __builtin_bit_cast(bf16x8, v0); qf[1][1] = __builtin_bit_cast(bf16x8, (u32x4){h ? 0u : v1[0], h ? 0u : v1[1], 0u, 0u}); }
      pre3();
      f32x16 cz = {}, ca, cb;
#pragma unroll
      for (int r = 0; r < 16; ++r) {
        const bool le = (r & 3) + 8 * (r >> 2) + 4 * h <= l31;
        const float d = le ? 0.f : -1e30f, f = le || R == 0 ? -1e30f : 0.f;
        ca[r] = lo ? d : f; cb[r] = lo ? f : d;
      }
      const unsigned ka = lds0 + L_KV + h * 512 + l31 * 16;
      const int pi = min(4 * (g4 & 1) + p4, 5);
      const unsigned va = lds0 + L_KV + L_V + (pi >> 1) * VPS + (pi & 1) * 8 + (4 * h + q4) * 16;
      struct KF { bf16x8 a, b; };
      auto kread = [&](int kt) {
        const unsigned a = ka + kt * KVS;
        return KF{lds128(a), lds128(a + 1024)};
      };
      auto vread = [&](int kt) { const unsigned v = va + kt * KVS; return KF{trfrag(v, v + 128), trfrag(v + 256, v + 384)}; };
      auto ex = [&](const f32x16& s, int e) { return pk(__builtin_amdgcn_exp2f(s[2 * e]), __builtin_amdgcn_exp2f(s[2 * e + 1])); };
      const int P0 = lo ? 6 : 8, P1 = w == 0 ? 4 : w == 1 ? 8 : w == 2 ? 6 : 10, P2 = lo ? 2 + 2 * w : 6 + 2 * w;
      const int tp[10] = {P0, P0 + 1, P1, P1 + 1, lo ? 2 * w + 8 : 2 * w + 1, lo ? 2 * w + 9 : 2 * w, P2, P2 + 1, lo ? 2 * w + 1 : 2 * w + 8, lo ? 2 * w : 2 * w + 9};
      auto at = [&](int ps) { const int e = (ps == 1 || ps == 3 || ps == 7), t = tp[ps - e]; return (R == 0 && t < 8 ? 8 : t) + e; };
      auto setz = [&](int ps) {
        if (ps == 1 || ps == 3 || ps == 5 || ps == 7 || ps == 9) return;
        if (R == 0) { const float z = tp[ps] < 8 ? -1e30f : 0.f;
#pragma unroll
          for (int r = 0; r < 16; ++r) cz[r] = z;
          asm volatile("" : "+v"(cz)); } };
      setz(0);
      asm volatile("" : "+v"(ca), "+v"(cb), "+v"(cz), "+v"(qf[0][0]), "+v"(qf[0][1]), "+v"(qf[1][0]), "+v"(qf[1][1]));
      BAR(); ST(6); FST(39);
      KF K0, K1, K2, V0, V1, V2; f32x16 S0, S1, o[2] = {};
      K0 = kread(at(0)); V0 = vread(at(0));
      S0 = MFMA32(K0.b, qf[0][1], MFMA32(K0.a, qf[0][0], cz));
      u32x4 pa = {}, pb = {}; float x0, x1, x2, x3;
#define SET3(N, p) ((p) % 3 == 0 ? N##0 : (p) % 3 == 1 ? N##1 : N##2)
#define EXP4(s, e) do { x0 = __builtin_amdgcn_exp2f(s[4 * e]); x1 = __builtin_amdgcn_exp2f(s[4 * e + 1]); x2 = __builtin_amdgcn_exp2f(s[4 * e + 2]); x3 = __builtin_amdgcn_exp2f(s[4 * e + 3]); \
                        asm volatile("" : "+v"(x0), "+v"(x1), "+v"(x2), "+v"(x3)); FENCE(); } while (0)
#define PACK2(d, e) do { d[e] = pk(x0, x1); d[e + 1] = pk(x2, x3); asm volatile("" : "+v"(d)); FENCE(); } while (0)
      auto unit = [&](auto U, f32x16& sc, f32x16& sn) {
        constexpr int u = decltype(U)::value, np = u + 1 < 18 ? UP[u + 1] : 0, nq = u + 1 < 18 ? UQ[u + 1] : 0, pu = u > 0 ? u - 1 : 0;
        if constexpr (u == 2 || u == BC_AT) BAR();
        FST(40 + u);
        if constexpr (u + 1 < 18) { if constexpr (np != UP[u]) setz(np); sn = MFMA32(SET3(K, np).a, qf[nq][0], u + 1 == 8 || u + 1 == 10 ? ca : u + 1 >= 16 ? cb : cz); FENCE(); }
        if constexpr (u > 0) PACK2(pb, 2);
        EXP4(sc, 0);
        if constexpr (u > 0) { o[UQ[pu]] = MFMA32(SET3(V, UP[pu]).b, __builtin_bit_cast(bf16x8, pb), o[UQ[pu]]); FENCE(); }
        if constexpr (u + 2 < 18 && UP[u + 2] != UP[u + 1]) SET3(K, UP[u + 2]) = kread(at(UP[u + 2]));
        if constexpr (u + 1 < 18 && np != UP[u]) SET3(V, np) = vread(at(np));
        PACK2(pa, 0);
        EXP4(sc, 1);
        if constexpr (u + 1 < 18) { sn = MFMA32(SET3(K, np).b, qf[nq][1], sn); FENCE(); }
        PACK2(pa, 2);
        EXP4(sc, 2);
        o[UQ[u]] = MFMA32(SET3(V, UP[u]).a, __builtin_bit_cast(bf16x8, pa), o[UQ[u]]); FENCE();
        PACK2(pb, 0);
        EXP4(sc, 3);
      };
#define UNIT2(n) unit(IC<n>{}, S0, S1); unit(IC<n + 1>{}, S1, S0);
      UNIT2(0) UNIT2(2) UNIT2(4) UNIT2(6) UNIT2(8) UNIT2(10) UNIT2(12) UNIT2(14) UNIT2(16)
      PACK2(pb, 2);
      o[0] = MFMA32(V0.b, __builtin_bit_cast(bf16x8, pb), o[0]);
      auto rowsum = [&](const f32x16& a) { auto r = __builtin_amdgcn_permlane32_swap(__float_as_uint(a[8]), __float_as_uint(a[8]), false, false); return __uint_as_float(r[1]); };
      float l[2] = {rowsum(o[1]), rowsum(o[0])};
      if (FORCE_FB || !__all(l[0] > 0x1p-60f && l[0] < 0x1p60f && l[1] > 0x1p-60f && l[1] < 0x1p60f)) {
        int qt = tu;
#pragma unroll 1
        for (int e = 0; e < 2; ++e) {
          const int k0 = R > 0 ? qt : 8;
          auto score = [&](int kt, float base) {
            f32x16 c;
#pragma unroll
            for (int r = 0; r < 16; ++r) { const bool le = (r & 3) + 8 * (r >> 2) + 4 * h <= l31; c[r] = (kt == qt + 8 && !le) || (kt == qt && le) ? -1e30f : base; }
            const KF k = kread(kt);
            return MFMA32(k.b, qf[0][1], MFMA32(k.a, qf[0][0], c));
          };
          float m = -3e38f;
#pragma unroll 1
          for (int kt = k0; kt <= qt + 8; ++kt) { const f32x16 s = score(kt, 0.f);
#pragma unroll
            for (int r = 0; r < 16; ++r) m = fmaxf(m, s[r]); }
          { auto r = __builtin_amdgcn_permlane32_swap(__float_as_uint(m), __float_as_uint(m), false, false); m = fmaxf(__uint_as_float(r[0]), __uint_as_float(r[1])); }
          o[0] = (f32x16){};
#pragma unroll 1
          for (int kt = k0; kt <= qt + 8; ++kt) {
            const f32x16 s = score(kt, -m); const KF v = vread(kt);
            o[0] = MFMA32(v.a, __builtin_bit_cast(bf16x8, (u32x4){ex(s, 0), ex(s, 1), ex(s, 2), ex(s, 3)}), o[0]);
            o[0] = MFMA32(v.b, __builtin_bit_cast(bf16x8, (u32x4){ex(s, 4), ex(s, 5), ex(s, 6), ex(s, 7)}), o[0]);
          }
          const f32x16 to = o[0]; o[0] = o[1]; o[1] = to;
          const bf16x8 t0 = qf[0][0], t1 = qf[0][1]; qf[0][0] = qf[1][0]; qf[0][1] = qf[1][1]; qf[1][0] = t0; qf[1][1] = t1;
          qt = tv;
        }
        l[0] = rowsum(o[1]); l[1] = rowsum(o[0]);
      }
      ST(7); FST(59);
#pragma unroll
      for (int e = 0; e < 2; ++e) {
        const f32x16& a = o[1 - e]; const float ri = __builtin_amdgcn_rcpf(l[e]);
        const unsigned e0 = pk(a[0] * ri, a[1] * ri), e1 = pk(a[2] * ri, a[3] * ri), e2 = pk(a[4] * ri, a[5] * ri), e3 = pk(a[6] * ri, a[7] * ri);
        const u32x2 el = {pk(a[8] * ri, a[9] * ri), pk(a[10] * ri, a[11] * ri)};
        auto r0 = __builtin_amdgcn_permlane32_swap(e0, e2, false, false), r1 = __builtin_amdgcn_permlane32_swap(e1, e3, false, false);
        const unsigned ob = WS_O + (16 * R + 2 * (e ? tu : tv) + (l31 >> 4)) * 20480;
        __builtin_amdgcn_raw_buffer_store_b128((u32x4){r0[0], r1[0], r0[1], r1[1]}, rs, ob + (2 * hd + h) * 256 + (lane & 15) * 16, 0, 16);
        if (h == 0) __builtin_amdgcn_raw_buffer_store_b64(el, rs, ob + 16384 + hd * 128 + (lane & 15) * 8, 0, 16);
      }
      WAITV(0);
      if (lane == 0 && __hip_atomic_fetch_add((LAS unsigned*)(lds0 + L_CNT + 4), 1u, __ATOMIC_RELAXED, __HIP_MEMORY_SCOPE_WORKGROUP) == 3u)
        stg64(ws + WS_FLAG2 + (R * 32 + hd) * 256, ftag);
      ST(8); FST(60);
      BAR();
    } else {
      __builtin_amdgcn_s_setprio(2);
      const int j = w - 4;
      const WoOff woo = wo_offsets(lane);
    const int sp_ = 2 * xc + i;
    unsigned ga[3]; u32x2 pa_[30];
    auto gather = [&]() {
      asm volatile("" ::: "memory");
#pragma unroll
      for (int n = 0; n < 30; ++n) pa_[n] = __builtin_amdgcn_raw_buffer_load_b64(rs, ga[n / 10], WS_PARTA + (n % 10) * 3072, 16);
    };
#pragma unroll
      for (int r = 0; r < 3; ++r) { const unsigned e = min(144 * j + 64 * r + lane, 575), rc = (e * 683) >> 16, F = 96 * sp_ + e - 96 * rc; ga[r] = ((F >> 6) * 3840 + rc * 64 + (F & 63)) * 8; asm volatile("" : "+v"(ga[r])); }
      const int kt[4] = {6 + j, j < 2 ? 4 + j : 8 + j, j, 12 + j};
      { const int e = lane >> 2, T = 8 * R - 8 + (e == 0 ? kt[0] : e == 1 ? kt[1] : e == 2 ? kt[2] : kt[3]);
        const bool act = lane < 16 && T >= 0;
        poll(ws + WS_FLAG1 + (act ? ((T >> 2) * 16 + (lane & 2 ? 10 : 5) + cc) * 256 : 0), act, ftag); }
      ST(5); FST(36);
      const unsigned so0 = (80 + pf + h) * 512 + l31 * 16, so1 = (h ? (160 + pf) * 512 : (80 + pp) * 512 + 8 * oh) + l31 * 16, so2 = (h ? (160 + pp) * 512 + 8 * oh : (161 + pf) * 512) + l31 * 16;
      struct T3 { u32x4 a, b, c; };
      auto fetch = [&](int t) {
        const int T = 8 * R - 8 + t; const unsigned m = T < 0 ? 0x40000000u : 0u, o = WS_QKV + max(T, 0) * 122880;
        return T3{__builtin_amdgcn_raw_buffer_load_b128(rs, so0 | m, o, 16), __builtin_amdgcn_raw_buffer_load_b128(rs, so1 | m, o, 16), __builtin_amdgcn_raw_buffer_load_b128(rs, so2 | m, o, 16)};
      };
      auto stage = [&](int t, T3 v) {
        if (R == 0 && t < 8) return;
        const unsigned kd = lds0 + L_KV + t * KVS + l31 * 16;
        if (h) v.c[2] = v.c[3] = ONE2;
        *(LAS u32x4*)(kd + h * 512) = v.a; *(LAS u32x4*)(kd + 1024 + h * 512) = v.b; *(LAS u32x4*)(kd + L_V + (1 + h) * VPS) = v.c;
      };
      const T3 t0 = fetch(kt[0]), t1 = fetch(kt[1]);
      FENCE();
      stage(kt[0], t0);
      FENCE();
      const T3 t2 = fetch(kt[2]), t3 = fetch(kt[3]);
      FENCE();
      BAR(); ST(6); FST(39);
      auto pause = [&](int n) { for (int e = 0; e < n; ++e) __builtin_amdgcn_s_sleep(1); };
      pause(j); stage(kt[1], t1);
      BAR(); FST(40);
      pause(2 * j); stage(kt[2], t2); pause(1); stage(kt[3], t3);
      unsigned pf = 0;
      if (!spare) {
        const unsigned po = ((i >> 3) * 160 + j * 40 + (lane >> 1)) * 1280 + (lane & 1) * 128;
        const char* pb = (const char*)p.wo + ((cs80 * 160) & ~127);
        asm volatile("global_load_dword %0, %1, %2" : "+v"(pf) : "v"(po), "s"(pb) : "memory");
        if (lane < 16) asm volatile("global_load_dword %0, %1, %2" : "+v"(pf) : "v"(po + 32 * 1280), "s"(pb) : "memory");
        pre3();
        BAR(); FST(41);
        asm volatile("s_waitcnt vmcnt(0)" : "+v"(pf) :: "memory");
        FST(44);
        wo_steps(5 * j, 5 * j + 5, woo, p.wo, cs80, lds0);
      } else {
        gather();
        FENCE();
        BAR(); FST(41);
        wo_steps(5 * j, 5 * j + 5, woo, p.wo, cs80, lds0);
        FST(45);
#pragma unroll 1
        for (unsigned sp = 0; sp < SPIN_CAP; ++sp) {
          unsigned bad = 0;
#pragma unroll
          for (int n = 0; n < 30; ++n) bad |= pa_[n][1] ^ tag;
          if (__all(bad == 0)) break;
          gather();
        }
#pragma unroll
        for (int r = 0; r < 3; ++r) {
          float s0 = 0.f, s1 = 0.f;
#pragma unroll
          for (int kg = 0; kg < 5; ++kg) { s0 += __uint_as_float(pa_[10 * r + kg][0]); s1 += __uint_as_float(pa_[10 * r + 5 + kg][0]); }
          if (64 * r + lane < 144) *(LAS float*)(lds0 + L_TMP + (144 * j + 64 * r + lane) * 4) = s0 + s1;
        }
        FST(46);
        __builtin_amdgcn_s_waitcnt(0xC07F);
        if (lane == 0) __hip_atomic_fetch_add((LAS unsigned*)(lds0 + L_CNT + 8), 1u, __ATOMIC_RELAXED, __HIP_MEMORY_SCOPE_WORKGROUP);
#pragma unroll 1
        for (unsigned sp = 0; sp < SPIN_CAP; ++sp) { if (__builtin_amdgcn_readfirstlane(*(volatile LAS unsigned*)(lds0 + L_CNT + 8)) >= 4u) break; __builtin_amdgcn_s_sleep(1); }
        asm volatile("" ::: "memory");
        FST(47);
      const unsigned hb = lds0 + L_TMP + 2304 + j * 384, yb = lds0 + L_KV + j * 1280;
#pragma unroll
      for (int r = 0; r < 3; ++r) {
        const int e = 64 * r + lane, tk = e >= 96; const unsigned a = lds0 + L_TMP + (e - 96 * tk) * 4, sg = *(const LAS unsigned*)(lds0 + L_TOP + 8 + tk * 4);
        const float m = blo(sg), rd = __builtin_amdgcn_rcpf(bhi(sg));
        const float g = rb((*(const LAS float*)(a + tk * 384) - m * *(const LAS float*)(a + 768)) * rd);
        const float u = rb((*(const LAS float*)(a + 1152 + tk * 384) - m * *(const LAS float*)(a + 1920)) * rd);
        *(LAS u16*)(hb + e * 2) = (u16)pk(rb(g * __builtin_amdgcn_rcpf(1.f + __expf(-g))) * u, 0.f);
      }
      bf16x8 hf[3];
#pragma unroll
      for (int s = 0; s < 3; ++s) hf[s] = lds128(hb + (lane & 1) * 192 + g4 * 16 + s * 64);
      f32x4 y[10] = {};
#pragma unroll
      for (int s = 0; s < 3; ++s)
#pragma unroll
        for (int n = 0; n < 10; ++n) y[n] = MFMA16(wdf[(n / 5) * 15 + 5 * s + n % 5], hf[s], y[n]);
      if ((lane & 15) < 2) {
#pragma unroll
        for (int n = 0; n < 10; ++n) *(LAS f32x4*)(yb + ((lane & 15) * 160 + 32 * (n % 5) + 16 * (n / 5) + 4 * g4) * 4) = y[n];
      }
#pragma unroll
      for (int n = 0; n < 5; ++n) {
        const int e = 64 * n + lane, tk = e >= 160, r = e - 160 * tk;
        __builtin_amdgcn_raw_buffer_store_b64((u32x2){*(const LAS unsigned*)(yb + e * 4), tag}, rs, (tk * 640 + 128 * (r >> 5) + 32 * j + (r & 31)) * 8, WS_YPART + sp_ * 10240, 16);
      }
      pre3();
      ST(3);
      }
      FST(42);
      WAITV(0);
      FST(43);
      __builtin_amdgcn_s_setprio(0);
      BAR();
    }
  }

  {
    const int tg = tg3;
    const unsigned wa = lds0 + L_WO + (kh * 320 + 4 * g4 + q4) * 160 + 8 * p4;
    constexpr int NPRE = 6;
    bf16x8 wf[NPRE][5];
#pragma unroll
    for (int s = 0; s < NPRE; ++s)
#pragma unroll
      for (int n = 0; n < 5; ++n) wf[s][n] = trfrag(wa + s * 5120 + n * 32, wa + s * 5120 + 2560 + n * 32);
#pragma unroll 1
    for (unsigned sp = 0; sp < SPIN_CAP && !__all((unsigned)(sg0_ >> 32) == tag && (unsigned)(sg1_ >> 32) == tag); ++sp) {
      sg0_ = ldg64(ws + WS_STATS + (tok0 + tk0) * 8); sg1_ = ldg64(ws + WS_STATS + (tok0 + tk1) * 8); }
    f32x4 base[4];
#pragma unroll
    for (int e = 0; e < 4; ++e) {
      const unsigned sg = (unsigned)(e < 2 ? sg0_ : sg1_); const float m = blo(sg), rd = __builtin_amdgcn_rcpf(bhi(sg));
#pragma unroll
      for (int c = 0; c < 4; ++c) { const unsigned u = (e < 2 ? xv0 : xv1)[2 * (e & 1) + (c >> 1)]; const float v = c & 1 ? bhi(u) : blo(u); base[e][c] = v + (v - m) * rd; }
    }
    const u32x2 kk12 = *(const LAS u32x2*)(lds0 + L_TOP);
    const unsigned k1 = __builtin_amdgcn_readfirstlane(kk12[0]), k2 = __builtin_amdgcn_readfirstlane(kk12[1]);
    const unsigned i1 = 2047u - (k1 & 0xffffu) - tok0, i2 = 2047u - (k2 & 0xffffu) - tok0;
#pragma unroll
    for (int s = 0; s < NPRE; ++s)
#pragma unroll
      for (int n = 0; n < 5; ++n) asm volatile("" : "+v"(wf[s][n]));
    asm volatile("" : "+v"(base[0]), "+v"(base[1]), "+v"(base[2]), "+v"(base[3]));
    poll(ws + WS_FLAG2 + (xc * 32 + 16 * kh + (lane & 15)) * 256, lane < 16, ftag);
    ST(9); FST(61);
    const unsigned ob = WS_O + (tok0 / 16 + tg) * 20480;
    u32x4 of[10];
    auto oload = [&](int s) {
      if (s < 8) of[s] = __builtin_amdgcn_raw_buffer_load_b128(rs, lane * 16 + s * 1024, ob + kh * 8192, 16);
      else {
        const unsigned a = (8 * (s - 8) + 2 * g4) * 128 + (lane & 15) * 8;
        const u32x2 u0 = __builtin_amdgcn_raw_buffer_load_b64(rs, a, ob + 16384 + kh * 2048, 16), u1 = __builtin_amdgcn_raw_buffer_load_b64(rs, a + 128, ob + 16384 + kh * 2048, 16);
        of[s] = (u32x4){u0[0], u0[1], u1[0], u1[1]};
      }
    };
#pragma unroll
    for (int s = 0; s < OPRE; ++s) oload(s);
    FENCE();
    FST(62);
    f32x4 acc[5] = {};
#pragma unroll
    for (int s = 0; s < 10; ++s) {
      if (s + OPRE < 10) oload(s + OPRE);
      FENCE();
#pragma unroll
      for (int n = 0; n < 5; ++n) acc[n] = MFMA16(wf[s % NPRE][n], __builtin_bit_cast(bf16x8, of[s]), acc[n]);
      if (s + NPRE < 10) {
#pragma unroll
        for (int n = 0; n < 5; ++n) wf[s][n] = trfrag(wa + (s + NPRE) * 5120 + n * 32, wa + (s + NPRE) * 5120 + 2560 + n * 32);
      }
      FENCE();
    }
    ST(10); FST(63);
    if ((i1 < 64u || i2 < 64u) && w < 5) {
      const int c = tid >= 160, col = (tid - 160 * c) >> 1;
      const unsigned ya = (c * 640 + n0 + col) * 8 + (tid & 1) * 81920;
      float s = 0.f;
#pragma unroll 1
      for (unsigned sp = 0; sp < SPIN_CAP; ++sp) {
        unsigned bad = 0; s = 0.f;
        asm volatile("" ::: "memory");
#pragma unroll
        for (int e = 0; e < 8; ++e) { const u32x2 v = __builtin_amdgcn_raw_buffer_load_b64(rs, ya, WS_YPART + e * 10240, 16); bad |= v[1] ^ tag; s += __uint_as_float(v[0]); }
        if (__all(bad == 0)) break;
      }
      s += __uint_as_float(dpp<0xB1>(__float_as_uint(s)));
      const unsigned kk = (c ? k2 : k1) >> 16;
      const float r = blo(kk & 0x8000u ? kk & 0x7fffu : ~kk & 0xffffu);
      if (!(tid & 1)) *(LAS float*)(lds0 + L_UPD + (tid >> 1) * 4) = rb(rb(s) * rb(__builtin_amdgcn_rcpf(1.f + __expf(-r))));
    }
#pragma unroll
    for (int n = 0; n < 5; ++n) *(LAS f32x4*)(lds0 + L_RED + kh * 21504 + (16 * tg + (lane & 15)) * 336 + (16 * n + 4 * g4) * 4) = acc[n];
    BAR();
    auto fin = [&](unsigned tk, unsigned ch, unsigned eo, f32x4 b0, f32x4 b1) {
      const unsigned a = lds0 + L_RED + tk * 336 + ch * 32;
      b0 += *(const LAS f32x4*)a + *(const LAS f32x4*)(a + 21504); b1 += *(const LAS f32x4*)(a + 16) + *(const LAS f32x4*)(a + 21504 + 16);
      if (tk == i1 || tk == i2) { const unsigned u = lds0 + L_UPD + (tk == i1 ? 0 : 320) + ch * 32; b0 += *(const LAS f32x4*)u; b1 += *(const LAS f32x4*)(u + 16); }
      *(GAS u32x4*)((char*)p.out + eo) = (u32x4){pk(b0[0], b0[1]), pk(b0[2], b0[3]), pk(b1[0], b1[1]), pk(b1[2], b1[3])};
    };
    fin(tk0, ch0, eo0, base[0], base[1]);
    if (two) fin(tk1, ch1, eo1, base[2], base[3]);
    ST(11);
  }
}

extern "C" void kernel_launch(void* const* d_in, const int* in_sizes, int n_in, void* d_out, int out_size, void* d_ws, size_t ws_size,
                              hipStream_t stream) {
  Params p;
  p.x = (const u16*)d_in[0]; p.wqkv = (const u16*)d_in[1]; p.wo = (const u16*)d_in[2]; p.wr = (const u16*)d_in[4];
  p.wg = (const u16*)d_in[5]; p.wu = (const u16*)d_in[6]; p.wd = (const u16*)d_in[7]; p.out = (u16*)d_out; p.ws = (char*)d_ws;
  fused_block<<<256, 512, 0, stream>>>(p);
}
```

```cpp
#include <hip/hip_runtime.h>
#include <stdint.h>

typedef unsigned short u16;
typedef unsigned long long u64;
typedef short bf16x8 __attribute__((ext_vector_type(8)));
typedef short s16x4 __attribute__((ext_vector_type(4)));
typedef float f32x2 __attribute__((ext_vector_type(2)));
typedef float f32x4 __attribute__((ext_vector_type(4)));
typedef float f32x16 __attribute__((ext_vector_type(16)));
typedef unsigned u32x2 __attribute__((ext_vector_type(2)));
typedef unsigned u32x4 __attribute__((ext_vector_type(4)));
typedef __bf16 bf2 __attribute__((ext_vector_type(2)));
#define LAS __attribute__((address_space(3)))
#define GAS __attribute__((address_space(1)))
#define DI __device__ __forceinline__

constexpr unsigned WS_GEN = 0, WS_FLAG1 = 4096, WS_FLAG2 = 131072, WS_CAND = 262144, WS_STATS = 278528, WS_PARTA = 524288,
                   WS_YPART = 1572864, WS_QKV = 2097152, WS_O = 10485760, WS_END = 13631488, WS_STAMP = 16777216;
constexpr unsigned L_WO = 0, L_KV = 102400, L_FFNA = 65536;
constexpr unsigned KVS = 3264, L_V = 1536, VPS = 576;
constexpr unsigned L_RED = L_KV, L_UPD = L_KV + 43008;
constexpr unsigned L_KEY = 154624, L_TOP = L_KEY + 32, L_CNT = L_KEY + 48  , L_KEYS = L_KEY + 64, L_STAT = L_KEY + 96, L_CS = L_STAT + 1024, L_TMP = L_CS + 2048;
constexpr unsigned L_Y = L_KV + 44032;
constexpr unsigned SPIN_CAP = 1u << 20;
constexpr unsigned ONE2 = 0x3f803f80u;
#define CODE_KB 36

constexpr int UP[20] = {0, 0, 1, 1, 2, 2, 3, 3, 4, 4, 5, 6, 6, 7, 7, 8, 8, 9, 9, 9}, UQ[18] = {0, 1, 0, 1, 0, 1, 0, 1, 0, 1, 1, 0, 1, 0, 1, 0, 1, 0};
template <int N> struct IC { static constexpr int value = N; };

struct Params { const u16 *x, *wqkv, *wo, *wr, *wg, *wu, *wd; u16* out; char* ws; };

#define ST(i) do {} while (0)

#define FST(i) do {} while (0)
#define POLLGAP 1
#define WAITV(n) asm volatile("s_waitcnt vmcnt(" #n ")" ::: "memory")
#define REQ0 3
#define LA 3
#define LAMODE 1
#define BC_AT 5
#define FORCE_FB 0
#define WO1 0
#define OPRE 4
#define TLBW 0
#define HPRIO2 1
#define ROT 3
#define KEYDELAY 200
#define PRIO 0
#define WAITN_(n) WAITV(n)
#define WAITN(n) WAITN_(n)
#define WO_PRE 6
#define BAR() do { __builtin_amdgcn_s_waitcnt(0xC07F); asm volatile("s_barrier" ::: "memory"); } while (0)
#define FENCE() __builtin_amdgcn_sched_barrier(0)
#define MFMA32(a, b, c) __builtin_amdgcn_mfma_f32_32x32x16_bf16((a), (b), (c), 0, 0, 0)
#define MFMA16(a, b, c) __builtin_amdgcn_mfma_f32_16x16x32_bf16((a), (b), (c), 0, 0, 0)

DI void glds(unsigned voff, const void* sbase, unsigned lds) {
  asm volatile("s_mov_b32 m0, %2\n\ts_nop 0\n\tglobal_load_lds_dwordx4 %0, %1" :: "v"(voff), "s"(sbase), "s"(lds) : "memory");
}
DI bf16x8 lds128(unsigned a) { return *(const LAS bf16x8*)a; }
DI s16x4 ldstr(unsigned a) { return __builtin_amdgcn_ds_read_tr16_b64_v4i16((LAS s16x4*)a); }
DI bf16x8 trfrag(unsigned a, unsigned a2) { s16x4 lo = ldstr(a), hi = ldstr(a2); return __builtin_shufflevector(lo, hi, 0, 1, 2, 3, 4, 5, 6, 7); }
DI unsigned pk(float a, float b) { f32x2 v = {a, b}; return __builtin_bit_cast(unsigned, __builtin_convertvector(v, bf2)); }
DI float rb(float a) { return __uint_as_float(pk(a, 0.f) << 16); }
DI float blo(unsigned v) { return __uint_as_float(v << 16); }
DI float bhi(unsigned v) { return __uint_as_float(v & 0xffff0000u); }
DI float dot2(unsigned a, unsigned b, float c) { return __builtin_amdgcn_fdot2_f32_bf16(__builtin_bit_cast(bf2, a), __builtin_bit_cast(bf2, b), c, false); }
DI float xhalf_sum(float v) { auto r = __builtin_amdgcn_permlane32_swap(__float_as_uint(v), __float_as_uint(v), false, false); return __uint_as_float(r[0]) + __uint_as_float(r[1]); }
DI u64 ldg64(const char* a) { return __hip_atomic_load((GAS u64*)a, __ATOMIC_RELAXED, __HIP_MEMORY_SCOPE_AGENT); }
DI void stg64(char* a, u64 v) { __hip_atomic_store((GAS u64*)a, v, __ATOMIC_RELAXED, __HIP_MEMORY_SCOPE_AGENT); }
DI u64 gran(unsigned v, unsigned tag) { return ((u64)tag << 32) | v; }
template <int CTRL> DI unsigned dpp(unsigned v) { return __builtin_amdgcn_update_dpp(0u, v, CTRL, 0xf, 0xf, true); }
DI unsigned wave_umax(unsigned v) {
  v = max(v, dpp<0x128>(v)); v = max(v, dpp<0x124>(v)); v = max(v, dpp<0x122>(v)); v = max(v, dpp<0x121>(v));
  auto r = __builtin_amdgcn_permlane16_swap(v, v, false, false); v = max(r[0], r[1]);
  auto q = __builtin_amdgcn_permlane32_swap(v, v, false, false); return max(q[0], q[1]);
}
DI float sum32(float v) {
  v += __uint_as_float(dpp<0x128>(__float_as_uint(v))); v += __uint_as_float(dpp<0x124>(__float_as_uint(v)));
  v += __uint_as_float(dpp<0x122>(__float_as_uint(v))); v += __uint_as_float(dpp<0x121>(__float_as_uint(v)));
  auto r = __builtin_amdgcn_permlane16_swap(__float_as_uint(v), __float_as_uint(v), false, false);
  return __uint_as_float(r[0]) + __uint_as_float(r[1]);
}
DI void ln_stats(float s1, float s2, float& mb, float& db) {
  float mean = s1 * (1.f / 640.f); float var = fmaxf(s2 * (1.f / 640.f) - mean * mean, 0.f);
  mb = rb(mean); db = rb(__builtin_amdgcn_sqrtf(rb(rb(var) + 1e-5f)));
}
DI void poll(const char* addr, bool act, u64 want) {
#pragma unroll 1
  for (unsigned sp = 0; sp < SPIN_CAP; ++sp) { u64 v = ldg64(addr); if (__all(!act || v == want)) break; if (POLLGAP) __builtin_amdgcn_s_sleep(POLLGAP); }
  asm volatile("" ::: "memory");
}
struct Keys { u32x4 g[4]; };
DI bool keys_top2(const Keys& k, unsigned tag, unsigned& k1, unsigned& k2) {
  unsigned bad = 0, a = 0, b = 0;
#pragma unroll
  for (int e = 0; e < 4; ++e) {
    bad |= (k.g[e][1] ^ tag) | (k.g[e][3] ^ tag);
    const unsigned hi = max(k.g[e][0], k.g[e][2]), lo = min(k.g[e][0], k.g[e][2]);
    b = max(max(b, lo), min(a, hi)); a = max(a, hi);
  }
  if (!__all(bad == 0)) return false;
  k1 = wave_umax(a); k2 = wave_umax(a == k1 ? b : a);
  k1 = __builtin_amdgcn_readfirstlane(k1); k2 = __builtin_amdgcn_readfirstlane(k2);
  return true;
}
template <class RS> DI void keys_wait(RS rs, int lane, unsigned tag, unsigned& k1, unsigned& k2) {
#pragma unroll 1
  for (unsigned sp = 0; sp < SPIN_CAP; ++sp) {
    Keys k;
    asm volatile("" ::: "memory");
#pragma unroll
    for (int e = 0; e < 4; ++e) k.g[e] = __builtin_amdgcn_raw_buffer_load_b128(rs, lane * 16 + e * 1024, WS_CAND, 16);
    if (keys_top2(k, tag, k1, k2)) break;
    __builtin_amdgcn_s_sleep(16);
  }
}
struct WoOff { unsigned r[5], l[5]; };
DI WoOff wo_offsets(int lane) {
  WoOff o;
#pragma unroll
  for (int m = 0; m < 5; ++m) {
    const unsigned L = 64 * m + lane, rho = (L * 205) >> 11, c = L - rho * 10, q = rho & 3, g = (rho >> 2) & 3, t = rho >> 4;
    o.r[m] = (24 * (g >> 1) + 8 * (g & 1) + 4 * t + q) * 1280 + c * 16; o.l[m] = (40 * g + 16 + 4 * t + q) * 1280 + c * 16;
    asm volatile("" : "+v"(o.r[m]), "+v"(o.l[m]));
  }
  return o;
}
DI void wo_steps(int s0, int s1, const WoOff& o, const u16* wo, int cs80, unsigned lds0) {
#pragma unroll 1
  for (int u = s0; u < s1; ++u) {
    const int kh = u >= 10, st = u - 10 * kh;
    const char* sp = (const char*)wo + cs80 * 160 + (kh * 320 + (st < 8 ? st * 40 : (st - 8) * 160)) * 1280;
    const unsigned d = lds0 + L_WO + u * 5120;
    if (st < 8) {
#pragma unroll
      for (int m = 0; m < 5; ++m) glds(o.r[m], sp, d + m * 1024);
    } else {
#pragma unroll
      for (int m = 0; m < 5; ++m) glds(o.l[m], sp, d + m * 1024);
    }
  }
}

__global__ __launch_bounds__(512, 2) void fused_block(Params p) {
  __shared__ __attribute__((aligned(1024))) char smem[163840];
  const unsigned lds0 = (unsigned)(size_t)smem;
  u64 pc0; asm volatile("s_getpc_b64 %0" : "=s"(pc0));
  const int tid = threadIdx.x, lane = tid & 63, w = __builtin_amdgcn_readfirstlane(tid >> 6);
  const int b = blockIdx.x, xc = b & 7, i = b >> 3;
  const bool spare = i < 2;
  const int xp = xc >> 1, odd = xc & 1;
  const int cs80 = i & 7;
  const int h = lane >> 5, l31 = lane & 31, g4 = lane >> 4, q4 = (lane & 15) >> 2, p4 = lane & 3;
  unsigned tag = 0;
  auto lds_tag = [&]() { tag = __builtin_amdgcn_readfirstlane(*(const LAS unsigned*)(lds0 + L_KEY)); };
  const unsigned j30 = (unsigned)(i - 2) & 31u, j7 = (j30 * 37) >> 8;
  const unsigned trl = j30 < 28 ? j7 : (2 * odd + j30) & 3, ct = j30 < 28 ? j30 - j7 * 7 + odd * 8 : 7;
  unsigned sn = ROT == 3 ? 3 * (trl + ct - 3 * (((trl + ct) * 43) >> 7)) + ((xc * 3) >> 3) : ROT == 2 ? 5 * ((trl + ct) & 1) + ((xc * 5) >> 3) : ROT == 1 ? (xc * 5) >> 2 : ROT == 4 ? (5 * ((trl + ct) & 3)) / 2 + (xc & 1) : 0;
  const unsigned tr = 4 * xp + trl;
  const int rb8 = 64 * xp + 32 * odd + i;
  const unsigned x0 = (lane >> 3) * 1280 + (((lane & 7) ^ g4) * 16), x1 = (8 + (lane >> 3)) * 1280 + (((lane & 7) ^ (4 + g4)) * 16);
  const unsigned w0 = g4 * 3840 + (((lane & 15) ^ (g4 << 2)) * 16), w1 = w0 + 4 * 3840;
  if (w >= 4) sn = sn == 9 ? 0 : sn + 1;
  unsigned fx = tr * (128 * 1280) + sn * 128 + (w & 3) * (32 * 1280), fw = ct * 256 + (sn * 64 + (w & 3) * 16) * 3840, fd = lds0 + (w >> 2) * 32768 + (w & 3) * 4096;
  unsigned fv[8] = {x0 + fx, w0 + fw, x1 + fx, w1 + fw, x0 + fx + 16 * 1280, w0 + fw + 8 * 3840, x1 + fx + 16 * 1280, w1 + fw + 8 * 3840};
  sn = sn == 9 ? 0 : sn + 1;
  asm volatile("" : "+v"(fv[0]), "+v"(fv[1]), "+v"(fv[2]), "+v"(fv[3]), "+v"(fv[4]), "+v"(fv[5]), "+v"(fv[6]), "+v"(fv[7]));
  asm volatile("" : "+s"(fd), "+s"(sn));
  FENCE();
  if (!spare) {
#pragma unroll
    for (int e = 0; e < 8; ++e) glds(fv[e], e & 1 ? (const void*)p.wqkv : (const void*)p.x, fd + (e & 1) * 16384 + (e >> 1) * 1024);
  }
  FST(28);
  char* const ws = p.ws;
  const auto rs = __builtin_amdgcn_make_buffer_rsrc(ws, 0, WS_END, 0x00020000);
  const char* const xt = (const char*)p.x + tr * (128 * 1280);
  const char* const wt = (const char*)p.wqkv + ct * 256;
  auto pieces = [&](unsigned e, unsigned slot) {
    const char* xs = xt + (sn * 128 + e * (16 * 1280)); const char* wsb = wt + (sn * 64 + e * 8) * 3840; const unsigned d = lds0 + slot * 32768 + e * 2048;
    glds(x0, xs, d); glds(w0, wsb, d + 16384); glds(x1, xs, d + 1024); glds(w1, wsb, d + 16384 + 1024);
  };

  const int rrow = 8 * rb8 + 2 * w + h;
  u32x4 rx[3], rw[3]; unsigned pfv = 0, genv; float a1 = 0.f, a2 = 0.f, ar = 0.f, sw = 0.f;
  auto router_load = [&]() {
    if (CODE_KB && lane < CODE_KB / 2) asm volatile("global_load_dword %0, %1, %2" : "+v"(pfv) : "v"(((i & 3) * 4 + w) * (CODE_KB * 64) + lane * 128), "s"(pc0) : "memory");
    const char* xr = (const char*)p.x + rrow * 1280 + l31 * 16; const char* wr = (const char*)p.wr + l31 * 16;
    const int t3 = l31 < 16 ? 1024 : 0;
    rx[0] = *(const GAS u32x4*)xr; rx[1] = *(const GAS u32x4*)(xr + 512); rw[0] = *(const GAS u32x4*)wr; rw[1] = *(const GAS u32x4*)(wr + 512);
    rx[2] = *(const GAS u32x4*)(xr + t3); rw[2] = *(const GAS u32x4*)(wr + t3);
    genv = __hip_atomic_load((GAS unsigned*)(ws + WS_GEN), __ATOMIC_RELAXED, __HIP_MEMORY_SCOPE_AGENT);
    FENCE();
  };
  auto router_dots = [&]() {
    if (l31 >= 16) rx[2] = rw[2] = (u32x4){};
#pragma unroll
    for (int e = 0; e < 3; ++e)
#pragma unroll
      for (int c = 0; c < 4; ++c) { a1 = dot2(rx[e][c], ONE2, a1); a2 = dot2(rx[e][c], rx[e][c], a2); ar = dot2(rx[e][c], rw[e][c], ar); sw = dot2(rw[e][c], ONE2, sw); }
    tag = __builtin_amdgcn_readfirstlane(genv) + 1u;
    if (tid == 0) *(LAS unsigned*)(lds0 + L_KEY) = tag;
    asm volatile("" : "+v"(a1), "+v"(a2), "+v"(ar), "+v"(sw) : "v"(pfv));
  };
  float rmb = 0.f, rdb = 0.f; unsigned rk = 0, rv = 0;
  auto router_step = [&](int k) {
    auto fdpp = [&](float& v, int st) { const unsigned u = __float_as_uint(v); v += __uint_as_float(st == 0 ? dpp<0x128>(u) : st == 1 ? dpp<0x124>(u) : st == 2 ? dpp<0x122>(u) : dpp<0x121>(u)); };
    auto fswp = [&](float& v) { auto r = __builtin_amdgcn_permlane16_swap(__float_as_uint(v), __float_as_uint(v), false, false); v = __uint_as_float(r[0]) + __uint_as_float(r[1]); };
    if (k < 4) { fdpp(a1, k); fdpp(a2, k); fdpp(ar, k); fdpp(sw, k); }
    if (k == 4) { fswp(a1); fswp(a2); }
    if (k == 5) { fswp(ar); fswp(sw); }
    if (k == 6) { a1 *= 1.f / 640.f; a2 = fmaxf(a2 * (1.f / 640.f) - a1 * a1, 0.f); }
    if (k == 7) { rmb = rb(a1); a2 = rb(a2) + 1e-5f; }
    if (k == 8) { a2 = __builtin_amdgcn_sqrtf(rb(a2)); ar -= rmb * sw; }
    if (k == 9) { rdb = rb(a2); a2 = __builtin_amdgcn_rcpf(rdb); }
    if (k == 10) { rk = pk(ar * a2, 0.f) & 0xffffu; rv = (__float_as_uint(rmb) >> 16) | __float_as_uint(rdb); }
    if (k == 11) rk = (rk & 0x8000u ? (~rk & 0xffffu) : (rk | 0x8000u)) << 16;
    if (k == 12) { if (l31 == 0) *(LAS unsigned*)(lds0 + L_KEYS + (2 * w + h) * 4) = rk | (2047u - rrow); }
    if (k == 13) { if (l31 == 0) __builtin_amdgcn_raw_buffer_store_b64((u32x2){rv, tag}, rs, WS_STATS + rrow * 8, 0, 16); }
    auto max8 = [&](unsigned& v, int st) { v = max(v, st == 0 ? dpp<0xB1>(v) : st == 1 ? dpp<0x4E>(v) : dpp<0x141>(v)); };
    if (k == 14) rk = rv = *(const LAS unsigned*)(lds0 + L_KEYS + (lane & 7) * 4);
    if (k >= 16 && k < 19) max8(rv, k - 16);
    if (k == 19) rk = rk == rv ? 0u : rk;
    if (k >= 20 && k < 23) max8(rk, k - 20);
    if (k == 23) { if (w == 0 && lane < 2) __builtin_amdgcn_raw_buffer_store_b64((u32x2){lane ? rk : rv, tag}, rs, WS_CAND + (2 * rb8 + lane) * 8, 0, 16); }
    asm volatile("" : "+v"(a1), "+v"(a2), "+v"(ar), "+v"(sw), "+v"(rmb), "+v"(rdb), "+v"(rk), "+v"(rv));
  };

  bf16x8 wdf[30];
  if (!spare) {
    if (w >= 4) {
      const int j = w - 4;
      auto issue = [&](int slot) { pieces(2 * j, slot); pieces(2 * j + 1, slot); sn = sn == 9 ? 0 : sn + 1; };
      if (tid == 384) *(LAS u32x4*)(lds0 + L_CNT) = (u32x4){};
      const unsigned au = tr * 15 + ct, cg = (au * 205) >> 11, kg = au - cg * 10;
      FST(31); BAR();
      FST(1); issue(2); FST(2);
      WAITV(8); FST(3); BAR();
      issue(3); pieces(2 * j, 0);
      WAITV(12); BAR();
      pieces(2 * j + 1, 0); sn = sn == 9 ? 0 : sn + 1; issue(1);
      WAITV(16); BAR();
#pragma unroll 1
      for (int s = 6; s < 9; ++s) { FST(3 * s - 8); issue(s & 3); FST(3 * s - 7); WAITV(16); FST(3 * s - 6); BAR(); }
      lds_tag();
      auto ffn_dma = [&]() {
        const unsigned fo = (lane >> 3) * 3072 + (lane & 7) * 16;
        const char* fb = (const char*)(j < 2 ? p.wg : p.wu) + (kg * 64 + 32 * (j & 1)) * 3072 + cg * 128;
#pragma unroll
        for (int e = 0; e < 4; ++e) glds(fo, fb + e * 8 * 3072, lds0 + L_FFNA + (4 * j + e) * 1024);
      };
      auto ffn_landed = [&]() { WAITV(0); if (lane == 0) __hip_atomic_fetch_add((LAS unsigned*)(lds0 + L_CNT + 12), 1u, __ATOMIC_RELAXED, __HIP_MEMORY_SCOPE_WORKGROUP); };
      if (w == 6) {
        const char* ga = ws + WS_STATS + tr * 1024;
        u32x2 g0, g1;
        asm volatile("global_load_dwordx2 %0, %2, %3 sc1\n\tglobal_load_dwordx2 %1, %2, %3 offset:512 sc1" : "=&v"(g0), "=&v"(g1) : "v"(lane * 8), "s"(ga) : "memory");
        issue(1); WAITV(16); BAR();
        ffn_dma();
        asm volatile("s_waitcnt vmcnt(12)\n\ts_barrier" : "+v"(g0), "+v"(g1) :: "memory");
        if (!__all(g0[1] == tag && g1[1] == tag)) {
#pragma unroll 1
          for (unsigned sp = 0; sp < SPIN_CAP; ++sp) {
            g0 = __builtin_bit_cast(u32x2, ldg64(ga + lane * 8)); g1 = __builtin_bit_cast(u32x2, ldg64(ga + 512 + lane * 8));
            if (__all(g0[1] == tag && g1[1] == tag)) break; }
        }
        *(LAS f32x2*)(lds0 + L_STAT + lane * 8) = (f32x2){blo(g0[0]), __builtin_amdgcn_rcpf(bhi(g0[0]))};
        *(LAS f32x2*)(lds0 + L_STAT + 512 + lane * 8) = (f32x2){blo(g1[0]), __builtin_amdgcn_rcpf(bhi(g1[0]))};
        FST(23); WAITV(4); BAR();
        ffn_landed();
      } else if (w < 6) { issue(1); WAITV(16); BAR(); ffn_dma(); WAITV(12); BAR(); WAITV(4); BAR(); ffn_landed(); }
      else {
        Keys kk;
        asm volatile("buffer_load_dwordx4 %0, %4, %5, %6 offen sc1\n\tbuffer_load_dwordx4 %1, %4, %5, %6 offen offset:1024 sc1\n\t"
                     "buffer_load_dwordx4 %2, %4, %5, %6 offen offset:2048 sc1\n\tbuffer_load_dwordx4 %3, %4, %5, %6 offen offset:3072 sc1"
                     : "=&v"(kk.g[0]), "=&v"(kk.g[1]), "=&v"(kk.g[2]), "=&v"(kk.g[3]) : "v"(lane * 16), "s"(rs), "s"(WS_CAND) : "memory");
        issue(1); WAITV(16); BAR();
        ffn_dma();
        asm volatile("s_waitcnt vmcnt(12)\n\ts_barrier" : "+v"(kk.g[0]), "+v"(kk.g[1]), "+v"(kk.g[2]), "+v"(kk.g[3]) :: "memory");
        unsigned k1 = 0, k2 = 0;
        const bool early = keys_top2(kk, tag, k1, k2);
        u32x4 xc0 = {ONE2, ONE2, ONE2, ONE2}, xc1 = xc0;
        { const char* xq = (const char*)p.x + ((2047u - (lane & 1 ? k2 : k1)) & 2047u) * 1280 + kg * 128 + g4 * 16;
          if ((lane & 15) < 2) asm volatile("global_load_dwordx4 %0, %2, off\n\tglobal_load_dwordx4 %1, %2, off offset:64" : "+v"(xc0), "+v"(xc1) : "v"(xq) : "memory"); }
        asm volatile("s_waitcnt vmcnt(6)\n\ts_barrier\n\ts_waitcnt vmcnt(0)" : "+v"(xc0), "+v"(xc1) :: "memory");
        FST(23);
        if (!early) {
          keys_wait(rs, lane, tag, k1, k2);
          if ((lane & 15) < 2) { const char* xq = (const char*)p.x + ((2047u - (lane & 1 ? k2 : k1)) & 2047u) * 1280 + kg * 128 + g4 * 16; xc0 = *(const GAS u32x4*)xq; xc1 = *(const GAS u32x4*)(xq + 64); }
        }
        if (b == 16 && lane == 0) __hip_atomic_store((GAS unsigned*)(ws + WS_GEN), tag, __ATOMIC_RELAXED, __HIP_MEMORY_SCOPE_AGENT);
        if (lane == 0) *(LAS u32x2*)(lds0 + L_TOP) = (u32x2){k1, k2};
#pragma unroll 1
        for (unsigned sp = 0; sp < SPIN_CAP; ++sp) { if (__builtin_amdgcn_readfirstlane(*(volatile LAS unsigned*)(lds0 + L_CNT + 12)) >= 3u) break; __builtin_amdgcn_s_sleep(1); }
        asm volatile("" ::: "memory");
#pragma unroll
        for (int n = 0; n < 8; ++n) {
          const unsigned a = lds0 + L_FFNA + (n >> 2) * 8192 + (8 * g4 + q4) * 128 + (n & 3) * 32 + 8 * p4;
          const f32x4 y = MFMA16(trfrag(a + 4096, a + 4608), __builtin_bit_cast(bf16x8, xc1), MFMA16(trfrag(a, a + 512), __builtin_bit_cast(bf16x8, xc0), (f32x4){}));
          if ((lane & 15) < 3) *(LAS f32x4*)(lds0 + L_TMP + (((n >> 2) * 3 + (lane & 15)) * 64 + 16 * (n & 3) + 4 * g4) * 4) = y;
        }
#pragma unroll
        for (int n = 0; n < 6; ++n) stg64(ws + WS_PARTA + (au * 384 + n * 64 + lane) * 8, gran(*(const LAS unsigned*)(lds0 + L_TMP + (n * 64 + lane) * 4), tag));
        FST(25);
      }
    } else {
      router_load();
      const int fh = w >> 1, th = w & 1;
      unsigned xa[4], wa[2], ko[4];
#pragma unroll
      for (int e = 0; e < 4; ++e) { const int ks = (2 * th + e) & 3, row = 64 * th + l31; xa[e] = lds0 + row * 128 + (((2 * ks + h) ^ ((row >> 1) & 7)) * 16); ko[e] = ks * 4096; }
#pragma unroll
      for (int f = 0; f < 2; ++f) wa[f] = lds0 + 16384 + (8 * h + q4) * 256 + (((4 * (2 * fh + f) + 2 * (g4 & 1) + (p4 >> 1)) ^ (q4 << 2)) * 16) + 8 * (p4 & 1);
      f32x16 acc[2][2] = {};
      f32x4 cs0 = {}, cs1 = {};
      const unsigned selw = (lane & 15) == (g4 & 1) ? ONE2 : 0u;
      const bf16x8 sel = __builtin_bit_cast(bf16x8, (u32x4){selw, selw, selw, selw});
      struct Fr { bf16x8 v[4]; };
      Fr FA[4], FB[4];
      auto rd = [&](Fr* F, int n, unsigned so) {
        const int e = n >> 2, j = n & 3;
        if (j < 2) { const unsigned a = wa[j] + (so + ko[e]); F[e].v[j] = trfrag(a, a + 1024); } else F[e].v[j] = lds128(xa[e] + so + (j - 2) * 4096);
      };
      auto colsum = [&](const Fr* C, int e) { cs0 = MFMA16(C[e].v[0], sel, cs0); cs1 = MFMA16(C[e].v[1], sel, cs1); };
      bf16x8 ax[2], bx[2]; float rr[2], c0, c1; f32x2 st0, st1; float cp[4];
      const float qs = ct < 5 ? 0.22360679774997896f * 1.4426950408889634f : 1.f;
      const unsigned ca = lds0 + L_CS + fh * 512 + l31 * 4, cown = lds0 + L_CS + w * 256 + ((lane & 15) * 16 + 4 * g4) * 4;
      auto prep = [&](int n) {
        if (n == 12) { cp[0] = *(const LAS float*)ca; cp[1] = *(const LAS float*)(ca + 256); cp[2] = *(const LAS float*)(ca + 128); cp[3] = *(const LAS float*)(ca + 384); }
        if (n == 13) { st0 = *(const LAS f32x2*)(lds0 + L_STAT + (64 * th + l31) * 8); st1 = *(const LAS f32x2*)(lds0 + L_STAT + (64 * th + 32 + l31) * 8); }
      };
      auto body = [&](Fr* N, const Fr* C, unsigned so, int r0 = 99) {
#pragma unroll
        for (int n = 0; n < 16; ++n) {
          const int e = n >> 2, f = (n >> 1) & 1, t = n & 1;
          acc[f][t] = MFMA32(C[e].v[f], C[e].v[2 + t], acc[f][t]);
          if (n < 4) { rd(N, 2 * n, so); rd(N, 2 * n + 1, so); } else if (n < 12) rd(N, n + 4, so);
          if (n >= 2 && r0 + n - 2 < 24) router_step(r0 + n - 2);
          if (r0 == 100) prep(n);
          FENCE();
        }
        if (r0 == 100) { cs0 = cs1 = (f32x4){}; colsum(N, 2); colsum(N, 3); }
        colsum(N, 0); colsum(N, 1);
        FENCE();
      };
      FST(29);
      if (CODE_KB) WAITV(8); else WAITV(7);
      FST(30); BAR(); ST(1); FST(31);
#pragma unroll
      for (int n = 0; n < 16; ++n) rd(FA, n, 0);
      FST(32); router_dots(); FST(33);
      colsum(FA, 0); colsum(FA, 1);
      BAR();
#pragma unroll
      for (int s = 1; s <= 7; s += 2) {
        FST(2 * s); body(FB, FA, (s & 3) * 32768, s == 1 ? 0 : 99); FST(2 * s + 1); BAR();
        FST(2 * s + 2); body(FA, FB, ((s + 1) & 3) * 32768, s == 1 ? 14 : 99);
        if (s == 7 && (lane & 15) < 2) { *(LAS f32x4*)cown = cs0; *(LAS f32x4*)(cown + 128) = cs1; }
        FST(2 * s + 3); BAR();
      }
      FST(18);
      body(FB, FA, 32768, 100);
      FST(19);
      const unsigned qo = WS_QKV + (4 * tr + 2 * th) * 122880 + (16 * ct + 8 * fh + h) * 512 + l31 * 16;
      unsigned pw[8];
      auto split3 = [&](float c) {
        const float hi = rb(c), r1 = c - hi, mid = rb(r1);
        return __builtin_bit_cast(bf16x8, (u32x4){h ? 0u : pk(hi, mid), h ? 0u : pk(r1 - mid, 0.f), 0u, 0u});
      };
      auto epi = [&](int tile, int part) {
        const int f = tile & 1, t = tile >> 1;
#pragma unroll
        for (int g = 2 * part; g < 2 * part + 2; ++g) {
          float v0 = acc[f][t][2 * g] * rr[t]; asm("" : "+v"(v0));
          pw[g] = pk(v0, acc[f][t][2 * g + 1] * rr[t]);
        }
        if (part & 1) {
          const int g = part >> 1;
          auto r0 = __builtin_amdgcn_permlane32_swap(pw[4 * g], pw[4 * g + 2], false, false);
          auto r1 = __builtin_amdgcn_permlane32_swap(pw[4 * g + 1], pw[4 * g + 3], false, false);
          __builtin_amdgcn_raw_buffer_store_b128((u32x4){r0[0], r1[0], r0[1], r1[1]}, rs, qo + t * 122880 + (4 * f + 2 * g) * 512, 0, 16);
        }
      };
#pragma unroll
      for (int n = 0; n < 20; ++n) {
        const int tile = n < 8 ? n >> 2 : n < 10 ? n - 8 : n < 15 ? 2 : 3, e = n < 8 ? n & 3 : n < 10 ? 4 : (n - 10) % 5, f = tile & 1, t = tile >> 1;
        acc[f][t] = e < 4 ? MFMA32(FB[e].v[f], FB[e].v[2 + t], acc[f][t]) : MFMA32(ax[f], bx[t], acc[f][t]);
        if (n == 0) { const unsigned m = pk(-st0[0], -st0[0]); bx[0] = __builtin_bit_cast(bf16x8, (u32x4){m, m, 0u, 0u}); rr[0] = st0[1] * qs; c0 = cp[0] + cp[1]; asm volatile("" : "+v"(bx[0]), "+v"(rr[0]), "+v"(c0)); }
        if (n == 1) { const unsigned m = pk(-st1[0], -st1[0]); bx[1] = __builtin_bit_cast(bf16x8, (u32x4){m, m, 0u, 0u}); rr[1] = st1[1] * qs; c1 = cp[2] + cp[3]; asm volatile("" : "+v"(bx[1]), "+v"(rr[1]), "+v"(c1)); }
        if (n == 1 && (lane & 15) < 2) { *(LAS f32x4*)(cown + 1024) = cs0; *(LAS f32x4*)(cown + 1024 + 128) = cs1; }
        if (n == 2) { cp[0] = *(const LAS float*)(lds0 + L_CS + 1024 + w * 256 + l31 * 4); cp[1] = *(const LAS float*)(lds0 + L_CS + 1024 + w * 256 + 128 + l31 * 4); }
        if (n == 5) { ax[0] = split3(c0 + cp[0]); asm volatile("" : "+v"(ax[0])); }
        if (n == 6) { ax[1] = split3(c1 + cp[1]); asm volatile("" : "+v"(ax[1])); }
        if (n >= 10) epi((n - 10) >> 2, (n - 10) & 3);
        FENCE();
        if (n == 9 || n == 13 || n == 17) FST(24 + (n - 9) / 4);
      }
      ST(2); FST(20);
      epi(2, 2); epi(2, 3);
#pragma unroll
      for (int part = 0; part < 4; ++part) epi(3, part);
      FST(21);
      WAITV(0);
      if (lane == 0 && __hip_atomic_fetch_add((LAS unsigned*)(lds0 + L_CNT), 1u, __ATOMIC_RELAXED, __HIP_MEMORY_SCOPE_WORKGROUP) == 3u)
        stg64(ws + WS_FLAG1 + (tr * 16 + ct) * 256, gran(tag, ~tag));
      ST(3); FST(22);
    }
  } else {
    if (w < 4) router_load();
#pragma unroll 1
    for (int e = 0; e < 15; ++e) {
      const unsigned L = 512 * e + tid, r = (L * 52429) >> 22, pc = L - r * 80;
      glds(r * 1280 + (pc ^ (((r & 3) | ((r >> 1) & 4)) << 1)) * 16, (const char*)p.wd + (2 * xc + i) * 96 * 1280, lds0 + (8 * e + w) * 1024);
    }
    if (w < 4) {
      router_dots();
#pragma unroll
      for (int k = 0; k < 14; ++k) router_step(k);
    }
    if (tid == 384) *(LAS u32x4*)(lds0 + L_CNT) = (u32x4){};
    BAR();
    if (w == 0) {
#pragma unroll
      for (int k = 14; k < 24; ++k) router_step(k);
    }
    WAITV(0); BAR(); ST(1);
    if (w >= 4) {
      lds_tag();
#pragma unroll
      for (int n = 0; n < 30; ++n) {
        const unsigned a = lds0 + (8 * g4 + q4) * 1280 + (((2 * (w - 4) + n / 15) ^ (q4 | ((g4 & 1) << 2))) * 32) + 8 * p4 + (n % 15 / 5) * 40960 + (n % 5) * 256;
        wdf[n] = trfrag(a, a + 5120);
      }
    } else if (w == 0) {
      unsigned k1 = 0, k2 = 0;
      { const u64 t0 = __builtin_amdgcn_s_memrealtime() ; while (__builtin_amdgcn_s_memrealtime() - t0 < KEYDELAY) __builtin_amdgcn_s_sleep(8); }
      keys_wait(rs, lane, tag, k1, k2);
      const char* sa = ws + WS_STATS + ((2047u - (lane & 1 ? k2 : k1)) & 2047u) * 8;
      u64 sg = ldg64(sa);
#pragma unroll 1
      for (unsigned sp = 0; sp < SPIN_CAP && !__all((unsigned)(sg >> 32) == tag); ++sp) sg = ldg64(sa);
      if (lane < 2) { *(LAS unsigned*)(lds0 + L_TOP + lane * 4) = lane ? k2 : k1; *(LAS unsigned*)(lds0 + L_TOP + 8 + lane * 4) = (unsigned)sg; }
    }
    BAR(); ST(2);
  }
  ST(4); FST(35);
  const u64 ftag = gran(tag, ~tag);

  const int tg3 = w & 3, kh = w >> 2, tok0 = 256 * xc + 64 * (i >> 3), n0 = 80 * cs80;
  const bool two = w == 4 || w == 5;
  const unsigned it0 = tid, it1 = two ? tid + 256 : tid, tk0 = (it0 * 6554) >> 16, tk1 = (it1 * 6554) >> 16, ch0 = it0 - 10 * tk0, ch1 = it1 - 10 * tk1;
  const unsigned eo0 = (tok0 + tk0) * 1280 + n0 * 2 + ch0 * 16, eo1 = (tok0 + tk1) * 1280 + n0 * 2 + ch1 * 16;
  u32x4 xv0, xv1; u64 sg0_, sg1_;
  auto pre3 = [&]() {
    xv0 = *(const GAS u32x4*)((const char*)p.x + eo0); sg0_ = ldg64(ws + WS_STATS + (tok0 + tk0) * 8);
    xv1 = *(const GAS u32x4*)((const char*)p.x + eo1); sg1_ = ldg64(ws + WS_STATS + (tok0 + tk1) * 8);
  };

  {
    const int R = i >> 2, hd = 4 * xc + (i & 3), p0 = (5 * hd) >> 1, oh = hd & 1;
    const int pf = p0 + oh, pp = oh ? p0 : p0 + 2, cc = (lane & 1 ? p0 + 2 : p0) >> 4;
    if (w < 4) {
      const int lo = w < 2, tu = 2 * w + 1 - lo, tv = 2 * w + lo;
      poll(ws + WS_FLAG1 + ((2 * R + (w >> 1)) * 16 + cc) * 256, lane < 2, ftag);
      ST(5); FST(36);
      bf16x8 qf[2][2];
      { const unsigned a0 = (pf + h) * 512 + l31 * 16, a1 = pp * 512 + l31 * 16 + oh * 8, ou = WS_QKV + (8 * R + tu) * 122880, ov = WS_QKV + (8 * R + tv) * 122880;
        const u32x4 u0 = __builtin_amdgcn_raw_buffer_load_b128(rs, a0, ou, 16), v0 = __builtin_amdgcn_raw_buffer_load_b128(rs, a0, ov, 16);
        const u32x2 u1 = __builtin_amdgcn_raw_buffer_load_b64(rs, a1, ou, 16), v1 = __builtin_amdgcn_raw_buffer_load_b64(rs, a1, ov, 16);
        qf[0][0] = __builtin_bit_cast(bf16x8, u0); qf[0][1] = __builtin_bit_cast(bf16x8, (u32x4){h ? 0u : u1[0], h ? 0u : u1[1], 0u, 0u});
        qf[1][0] = __builtin_bit_cast(bf16x8, v0); qf[1][1] = __builtin_bit_cast(bf16x8, (u32x4){h ? 0u : v1[0], h ? 0u : v1[1], 0u, 0u}); }
      pre3();
      f32x16 cz = {}, ca, cb;
#pragma unroll
      for (int r = 0; r < 16; ++r) {
        const bool le = (r & 3) + 8 * (r >> 2) + 4 * h <= l31;
        const float d = le ? 0.f : -1e30f, f = le || R == 0 ? -1e30f : 0.f;
        ca[r] = lo ? d : f; cb[r] = lo ? f : d;
      }
      const unsigned ka = lds0 + L_KV + h * 512 + l31 * 16;
      const int pi = min(4 * (g4 & 1) + p4, 5);
      const unsigned va = lds0 + L_KV + L_V + (pi >> 1) * VPS + (pi & 1) * 8 + (4 * h + q4) * 16;
      struct KF { bf16x8 a, b; };
      auto kread = [&](int kt) {
        const unsigned a = ka + kt * KVS;
        return KF{lds128(a), lds128(a + 1024)};
      };
      auto vread = [&](int kt) { const unsigned v = va + kt * KVS; return KF{trfrag(v, v + 128), trfrag(v + 256, v + 384)}; };
      auto ex = [&](const f32x16& s, int e) { return pk(__builtin_amdgcn_exp2f(s[2 * e]), __builtin_amdgcn_exp2f(s[2 * e + 1])); };
      const int P0 = lo ? 6 : 8, P1 = w == 0 ? 4 : w == 1 ? 8 : w == 2 ? 6 : 10, P2 = lo ? 2 + 2 * w : 6 + 2 * w;
      const int tp[10] = {P0, P0 + 1, P1, P1 + 1, lo ? 2 * w + 8 : 2 * w + 1, lo ? 2 * w + 9 : 2 * w, P2, P2 + 1, lo ? 2 * w + 1 : 2 * w + 8, lo ? 2 * w : 2 * w + 9};
      auto at = [&](int ps) { const int e = (ps == 1 || ps == 3 || ps == 7), t = tp[ps - e]; return (R == 0 && t < 8 ? 8 : t) + e; };
      auto setz = [&](int ps) {
        if (ps == 1 || ps == 3 || ps == 5 || ps == 7 || ps == 9) return;
        if (R == 0) { const float z = tp[ps] < 8 ? -1e30f : 0.f;
#pragma unroll
          for (int r = 0; r < 16; ++r) cz[r] = z;
          asm volatile("" : "+v"(cz)); } };
      setz(0);
      asm volatile("" : "+v"(ca), "+v"(cb), "+v"(cz), "+v"(qf[0][0]), "+v"(qf[0][1]), "+v"(qf[1][0]), "+v"(qf[1][1]));
      BAR(); ST(6); FST(39);
      KF K0, K1, K2, V0, V1, V2; f32x16 S0, S1, o[2] = {};
      K0 = kread(at(0)); V0 = vread(at(0));
      S0 = MFMA32(K0.b, qf[0][1], MFMA32(K0.a, qf[0][0], cz));
      u32x4 pa = {}, pb = {}; float x0, x1, x2, x3;
#define SET3(N, p) ((p) % 3 == 0 ? N##0 : (p) % 3 == 1 ? N##1 : N##2)
#define EXP4(s, e) do { x0 = __builtin_amdgcn_exp2f(s[4 * e]); x1 = __builtin_amdgcn_exp2f(s[4 * e + 1]); x2 = __builtin_amdgcn_exp2f(s[4 * e + 2]); x3 = __builtin_amdgcn_exp2f(s[4 * e + 3]); \
                        asm volatile("" : "+v"(x0), "+v"(x1), "+v"(x2), "+v"(x3)); FENCE(); } while (0)
#define PACK2(d, e) do { d[e] = pk(x0, x1); d[e + 1] = pk(x2, x3); asm volatile("" : "+v"(d)); FENCE(); } while (0)
      auto unit = [&](auto U, f32x16& sc, f32x16& sn) {
        constexpr int u = decltype(U)::value, np = u + 1 < 18 ? UP[u + 1] : 0, nq = u + 1 < 18 ? UQ[u + 1] : 0, pu = u > 0 ? u - 1 : 0;
        if constexpr (u == 2 || u == BC_AT) BAR();
        FST(40 + u);
        if constexpr (u + 1 < 18) { if constexpr (np != UP[u]) setz(np); sn = MFMA32(SET3(K, np).a, qf[nq][0], u + 1 == 8 || u + 1 == 10 ? ca : u + 1 >= 16 ? cb : cz); FENCE(); }
        if constexpr (u > 0) PACK2(pb, 2);
        EXP4(sc, 0);
        if constexpr (u > 0) { o[UQ[pu]] = MFMA32(SET3(V, UP[pu]).b, __builtin_bit_cast(bf16x8, pb), o[UQ[pu]]); FENCE(); }
        if constexpr (u + 2 < 18 && UP[u + 2] != UP[u + 1]) SET3(K, UP[u + 2]) = kread(at(UP[u + 2]));
        if constexpr (u + 1 < 18 && np != UP[u]) SET3(V, np) = vread(at(np));
        PACK2(pa, 0);
        EXP4(sc, 1);
        if constexpr (u + 1 < 18) { sn = MFMA32(SET3(K, np).b, qf[nq][1], sn); FENCE(); }
        PACK2(pa, 2);
        EXP4(sc, 2);
        o[UQ[u]] = MFMA32(SET3(V, UP[u]).a, __builtin_bit_cast(bf16x8, pa), o[UQ[u]]); FENCE();
        PACK2(pb, 0);
        EXP4(sc, 3);
      };
#define UNIT2(n) unit(IC<n>{}, S0, S1); unit(IC<n + 1>{}, S1, S0);
      UNIT2(0) UNIT2(2) UNIT2(4) UNIT2(6) UNIT2(8) UNIT2(10) UNIT2(12) UNIT2(14) UNIT2(16)
      PACK2(pb, 2);
      o[0] = MFMA32(V0.b, __builtin_bit_cast(bf16x8, pb), o[0]);
      auto rowsum = [&](const f32x16& a) { auto r = __builtin_amdgcn_permlane32_swap(__float_as_uint(a[8]), __float_as_uint(a[8]), false, false); return __uint_as_float(r[1]); };
      float l[2] = {rowsum(o[1]), rowsum(o[0])};
      if (FORCE_FB || !__all(l[0] > 0x1p-60f && l[0] < 0x1p60f && l[1] > 0x1p-60f && l[1] < 0x1p60f)) {
        int qt = tu;
#pragma unroll 1
        for (int e = 0; e < 2; ++e) {
          const int k0 = R > 0 ? qt : 8;
          auto score = [&](int kt, float base) {
            f32x16 c;
#pragma unroll
            for (int r = 0; r < 16; ++r) { const bool le = (r & 3) + 8 * (r >> 2) + 4 * h <= l31; c[r] = (kt == qt + 8 && !le) || (kt == qt && le) ? -1e30f : base; }
            const KF k = kread(kt);
            return MFMA32(k.b, qf[0][1], MFMA32(k.a, qf[0][0], c));
          };
          float m = -3e38f;
#pragma unroll 1
          for (int kt = k0; kt <= qt + 8; ++kt) { const f32x16 s = score(kt, 0.f);
#pragma unroll
            for (int r = 0; r < 16; ++r) m = fmaxf(m, s[r]); }
          { auto r = __builtin_amdgcn_permlane32_swap(__float_as_uint(m), __float_as_uint(m), false, false); m = fmaxf(__uint_as_float(r[0]), __uint_as_float(r[1])); }
          o[0] = (f32x16){};
#pragma unroll 1
          for (int kt = k0; kt <= qt + 8; ++kt) {
            const f32x16 s = score(kt, -m); const KF v = vread(kt);
            o[0] = MFMA32(v.a, __builtin_bit_cast(bf16x8, (u32x4){ex(s, 0), ex(s, 1), ex(s, 2), ex(s, 3)}), o[0]);
            o[0] = MFMA32(v.b, __builtin_bit_cast(bf16x8, (u32x4){ex(s, 4), ex(s, 5), ex(s, 6), ex(s, 7)}), o[0]);
          }
          const f32x16 to = o[0]; o[0] = o[1]; o[1] = to;
          const bf16x8 t0 = qf[0][0], t1 = qf[0][1]; qf[0][0] = qf[1][0]; qf[0][1] = qf[1][1]; qf[1][0] = t0; qf[1][1] = t1;
          qt = tv;
        }
        l[0] = rowsum(o[1]); l[1] = rowsum(o[0]);
      }
      ST(7); FST(59);
#pragma unroll
      for (int e = 0; e < 2; ++e) {
        const f32x16& a = o[1 - e]; const float ri = __builtin_amdgcn_rcpf(l[e]);
        const unsigned e0 = pk(a[0] * ri, a[1] * ri), e1 = pk(a[2] * ri, a[3] * ri), e2 = pk(a[4] * ri, a[5] * ri), e3 = pk(a[6] * ri, a[7] * ri);
        const u32x2 el = {pk(a[8] * ri, a[9] * ri), pk(a[10] * ri, a[11] * ri)};
        auto r0 = __builtin_amdgcn_permlane32_swap(e0, e2, false, false), r1 = __builtin_amdgcn_permlane32_swap(e1, e3, false, false);
        const unsigned ob = WS_O + (16 * R + 2 * (e ? tu : tv) + (l31 >> 4)) * 20480;
        __builtin_amdgcn_raw_buffer_store_b128((u32x4){r0[0], r1[0], r0[1], r1[1]}, rs, ob + (2 * hd + h) * 256 + (lane & 15) * 16, 0, 16);
        if (h == 0) __builtin_amdgcn_raw_buffer_store_b64(el, rs, ob + 16384 + hd * 128 + (lane & 15) * 8, 0, 16);
      }
      WAITV(0);
      if (lane == 0 && __hip_atomic_fetch_add((LAS unsigned*)(lds0 + L_CNT + 4), 1u, __ATOMIC_RELAXED, __HIP_MEMORY_SCOPE_WORKGROUP) == 3u)
        stg64(ws + WS_FLAG2 + (R * 32 + hd) * 256, ftag);
      ST(8); FST(60);
      BAR();
    } else {
      __builtin_amdgcn_s_setprio(2);
      const int j = w - 4;
      const WoOff woo = wo_offsets(lane);
    const int sp_ = 2 * xc + i;
    unsigned ga[3]; u32x2 pa_[30];
    auto gather = [&]() {
      asm volatile("" ::: "memory");
#pragma unroll
      for (int n = 0; n < 30; ++n) pa_[n] = __builtin_amdgcn_raw_buffer_load_b64(rs, ga[n / 10], WS_PARTA + (n % 10) * 3072, 16);
    };
#pragma unroll
      for (int r = 0; r < 3; ++r) { const unsigned e = min(144 * j + 64 * r + lane, 575), rc = (e * 683) >> 16, F = 96 * sp_ + e - 96 * rc; ga[r] = ((F >> 6) * 3840 + rc * 64 + (F & 63)) * 8; asm volatile("" : "+v"(ga[r])); }
      const int kt[4] = {6 + j, j < 2 ? 4 + j : 8 + j, j, 12 + j};
      { const int e = lane >> 2, T = 8 * R - 8 + (e == 0 ? kt[0] : e == 1 ? kt[1] : e == 2 ? kt[2] : kt[3]);
        const bool act = lane < 16 && T >= 0;
        poll(ws + WS_FLAG1 + (act ? ((T >> 2) * 16 + (lane & 2 ? 10 : 5) + cc) * 256 : 0), act, ftag); }
      ST(5); FST(36);
      const unsigned so0 = (80 + pf + h) * 512 + l31 * 16, so1 = (h ? (160 + pf) * 512 : (80 + pp) * 512 + 8 * oh) + l31 * 16, so2 = (h ? (160 + pp) * 512 + 8 * oh : (161 + pf) * 512) + l31 * 16;
      struct T3 { u32x4 a, b, c; };
      auto fetch = [&](int t) {
        const int T = 8 * R - 8 + t; const unsigned m = T < 0 ? 0x40000000u : 0u, o = WS_QKV + max(T, 0) * 122880;
        return T3{__builtin_amdgcn_raw_buffer_load_b128(rs, so0 | m, o, 16), __builtin_amdgcn_raw_buffer_load_b128(rs, so1 | m, o, 16), __builtin_amdgcn_raw_buffer_load_b128(rs, so2 | m, o, 16)};
      };
      auto stage = [&](int t, T3 v) {
        if (R == 0 && t < 8) return;
        const unsigned kd = lds0 + L_KV + t * KVS + l31 * 16;
        if (h) v.c[2] = v.c[3] = ONE2;
        *(LAS u32x4*)(kd + h * 512) = v.a; *(LAS u32x4*)(kd + 1024 + h * 512) = v.b; *(LAS u32x4*)(kd + L_V + (1 + h) * VPS) = v.c;
      };
      const T3 t0 = fetch(kt[0]), t1 = fetch(kt[1]);
      FENCE();
      stage(kt[0], t0);
      FENCE();
      const T3 t2 = fetch(kt[2]), t3 = fetch(kt[3]);
      FENCE();
      BAR(); ST(6); FST(39);
      auto pause = [&](int n) { for (int e = 0; e < n; ++e) __builtin_amdgcn_s_sleep(1); };
      pause(j); stage(kt[1], t1);
      BAR(); FST(40);
      pause(2 * j); stage(kt[2], t2); pause(1); stage(kt[3], t3);
      unsigned pf = 0;
      if (!spare) {
        const unsigned po = ((i >> 3) * 160 + j * 40 + (lane >> 1)) * 1280 + (lane & 1) * 128;
        const char* pb = (const char*)p.wo + ((cs80 * 160) & ~127);
        asm volatile("global_load_dword %0, %1, %2" : "+v"(pf) : "v"(po), "s"(pb) : "memory");
        if (lane < 16) asm volatile("global_load_dword %0, %1, %2" : "+v"(pf) : "v"(po + 32 * 1280), "s"(pb) : "memory");
        pre3();
        BAR(); FST(41);
        asm volatile("s_waitcnt vmcnt(0)" : "+v"(pf) :: "memory");
        FST(44);
        wo_steps(5 * j, 5 * j + 5, woo, p.wo, cs80, lds0);
      } else {
        pre3();
        gather();
        FENCE();
        BAR(); FST(41);
        wo_steps(5 * j, 5 * j + 5, woo, p.wo, cs80, lds0);
        FST(45);
#pragma unroll 1
        for (unsigned sp = 0; sp < SPIN_CAP; ++sp) {
          WAITV(0);
          unsigned bad[5];
#pragma unroll
          for (int n = 0; n < 5; ++n) bad[n] = (pa_[n][1] ^ tag) | (pa_[n + 5][1] ^ tag) | (pa_[n + 10][1] ^ tag) | (pa_[n + 15][1] ^ tag) | (pa_[n + 20][1] ^ tag) | (pa_[n + 25][1] ^ tag);
          if (__all((bad[0] | bad[1] | bad[2] | bad[3] | bad[4]) == 0)) break;
          gather();
        }
#pragma unroll
        for (int r = 0; r < 3; ++r) {
          auto v = [&](int kg) { return __uint_as_float(pa_[10 * r + kg][0]); };
          const float sm = (((v(0) + v(1)) + (v(2) + v(3))) + ((v(4) + v(5)) + (v(6) + v(7)))) + (v(8) + v(9));
          *(LAS float*)(lds0 + L_TMP + min(144 * j + 64 * r + lane, 575) * 4) = sm;
        }
        FST(46);
        __builtin_amdgcn_s_waitcnt(0xC07F);
        if (lane == 0) __hip_atomic_fetch_add((LAS unsigned*)(lds0 + L_CNT + 8), 1u, __ATOMIC_RELAXED, __HIP_MEMORY_SCOPE_WORKGROUP);
#pragma unroll 1
        for (unsigned sp = 0; sp < SPIN_CAP; ++sp) { if (__builtin_amdgcn_readfirstlane(*(volatile LAS unsigned*)(lds0 + L_CNT + 8)) >= 4u) break; __builtin_amdgcn_s_sleep(1); }
        asm volatile("" ::: "memory");
        FST(47);
      const unsigned hb = lds0 + L_TMP + 2304 + j * 384;
#pragma unroll
      for (int r = 0; r < 3; ++r) {
        const int e = 64 * r + lane, tk = e >= 96; const unsigned a = lds0 + L_TMP + (e - 96 * tk) * 4, sg = *(const LAS unsigned*)(lds0 + L_TOP + 8 + tk * 4);
        const float m = blo(sg), rd = __builtin_amdgcn_rcpf(bhi(sg));
        const float g = rb((*(const LAS float*)(a + tk * 384) - m * *(const LAS float*)(a + 768)) * rd);
        const float u = rb((*(const LAS float*)(a + 1152 + tk * 384) - m * *(const LAS float*)(a + 1920)) * rd);
        *(LAS u16*)(hb + e * 2) = (u16)pk(rb(g * __builtin_amdgcn_rcpf(1.f + __expf(-g))) * u, 0.f);
      }
      bf16x8 hf[3];
#pragma unroll
      for (int s = 0; s < 3; ++s) hf[s] = lds128(hb + (lane & 1) * 192 + g4 * 16 + s * 64);
      f32x4 y[10] = {};
#pragma unroll
      for (int s = 0; s < 3; ++s)
#pragma unroll
        for (int n = 0; n < 10; ++n) y[n] = MFMA16(hf[s], wdf[(n / 5) * 15 + 5 * s + n % 5], y[n]);
#pragma unroll
      for (int n = 0; n < 5; ++n) {
        const float v = g4 & 1 ? (g4 & 2 ? y[n + 5][1] : y[n + 5][0]) : (g4 & 2 ? y[n][1] : y[n][0]);
        __builtin_amdgcn_raw_buffer_store_b64((u32x2){__float_as_uint(v), tag}, rs, (h * 640 + 32 * j + l31) * 8 + n * 1024, WS_YPART + sp_ * 10240, 16);
      }
      ST(3);
      }
      FST(42);
      if (!spare) WAITV(0);
      FST(43);
      __builtin_amdgcn_s_setprio(0);
      BAR();
    }
  }

  {
    const int tg = tg3;
    const unsigned wa = lds0 + L_WO + (kh * 320 + 4 * g4 + q4) * 160 + 8 * p4;
    constexpr int NPRE = 6;
    bf16x8 wf[NPRE][5];
#pragma unroll
    for (int s = 0; s < NPRE; ++s)
#pragma unroll
      for (int n = 0; n < 5; ++n) wf[s][n] = trfrag(wa + s * 5120 + n * 32, wa + s * 5120 + 2560 + n * 32);
#pragma unroll 1
    for (unsigned sp = 0; sp < SPIN_CAP && !__all((unsigned)(sg0_ >> 32) == tag && (unsigned)(sg1_ >> 32) == tag); ++sp) {
      sg0_ = ldg64(ws + WS_STATS + (tok0 + tk0) * 8); sg1_ = ldg64(ws + WS_STATS + (tok0 + tk1) * 8); }
    f32x4 base[4];
#pragma unroll
    for (int e = 0; e < 4; ++e) {
      const unsigned sg = (unsigned)(e < 2 ? sg0_ : sg1_); const float m = blo(sg), rd = __builtin_amdgcn_rcpf(bhi(sg));
#pragma unroll
      for (int c = 0; c < 4; ++c) { const unsigned u = (e < 2 ? xv0 : xv1)[2 * (e & 1) + (c >> 1)]; const float v = c & 1 ? bhi(u) : blo(u); base[e][c] = v + (v - m) * rd; }
    }
    const u32x2 kk12 = *(const LAS u32x2*)(lds0 + L_TOP);
    const unsigned k1 = __builtin_amdgcn_readfirstlane(kk12[0]), k2 = __builtin_amdgcn_readfirstlane(kk12[1]);
    const unsigned i1 = 2047u - (k1 & 0xffffu) - tok0, i2 = 2047u - (k2 & 0xffffu) - tok0;
#pragma unroll
    for (int s = 0; s < NPRE; ++s)
#pragma unroll
      for (int n = 0; n < 5; ++n) asm volatile("" : "+v"(wf[s][n]));
    asm volatile("" : "+v"(base[0]), "+v"(base[1]), "+v"(base[2]), "+v"(base[3]));
    poll(ws + WS_FLAG2 + (xc * 32 + 16 * kh + (lane & 15)) * 256, lane < 16, ftag);
    ST(9); FST(61);
    const unsigned ob = WS_O + (tok0 / 16 + tg) * 20480;
    u32x4 of[10];
    auto oload = [&](int s) {
      if (s < 8) of[s] = __builtin_amdgcn_raw_buffer_load_b128(rs, lane * 16 + s * 1024, ob + kh * 8192, 16);
      else {
        const unsigned a = (8 * (s - 8) + 2 * g4) * 128 + (lane & 15) * 8;
        const u32x2 u0 = __builtin_amdgcn_raw_buffer_load_b64(rs, a, ob + 16384 + kh * 2048, 16), u1 = __builtin_amdgcn_raw_buffer_load_b64(rs, a + 128, ob + 16384 + kh * 2048, 16);
        of[s] = (u32x4){u0[0], u0[1], u1[0], u1[1]};
      }
    };
#pragma unroll
    for (int s = 0; s < OPRE; ++s) oload(s);
    FENCE();
    FST(62);
    const bool own = (i1 < 64u || i2 < 64u) && w < 5;
    const unsigned ya = own ? ((tid >= 160) * 640 + n0 + ((tid - 160 * (tid >= 160)) >> 1)) * 8 + (tid & 1) * 81920 : 0x40000000u;
    u32x2 yv[8];
    auto yload = [&]() {
      asm volatile("" ::: "memory");
#pragma unroll
      for (int e = 0; e < 8; ++e) yv[e] = __builtin_amdgcn_raw_buffer_load_b64(rs, ya, WS_YPART + e * 10240, 16);
    };
    f32x4 acc[5] = {};
#pragma unroll
    for (int s = 0; s < 10; ++s) {
      if (s + OPRE < 10) oload(s + OPRE);
      if (s + OPRE == 10 && own) yload();
      FENCE();
#pragma unroll
      for (int n = 0; n < 5; ++n) acc[n] = MFMA16(wf[s % NPRE][n], __builtin_bit_cast(bf16x8, of[s]), acc[n]);
      if (s + NPRE < 10) {
#pragma unroll
        for (int n = 0; n < 5; ++n) wf[s][n] = trfrag(wa + (s + NPRE) * 5120 + n * 32, wa + (s + NPRE) * 5120 + 2560 + n * 32);
      }
      FENCE();
    }
    ST(10); FST(63);
    if (own) {
#pragma unroll 1
      for (unsigned sp = 0; sp < SPIN_CAP; ++sp) {
        unsigned bad = 0;
#pragma unroll
        for (int e = 0; e < 8; ++e) bad |= yv[e][1] ^ tag;
        if (__all(bad == 0)) break;
        yload();
      }
      float s = 0.f;
#pragma unroll
      for (int e = 0; e < 8; ++e) s += __uint_as_float(yv[e][0]);
      s += __uint_as_float(dpp<0xB1>(__float_as_uint(s)));
      const unsigned kk = (tid >= 160 ? k2 : k1) >> 16;
      const float r = blo(kk & 0x8000u ? kk & 0x7fffu : ~kk & 0xffffu);
      if (!(tid & 1)) *(LAS float*)(lds0 + L_UPD + (tid >> 1) * 4) = rb(rb(s) * rb(__builtin_amdgcn_rcpf(1.f + __expf(-r))));
    }
#pragma unroll
    for (int n = 0; n < 5; ++n) *(LAS f32x4*)(lds0 + L_RED + kh * 21504 + (16 * tg + (lane & 15)) * 336 + (16 * n + 4 * g4) * 4) = acc[n];
    BAR();
    auto fin = [&](unsigned tk, unsigned ch, unsigned eo, f32x4 b0, f32x4 b1) {
      const unsigned a = lds0 + L_RED + tk * 336 + ch * 32;
      b0 += *(const LAS f32x4*)a + *(const LAS f32x4*)(a + 21504); b1 += *(const LAS f32x4*)(a + 16) + *(const LAS f32x4*)(a + 21504 + 16);
      if (tk == i1 || tk == i2) { const unsigned u = lds0 + L_UPD + (tk == i1 ? 0 : 320) + ch * 32; b0 += *(const LAS f32x4*)u; b1 += *(const LAS f32x4*)(u + 16); }
      *(GAS u32x4*)((char*)p.out + eo) = (u32x4){pk(b0[0], b0[1]), pk(b0[2], b0[3]), pk(b1[0], b1[1]), pk(b1[2], b1[3])};
    };
    fin(tk0, ch0, eo0, base[0], base[1]);
    if (two) fin(tk1, ch1, eo1, base[2], base[3]);
    ST(11);
  }
}

extern "C" void kernel_launch(void* const* d_in, const int* in_sizes, int n_in, void* d_out, int out_size, void* d_ws, size_t ws_size,
                              hipStream_t stream) {
  Params p;
  p.x = (const u16*)d_in[0]; p.wqkv = (const u16*)d_in[1]; p.wo = (const u16*)d_in[2]; p.wr = (const u16*)d_in[4];
  p.wg = (const u16*)d_in[5]; p.wu = (const u16*)d_in[6]; p.wd = (const u16*)d_in[7]; p.out = (u16*)d_out; p.ws = (char*)d_ws;
  fused_block<<<256, 512, 0, stream>>>(p);
}
```

```cpp
#define STAMP
#include <hip/hip_runtime.h>
#include <stdint.h>

typedef unsigned short u16;
typedef unsigned long long u64;
typedef short bf16x8 __attribute__((ext_vector_type(8)));
typedef short s16x4 __attribute__((ext_vector_type(4)));
typedef float f32x2 __attribute__((ext_vector_type(2)));
typedef float f32x4 __attribute__((ext_vector_type(4)));
typedef float f32x16 __attribute__((ext_vector_type(16)));
typedef unsigned u32x2 __attribute__((ext_vector_type(2)));
typedef unsigned u32x4 __attribute__((ext_vector_type(4)));
typedef __bf16 bf2 __attribute__((ext_vector_type(2)));
#define LAS __attribute__((address_space(3)))
#define GAS __attribute__((address_space(1)))
#define DI __device__ __forceinline__

constexpr unsigned WS_GEN = 0, WS_FLAG1 = 4096, WS_FLAG2 = 131072, WS_CAND = 262144, WS_STATS = 278528, WS_PARTA = 524288,
                   WS_YPART = 1572864, WS_QKV = 2097152, WS_O = 10485760, WS_END = 13631488, WS_STAMP = 16777216;
constexpr unsigned L_WO = 0, L_KV = 102400, L_FFNA = 65536;
constexpr unsigned KVS = 3264, L_V = 1536, VPS = 576;
constexpr unsigned L_RED = L_KV, L_UPD = L_KV + 43008;
constexpr unsigned L_KEY = 154624, L_TOP = L_KEY + 32, L_CNT = L_KEY + 48  , L_KEYS = L_KEY + 64, L_STAT = L_KEY + 96, L_CS = L_STAT + 1024, L_TMP = L_CS + 2048;
constexpr unsigned L_Y = L_KV + 44032;
constexpr unsigned SPIN_CAP = 1u << 20;
constexpr unsigned ONE2 = 0x3f803f80u;
#define CODE_KB 36

constexpr int UP[20] = {0, 0, 1, 1, 2, 2, 3, 3, 4, 4, 5, 6, 6, 7, 7, 8, 8, 9, 9, 9}, UQ[18] = {0, 1, 0, 1, 0, 1, 0, 1, 0, 1, 1, 0, 1, 0, 1, 0, 1, 0};
template <int N> struct IC { static constexpr int value = N; };

struct Params { const u16 *x, *wqkv, *wo, *wr, *wg, *wu, *wd; u16* out; char* ws; };

#define ST(i) do { if (lane == 0) { GAS u64* sp_ = (GAS u64*)(p.ws + WS_STAMP) + ((blockIdx.x * 8 + w) * 48 + (i)) * 2; sp_[0] = __builtin_amdgcn_s_memrealtime(); sp_[1] = __builtin_amdgcn_s_memtime(); } } while (0)

#define FST(i) do {} while (0)
#define POLLGAP 1
#define WAITV(n) asm volatile("s_waitcnt vmcnt(" #n ")" ::: "memory")
#define REQ0 3
#define LA 3
#define LAMODE 1
#define BC_AT 7
#define FORCE_FB 0
#define WO1 0
#define OPRE 4
#define TLBW 0
#define HPRIO2 1
#define ROT 3
#define KEYDELAY 200
#define PRIO 0
#define WAITN_(n) WAITV(n)
#define WAITN(n) WAITN_(n)
#define WO_PRE 6
#define BAR() do { __builtin_amdgcn_s_waitcnt(0xC07F); asm volatile("s_barrier" ::: "memory"); } while (0)
#define FENCE() __builtin_amdgcn_sched_barrier(0)
#define MFMA32(a, b, c) __builtin_amdgcn_mfma_f32_32x32x16_bf16((a), (b), (c), 0, 0, 0)
#define MFMA16(a, b, c) __builtin_amdgcn_mfma_f32_16x16x32_bf16((a), (b), (c), 0, 0, 0)

DI void glds(unsigned voff, const void* sbase, unsigned lds) {
  asm volatile("s_mov_b32 m0, %2\n\ts_nop 0\n\tglobal_load_lds_dwordx4 %0, %1" :: "v"(voff), "s"(sbase), "s"(lds) : "memory");
}
DI bf16x8 lds128(unsigned a) { return *(const LAS bf16x8*)a; }
DI s16x4 ldstr(unsigned a) { return __builtin_amdgcn_ds_read_tr16_b64_v4i16((LAS s16x4*)a); }
DI bf16x8 trfrag(unsigned a, unsigned a2) { s16x4 lo = ldstr(a), hi = ldstr(a2); return __builtin_shufflevector(lo, hi, 0, 1, 2, 3, 4, 5, 6, 7); }
DI unsigned pk(float a, float b) { f32x2 v = {a, b}; return __builtin_bit_cast(unsigned, __builtin_convertvector(v, bf2)); }
DI float rb(float a) { return __uint_as_float(pk(a, 0.f) << 16); }
DI float blo(unsigned v) { return __uint_as_float(v << 16); }
DI float bhi(unsigned v) { return __uint_as_float(v & 0xffff0000u); }
DI float dot2(unsigned a, unsigned b, float c) { return __builtin_amdgcn_fdot2_f32_bf16(__builtin_bit_cast(bf2, a), __builtin_bit_cast(bf2, b), c, false); }
DI float xhalf_sum(float v) { auto r = __builtin_amdgcn_permlane32_swap(__float_as_uint(v), __float_as_uint(v), false, false); return __uint_as_float(r[0]) + __uint_as_float(r[1]); }
DI u64 ldg64(const char* a) { return __hip_atomic_load((GAS u64*)a, __ATOMIC_RELAXED, __HIP_MEMORY_SCOPE_AGENT); }
DI void stg64(char* a, u64 v) { __hip_atomic_store((GAS u64*)a, v, __ATOMIC_RELAXED, __HIP_MEMORY_SCOPE_AGENT); }
DI u64 gran(unsigned v, unsigned tag) { return ((u64)tag << 32) | v; }
template <int CTRL> DI unsigned dpp(unsigned v) { return __builtin_amdgcn_update_dpp(0u, v, CTRL, 0xf, 0xf, true); }
DI unsigned wave_umax(unsigned v) {
  v = max(v, dpp<0x128>(v)); v = max(v, dpp<0x124>(v)); v = max(v, dpp<0x122>(v)); v = max(v, dpp<0x121>(v));
  auto r = __builtin_amdgcn_permlane16_swap(v, v, false, false); v = max(r[0], r[1]);
  auto q = __builtin_amdgcn_permlane32_swap(v, v, false, false); return max(q[0], q[1]);
}
DI float sum32(float v) {
  v += __uint_as_float(dpp<0x128>(__float_as_uint(v))); v += __uint_as_float(dpp<0x124>(__float_as_uint(v)));
  v += __uint_as_float(dpp<0x122>(__float_as_uint(v))); v += __uint_as_float(dpp<0x121>(__float_as_uint(v)));
  auto r = __builtin_amdgcn_permlane16_swap(__float_as_uint(v), __float_as_uint(v), false, false);
  return __uint_as_float(r[0]) + __uint_as_float(r[1]);
}
DI void ln_stats(float s1, float s2, float& mb, float& db) {
  float mean = s1 * (1.f / 640.f); float var = fmaxf(s2 * (1.f / 640.f) - mean * mean, 0.f);
  mb = rb(mean); db = rb(__builtin_amdgcn_sqrtf(rb(rb(var) + 1e-5f)));
}
DI void poll(const char* addr, bool act, u64 want) {
#pragma unroll 1
  for (unsigned sp = 0; sp < SPIN_CAP; ++sp) { u64 v = ldg64(addr); if (__all(!act || v == want)) break; if (POLLGAP) __builtin_amdgcn_s_sleep(POLLGAP); }
  asm volatile("" ::: "memory");
}
struct Keys { u32x4 g[4]; };
DI bool keys_top2(const Keys& k, unsigned tag, unsigned& k1, unsigned& k2) {
  unsigned bad = 0, a = 0, b = 0;
#pragma unroll
  for (int e = 0; e < 4; ++e) {
    bad |= (k.g[e][1] ^ tag) | (k.g[e][3] ^ tag);
    const unsigned hi = max(k.g[e][0], k.g[e][2]), lo = min(k.g[e][0], k.g[e][2]);
    b = max(max(b, lo), min(a, hi)); a = max(a, hi);
  }
  if (!__all(bad == 0)) return false;
  k1 = wave_umax(a); k2 = wave_umax(a == k1 ? b : a);
  k1 = __builtin_amdgcn_readfirstlane(k1); k2 = __builtin_amdgcn_readfirstlane(k2);
  return true;
}
template <class RS> DI void keys_wait(RS rs, int lane, unsigned tag, unsigned& k1, unsigned& k2) {
#pragma unroll 1
  for (unsigned sp = 0; sp < SPIN_CAP; ++sp) {
    Keys k;
    asm volatile("" ::: "memory");
#pragma unroll
    for (int e = 0; e < 4; ++e) k.g[e] = __builtin_amdgcn_raw_buffer_load_b128(rs, lane * 16 + e * 1024, WS_CAND, 16);
    if (keys_top2(k, tag, k1, k2)) break;
    __builtin_amdgcn_s_sleep(16);
  }
}
struct WoOff { unsigned r[5], l[5]; };
DI WoOff wo_offsets(int lane) {
  WoOff o;
#pragma unroll
  for (int m = 0; m < 5; ++m) {
    const unsigned L = 64 * m + lane, rho = (L * 205) >> 11, c = L - rho * 10, q = rho & 3, g = (rho >> 2) & 3, t = rho >> 4;
    o.r[m] = (24 * (g >> 1) + 8 * (g & 1) + 4 * t + q) * 1280 + c * 16; o.l[m] = (40 * g + 16 + 4 * t + q) * 1280 + c * 16;
    asm volatile("" : "+v"(o.r[m]), "+v"(o.l[m]));
  }
  return o;
}
DI void wo_steps(int s0, int s1, const WoOff& o, const u16* wo, int cs80, unsigned lds0) {
#pragma unroll 1
  for (int u = s0; u < s1; ++u) {
    const int kh = u >= 10, st = u - 10 * kh;
    const char* sp = (const char*)wo + cs80 * 160 + (kh * 320 + (st < 8 ? st * 40 : (st - 8) * 160)) * 1280;
    const unsigned d = lds0 + L_WO + u * 5120;
    if (st < 8) {
#pragma unroll
      for (int m = 0; m < 5; ++m) glds(o.r[m], sp, d + m * 1024);
    } else {
#pragma unroll
      for (int m = 0; m < 5; ++m) glds(o.l[m], sp, d + m * 1024);
    }
  }
}

__global__ __launch_bounds__(512, 2) void fused_block(Params p) {
  __shared__ __attribute__((aligned(1024))) char smem[163840];
  const unsigned lds0 = (unsigned)(size_t)smem;
  const u64 t_in = __builtin_amdgcn_s_memrealtime(), c_in = __builtin_amdgcn_s_memtime();
  u64 pc0; asm volatile("s_getpc_b64 %0" : "=s"(pc0));
  const int tid = threadIdx.x, lane = tid & 63, w = __builtin_amdgcn_readfirstlane(tid >> 6);
  const int b = blockIdx.x, xc = b & 7, i = b >> 3;
  const bool spare = i < 2;
  const int xp = xc >> 1, odd = xc & 1;
  const int cs80 = i & 7;
  const int h = lane >> 5, l31 = lane & 31, g4 = lane >> 4, q4 = (lane & 15) >> 2, p4 = lane & 3;
  unsigned tag = 0;
  auto lds_tag = [&]() { tag = __builtin_amdgcn_readfirstlane(*(const LAS unsigned*)(lds0 + L_KEY)); };
  const unsigned j30 = (unsigned)(i - 2) & 31u, j7 = (j30 * 37) >> 8;
  const unsigned trl = j30 < 28 ? j7 : (2 * odd + j30) & 3, ct = j30 < 28 ? j30 - j7 * 7 + odd * 8 : 7;
  unsigned sn = ROT == 3 ? 3 * (trl + ct - 3 * (((trl + ct) * 43) >> 7)) + ((xc * 3) >> 3) : ROT == 2 ? 5 * ((trl + ct) & 1) + ((xc * 5) >> 3) : ROT == 1 ? (xc * 5) >> 2 : ROT == 4 ? (5 * ((trl + ct) & 3)) / 2 + (xc & 1) : 0;
  const unsigned tr = 4 * xp + trl;
  const int rb8 = 64 * xp + 32 * odd + i;
  const unsigned x0 = (lane >> 3) * 1280 + (((lane & 7) ^ g4) * 16), x1 = (8 + (lane >> 3)) * 1280 + (((lane & 7) ^ (4 + g4)) * 16);
  const unsigned w0 = g4 * 3840 + (((lane & 15) ^ (g4 << 2)) * 16), w1 = w0 + 4 * 3840;
  if (w >= 4) sn = sn == 9 ? 0 : sn + 1;
  unsigned fx = tr * (128 * 1280) + sn * 128 + (w & 3) * (32 * 1280), fw = ct * 256 + (sn * 64 + (w & 3) * 16) * 3840, fd = lds0 + (w >> 2) * 32768 + (w & 3) * 4096;
  unsigned fv[8] = {x0 + fx, w0 + fw, x1 + fx, w1 + fw, x0 + fx + 16 * 1280, w0 + fw + 8 * 3840, x1 + fx + 16 * 1280, w1 + fw + 8 * 3840};
  sn = sn == 9 ? 0 : sn + 1;
  asm volatile("" : "+v"(fv[0]), "+v"(fv[1]), "+v"(fv[2]), "+v"(fv[3]), "+v"(fv[4]), "+v"(fv[5]), "+v"(fv[6]), "+v"(fv[7]));
  asm volatile("" : "+s"(fd), "+s"(sn));
  FENCE();
  if (!spare) {
#pragma unroll
    for (int e = 0; e < 8; ++e) glds(fv[e], e & 1 ? (const void*)p.wqkv : (const void*)p.x, fd + (e & 1) * 16384 + (e >> 1) * 1024);
  }
  FST(28);
  char* const ws = p.ws;
  const auto rs = __builtin_amdgcn_make_buffer_rsrc(ws, 0, WS_END, 0x00020000);
  if (lane == 0) { GAS u64* sp_ = (GAS u64*)(p.ws + WS_STAMP) + ((blockIdx.x * 8 + w) * 48) * 2; sp_[0] = t_in; sp_[1] = c_in; }
  const char* const xt = (const char*)p.x + tr * (128 * 1280);
  const char* const wt = (const char*)p.wqkv + ct * 256;
  auto pieces = [&](unsigned e, unsigned slot) {
    const char* xs = xt + (sn * 128 + e * (16 * 1280)); const char* wsb = wt + (sn * 64 + e * 8) * 3840; const unsigned d = lds0 + slot * 32768 + e * 2048;
    glds(x0, xs, d); glds(w0, wsb, d + 16384); glds(x1, xs, d + 1024); glds(w1, wsb, d + 16384 + 1024);
  };

  const int rrow = 8 * rb8 + 2 * w + h;
  u32x4 rx[3], rw[3]; unsigned pfv = 0, genv; float a1 = 0.f, a2 = 0.f, ar = 0.f, sw = 0.f;
  auto router_load = [&]() {
    if (CODE_KB && lane < CODE_KB / 2) asm volatile("global_load_dword %0, %1, %2" : "+v"(pfv) : "v"(((i & 3) * 4 + w) * (CODE_KB * 64) + lane * 128), "s"(pc0) : "memory");
    if (TLBW) {
      const unsigned n = (i * 4 + w) * 3 + lane;
      const char* a = n < 208 ? ws + n * 65536 : n < 248 ? (const char*)p.out + (n - 208) * 65536 : n < 261 ? (const char*)p.wo + min((n - 248) * 65536, 819072u)
                    : n < 291 ? (const char*)p.wg + (n - 261) * 65536 : n < 321 ? (const char*)p.wu + (n - 291) * 65536 : (const char*)p.wd + (n - 321 < 30 ? n - 321 : 0) * 65536;
      if (lane < 3) asm volatile("global_load_dword %0, %1, off" : "+v"(pfv) : "v"(a) : "memory");
    }
    const char* xr = (const char*)p.x + rrow * 1280 + l31 * 16; const char* wr = (const char*)p.wr + l31 * 16;
    const int t3 = l31 < 16 ? 1024 : 0;
    rx[0] = *(const GAS u32x4*)xr; rx[1] = *(const GAS u32x4*)(xr + 512); rw[0] = *(const GAS u32x4*)wr; rw[1] = *(const GAS u32x4*)(wr + 512);
    rx[2] = *(const GAS u32x4*)(xr + t3); rw[2] = *(const GAS u32x4*)(wr + t3);
    genv = __hip_atomic_load((GAS unsigned*)(ws + WS_GEN), __ATOMIC_RELAXED, __HIP_MEMORY_SCOPE_AGENT);
    FENCE();
  };
  auto router_dots = [&]() {
    if (l31 >= 16) rx[2] = rw[2] = (u32x4){};
#pragma unroll
    for (int e = 0; e < 3; ++e)
#pragma unroll
      for (int c = 0; c < 4; ++c) { a1 = dot2(rx[e][c], ONE2, a1); a2 = dot2(rx[e][c], rx[e][c], a2); ar = dot2(rx[e][c], rw[e][c], ar); sw = dot2(rw[e][c], ONE2, sw); }
    tag = __builtin_amdgcn_readfirstlane(genv) + 1u;
    if (tid == 0) *(LAS unsigned*)(lds0 + L_KEY) = tag;
    asm volatile("" : "+v"(a1), "+v"(a2), "+v"(ar), "+v"(sw) : "v"(pfv));
  };
  float rmb = 0.f, rdb = 0.f; unsigned rk = 0, rv = 0;
  auto router_step = [&](int k) {
    auto fdpp = [&](float& v, int st) { const unsigned u = __float_as_uint(v); v += __uint_as_float(st == 0 ? dpp<0x128>(u) : st == 1 ? dpp<0x124>(u) : st == 2 ? dpp<0x122>(u) : dpp<0x121>(u)); };
    auto fswp = [&](float& v) { auto r = __builtin_amdgcn_permlane16_swap(__float_as_uint(v), __float_as_uint(v), false, false); v = __uint_as_float(r[0]) + __uint_as_float(r[1]); };
    if (k < 4) { fdpp(a1, k); fdpp(a2, k); fdpp(ar, k); fdpp(sw, k); }
    if (k == 4) { fswp(a1); fswp(a2); }
    if (k == 5) { fswp(ar); fswp(sw); }
    if (k == 6) { a1 *= 1.f / 640.f; a2 = fmaxf(a2 * (1.f / 640.f) - a1 * a1, 0.f); }
    if (k == 7) { rmb = rb(a1); a2 = rb(a2) + 1e-5f; }
    if (k == 8) { a2 = __builtin_amdgcn_sqrtf(rb(a2)); ar -= rmb * sw; }
    if (k == 9) { rdb = rb(a2); a2 = __builtin_amdgcn_rcpf(rdb); }
    if (k == 10) { rk = pk(ar * a2, 0.f) & 0xffffu; rv = (__float_as_uint(rmb) >> 16) | __float_as_uint(rdb); }
    if (k == 11) rk = (rk & 0x8000u ? (~rk & 0xffffu) : (rk | 0x8000u)) << 16;
    if (k == 12) { if (l31 == 0) *(LAS unsigned*)(lds0 + L_KEYS + (2 * w + h) * 4) = rk | (2047u - rrow); }
    if (k == 13) { if (l31 == 0) __builtin_amdgcn_raw_buffer_store_b64((u32x2){rv, tag}, rs, WS_STATS + rrow * 8, 0, 16); }
    auto max8 = [&](unsigned& v, int st) { v = max(v, st == 0 ? dpp<0xB1>(v) : st == 1 ? dpp<0x4E>(v) : dpp<0x141>(v)); };
    if (k == 14) rk = rv = *(const LAS unsigned*)(lds0 + L_KEYS + (lane & 7) * 4);
    if (k >= 16 && k < 19) max8(rv, k - 16);
    if (k == 19) rk = rk == rv ? 0u : rk;
    if (k >= 20 && k < 23) max8(rk, k - 20);
    if (k == 23) { if (w == 0 && lane < 2) __builtin_amdgcn_raw_buffer_store_b64((u32x2){lane ? rk : rv, tag}, rs, WS_CAND + (2 * rb8 + lane) * 8, 0, 16); }
    asm volatile("" : "+v"(a1), "+v"(a2), "+v"(ar), "+v"(sw), "+v"(rmb), "+v"(rdb), "+v"(rk), "+v"(rv));
  };

  bf16x8 wdf[30];
  if (!spare) {
    if (w >= 4) {
      const int j = w - 4;
      auto issue = [&](int slot) { pieces(2 * j, slot); pieces(2 * j + 1, slot); sn = sn == 9 ? 0 : sn + 1; };
      if (tid == 384) *(LAS u32x4*)(lds0 + L_CNT) = (u32x4){};
      const unsigned au = tr * 15 + ct, cg = (au * 205) >> 11, kg = au - cg * 10;
      FST(31); BAR();
      FST(1); issue(2); FST(2);
      WAITV(8); FST(3); BAR();
      issue(3); pieces(2 * j, 0);
      WAITV(12); BAR();
      pieces(2 * j + 1, 0); sn = sn == 9 ? 0 : sn + 1; issue(1);
      WAITV(16); BAR();
#pragma unroll 1
      for (int s = 6; s < 9; ++s) { FST(3 * s - 8); issue(s & 3); FST(3 * s - 7); WAITV(16); FST(3 * s - 6); BAR(); }
      lds_tag();
      auto ffn_dma = [&]() {
        const unsigned fo = (lane >> 3) * 3072 + (lane & 7) * 16;
        const char* fb = (const char*)(j < 2 ? p.wg : p.wu) + (kg * 64 + 32 * (j & 1)) * 3072 + cg * 128;
#pragma unroll
        for (int e = 0; e < 4; ++e) glds(fo, fb + e * 8 * 3072, lds0 + L_FFNA + (4 * j + e) * 1024);
      };
      auto ffn_landed = [&]() { WAITV(0); if (lane == 0) __hip_atomic_fetch_add((LAS unsigned*)(lds0 + L_CNT + 12), 1u, __ATOMIC_RELAXED, __HIP_MEMORY_SCOPE_WORKGROUP); };
      if (w == 6) {
        const char* ga = ws + WS_STATS + tr * 1024;
        u32x2 g0, g1;
        asm volatile("global_load_dwordx2 %0, %2, %3 sc1\n\tglobal_load_dwordx2 %1, %2, %3 offset:512 sc1" : "=&v"(g0), "=&v"(g1) : "v"(lane * 8), "s"(ga) : "memory");
        issue(1); WAITV(16); BAR();
        ffn_dma();
        asm volatile("s_waitcnt vmcnt(12)\n\ts_barrier" : "+v"(g0), "+v"(g1) :: "memory");
        if (!__all(g0[1] == tag && g1[1] == tag)) {
#pragma unroll 1
          for (unsigned sp = 0; sp < SPIN_CAP; ++sp) {
            g0 = __builtin_bit_cast(u32x2, ldg64(ga + lane * 8)); g1 = __builtin_bit_cast(u32x2, ldg64(ga + 512 + lane * 8));
            if (__all(g0[1] == tag && g1[1] == tag)) break; }
        }
        *(LAS f32x2*)(lds0 + L_STAT + lane * 8) = (f32x2){blo(g0[0]), __builtin_amdgcn_rcpf(bhi(g0[0]))};
        *(LAS f32x2*)(lds0 + L_STAT + 512 + lane * 8) = (f32x2){blo(g1[0]), __builtin_amdgcn_rcpf(bhi(g1[0]))};
        FST(23); WAITV(4); BAR();
        ffn_landed();
      } else if (w < 6) { issue(1); WAITV(16); BAR(); ffn_dma(); WAITV(12); BAR(); WAITV(4); BAR(); ffn_landed(); }
      else {
        Keys kk;
        asm volatile("buffer_load_dwordx4 %0, %4, %5, %6 offen sc1\n\tbuffer_load_dwordx4 %1, %4, %5, %6 offen offset:1024 sc1\n\t"
                     "buffer_load_dwordx4 %2, %4, %5, %6 offen offset:2048 sc1\n\tbuffer_load_dwordx4 %3, %4, %5, %6 offen offset:3072 sc1"
                     : "=&v"(kk.g[0]), "=&v"(kk.g[1]), "=&v"(kk.g[2]), "=&v"(kk.g[3]) : "v"(lane * 16), "s"(rs), "s"(WS_CAND) : "memory");
        issue(1); WAITV(16); BAR();
        ffn_dma();
        asm volatile("s_waitcnt vmcnt(12)\n\ts_barrier" : "+v"(kk.g[0]), "+v"(kk.g[1]), "+v"(kk.g[2]), "+v"(kk.g[3]) :: "memory");
        unsigned k1 = 0, k2 = 0;
        const bool early = keys_top2(kk, tag, k1, k2);
        u32x4 xc0 = {ONE2, ONE2, ONE2, ONE2}, xc1 = xc0;
        { const char* xq = (const char*)p.x + ((2047u - (lane & 1 ? k2 : k1)) & 2047u) * 1280 + kg * 128 + g4 * 16;
          if ((lane & 15) < 2) asm volatile("global_load_dwordx4 %0, %2, off\n\tglobal_load_dwordx4 %1, %2, off offset:64" : "+v"(xc0), "+v"(xc1) : "v"(xq) : "memory"); }
        asm volatile("s_waitcnt vmcnt(6)\n\ts_barrier\n\ts_waitcnt vmcnt(0)" : "+v"(xc0), "+v"(xc1) :: "memory");
        FST(23);
        if (!early) {
          keys_wait(rs, lane, tag, k1, k2);
          if ((lane & 15) < 2) { const char* xq = (const char*)p.x + ((2047u - (lane & 1 ? k2 : k1)) & 2047u) * 1280 + kg * 128 + g4 * 16; xc0 = *(const GAS u32x4*)xq; xc1 = *(const GAS u32x4*)(xq + 64); }
        }
        if (b == 16 && lane == 0) __hip_atomic_store((GAS unsigned*)(ws + WS_GEN), tag, __ATOMIC_RELAXED, __HIP_MEMORY_SCOPE_AGENT);
        if (lane == 0) *(LAS u32x2*)(lds0 + L_TOP) = (u32x2){k1, k2};
#pragma unroll 1
        for (unsigned sp = 0; sp < SPIN_CAP; ++sp) { if (__builtin_amdgcn_readfirstlane(*(volatile LAS unsigned*)(lds0 + L_CNT + 12)) >= 3u) break; __builtin_amdgcn_s_sleep(1); }
        asm volatile("" ::: "memory");
#pragma unroll
        for (int n = 0; n < 8; ++n) {
          const unsigned a = lds0 + L_FFNA + (n >> 2) * 8192 + (8 * g4 + q4) * 128 + (n & 3) * 32 + 8 * p4;
          const f32x4 y = MFMA16(trfrag(a + 4096, a + 4608), __builtin_bit_cast(bf16x8, xc1), MFMA16(trfrag(a, a + 512), __builtin_bit_cast(bf16x8, xc0), (f32x4){}));
          if ((lane & 15) < 3) *(LAS f32x4*)(lds0 + L_TMP + (((n >> 2) * 3 + (lane & 15)) * 64 + 16 * (n & 3) + 4 * g4) * 4) = y;
        }
#pragma unroll
        for (int n = 0; n < 6; ++n) stg64(ws + WS_PARTA + (au * 384 + n * 64 + lane) * 8, gran(*(const LAS unsigned*)(lds0 + L_TMP + (n * 64 + lane) * 4), tag));
        FST(25);
      }
    } else {
      router_load();
      const int fh = w >> 1, th = w & 1;
      unsigned xa[4], wa[2], ko[4];
#pragma unroll
      for (int e = 0; e < 4; ++e) { const int ks = (2 * th + e) & 3, row = 64 * th + l31; xa[e] = lds0 + row * 128 + (((2 * ks + h) ^ ((row >> 1) & 7)) * 16); ko[e] = ks * 4096; }
#pragma unroll
      for (int f = 0; f < 2; ++f) wa[f] = lds0 + 16384 + (8 * h + q4) * 256 + (((4 * (2 * fh + f) + 2 * (g4 & 1) + (p4 >> 1)) ^ (q4 << 2)) * 16) + 8 * (p4 & 1);
      f32x16 acc[2][2] = {};
      f32x4 cs0 = {}, cs1 = {};
      const unsigned selw = (lane & 15) == (g4 & 1) ? ONE2 : 0u;
      const bf16x8 sel = __builtin_bit_cast(bf16x8, (u32x4){selw, selw, selw, selw});
      struct Fr { bf16x8 v[4]; };
      Fr FA[4], FB[4];
      auto rd = [&](Fr* F, int n, unsigned so) {
        const int e = n >> 2, j = n & 3;
        if (j < 2) { const unsigned a = wa[j] + (so + ko[e]); F[e].v[j] = trfrag(a, a + 1024); } else F[e].v[j] = lds128(xa[e] + so + (j - 2) * 4096);
      };
      auto colsum = [&](const Fr* C, int e) { cs0 = MFMA16(C[e].v[0], sel, cs0); cs1 = MFMA16(C[e].v[1], sel, cs1); };
      bf16x8 ax[2], bx[2]; float rr[2], c0, c1; f32x2 st0, st1; float cp[4];
      const float qs = ct < 5 ? 0.22360679774997896f * 1.4426950408889634f : 1.f;
      const unsigned ca = lds0 + L_CS + fh * 512 + l31 * 4, cown = lds0 + L_CS + w * 256 + ((lane & 15) * 16 + 4 * g4) * 4;
      auto prep = [&](int n) {
        if (n == 12) { cp[0] = *(const LAS float*)ca; cp[1] = *(const LAS float*)(ca + 256); cp[2] = *(const LAS float*)(ca + 128); cp[3] = *(const LAS float*)(ca + 384); }
        if (n == 13) { st0 = *(const LAS f32x2*)(lds0 + L_STAT + (64 * th + l31) * 8); st1 = *(const LAS f32x2*)(lds0 + L_STAT + (64 * th + 32 + l31) * 8); }
      };
      auto body = [&](Fr* N, const Fr* C, unsigned so, int r0 = 99) {
#pragma unroll
        for (int n = 0; n < 16; ++n) {
          const int e = n >> 2, f = (n >> 1) & 1, t = n & 1;
          acc[f][t] = MFMA32(C[e].v[f], C[e].v[2 + t], acc[f][t]);
          if (n < 4) { rd(N, 2 * n, so); rd(N, 2 * n + 1, so); } else if (n < 12) rd(N, n + 4, so);
          if (n >= 2 && r0 + n - 2 < 24) router_step(r0 + n - 2);
          if (r0 == 100) prep(n);
          FENCE();
        }
        if (r0 == 100) { cs0 = cs1 = (f32x4){}; colsum(N, 2); colsum(N, 3); }
        colsum(N, 0); colsum(N, 1);
        FENCE();
      };
      FST(29);
      if (TLBW) WAITV(10); else WAITV(9);
      FST(30); BAR(); ST(1); FST(31);
#pragma unroll
      for (int n = 0; n < 16; ++n) rd(FA, n, 0);
      FST(32); router_dots(); FST(33);
      colsum(FA, 0); colsum(FA, 1);
      BAR();
#pragma unroll
      for (int s = 1; s <= 7; s += 2) {
        FST(2 * s); body(FB, FA, (s & 3) * 32768, s == 1 ? 0 : 99); FST(2 * s + 1); BAR();
        FST(2 * s + 2); body(FA, FB, ((s + 1) & 3) * 32768, s == 1 ? 14 : 99);
        if (s == 7 && (lane & 15) < 2) { *(LAS f32x4*)cown = cs0; *(LAS f32x4*)(cown + 128) = cs1; }
        FST(2 * s + 3); BAR();
      }
      FST(18);
      body(FB, FA, 32768, 100);
      FST(19);
      const unsigned qo = WS_QKV + (4 * tr + 2 * th) * 122880 + (16 * ct + 8 * fh + h) * 512 + l31 * 16;
      unsigned pw[8];
      auto split3 = [&](float c) {
        const float hi = rb(c), r1 = c - hi, mid = rb(r1);
        return __builtin_bit_cast(bf16x8, (u32x4){h ? 0u : pk(hi, mid), h ? 0u : pk(r1 - mid, 0.f), 0u, 0u});
      };
      auto epi = [&](int tile, int part) {
        const int f = tile & 1, t = tile >> 1;
#pragma unroll
        for (int g = 2 * part; g < 2 * part + 2; ++g) {
          float v0 = acc[f][t][2 * g] * rr[t]; asm("" : "+v"(v0));
          pw[g] = pk(v0, acc[f][t][2 * g + 1] * rr[t]);
        }
        if (part & 1) {
          const int g = part >> 1;
          auto r0 = __builtin_amdgcn_permlane32_swap(pw[4 * g], pw[4 * g + 2], false, false);
          auto r1 = __builtin_amdgcn_permlane32_swap(pw[4 * g + 1], pw[4 * g + 3], false, false);
          __builtin_amdgcn_raw_buffer_store_b128((u32x4){r0[0], r1[0], r0[1], r1[1]}, rs, qo + t * 122880 + (4 * f + 2 * g) * 512, 0, 16);
        }
      };
#pragma unroll
      for (int n = 0; n < 20; ++n) {
        const int tile = n < 8 ? n >> 2 : n < 10 ? n - 8 : n < 15 ? 2 : 3, e = n < 8 ? n & 3 : n < 10 ? 4 : (n - 10) % 5, f = tile & 1, t = tile >> 1;
        acc[f][t] = e < 4 ? MFMA32(FB[e].v[f], FB[e].v[2 + t], acc[f][t]) : MFMA32(ax[f], bx[t], acc[f][t]);
        if (n == 0) { const unsigned m = pk(-st0[0], -st0[0]); bx[0] = __builtin_bit_cast(bf16x8, (u32x4){m, m, 0u, 0u}); rr[0] = st0[1] * qs; c0 = cp[0] + cp[1]; asm volatile("" : "+v"(bx[0]), "+v"(rr[0]), "+v"(c0)); }
        if (n == 1) { const unsigned m = pk(-st1[0], -st1[0]); bx[1] = __builtin_bit_cast(bf16x8, (u32x4){m, m, 0u, 0u}); rr[1] = st1[1] * qs; c1 = cp[2] + cp[3]; asm volatile("" : "+v"(bx[1]), "+v"(rr[1]), "+v"(c1)); }
        if (n == 1 && (lane & 15) < 2) { *(LAS f32x4*)(cown + 1024) = cs0; *(LAS f32x4*)(cown + 1024 + 128) = cs1; }
        if (n == 2) { cp[0] = *(const LAS float*)(lds0 + L_CS + 1024 + w * 256 + l31 * 4); cp[1] = *(const LAS float*)(lds0 + L_CS + 1024 + w * 256 + 128 + l31 * 4); }
        if (n == 5) { ax[0] = split3(c0 + cp[0]); asm volatile("" : "+v"(ax[0])); }
        if (n == 6) { ax[1] = split3(c1 + cp[1]); asm volatile("" : "+v"(ax[1])); }
        if (n >= 10) epi((n - 10) >> 2, (n - 10) & 3);
        FENCE();
        if (n == 9 || n == 13 || n == 17) FST(24 + (n - 9) / 4);
      }
      ST(2); FST(20);
      epi(2, 2); epi(2, 3);
#pragma unroll
      for (int part = 0; part < 4; ++part) epi(3, part);
      FST(21);
      WAITV(0);
      if (lane == 0 && __hip_atomic_fetch_add((LAS unsigned*)(lds0 + L_CNT), 1u, __ATOMIC_RELAXED, __HIP_MEMORY_SCOPE_WORKGROUP) == 3u)
        stg64(ws + WS_FLAG1 + (tr * 16 + ct) * 256, gran(tag, ~tag));
      ST(3); FST(22);
    }
  } else {
    if (w < 4) router_load();
#pragma unroll 1
    for (int e = 0; e < 15; ++e) {
      const unsigned L = 512 * e + tid, r = (L * 52429) >> 22, pc = L - r * 80;
      glds(r * 1280 + (pc ^ (((r & 3) | ((r >> 1) & 4)) << 1)) * 16, (const char*)p.wd + (2 * xc + i) * 96 * 1280, lds0 + (8 * e + w) * 1024);
    }
    if (w < 4) {
      router_dots();
#pragma unroll
      for (int k = 0; k < 14; ++k) router_step(k);
    }
    if (tid == 384) *(LAS u32x4*)(lds0 + L_CNT) = (u32x4){};
    BAR();
    if (w == 0) {
#pragma unroll
      for (int k = 14; k < 24; ++k) router_step(k);
    }
    WAITV(0); BAR(); ST(1);
    if (w >= 4) {
      lds_tag();
#pragma unroll
      for (int n = 0; n < 30; ++n) {
        const unsigned a = lds0 + (8 * g4 + q4) * 1280 + (((2 * (w - 4) + n / 15) ^ (q4 | ((g4 & 1) << 2))) * 32) + 8 * p4 + (n % 15 / 5) * 40960 + (n % 5) * 256;
        wdf[n] = trfrag(a, a + 5120);
      }
    } else if (w == 0) {
      unsigned k1 = 0, k2 = 0;
      { const u64 t0 = __builtin_amdgcn_s_memrealtime() ; while (__builtin_amdgcn_s_memrealtime() - t0 < KEYDELAY) __builtin_amdgcn_s_sleep(8); }
      keys_wait(rs, lane, tag, k1, k2);
      const char* sa = ws + WS_STATS + ((2047u - (lane & 1 ? k2 : k1)) & 2047u) * 8;
      u64 sg = ldg64(sa);
#pragma unroll 1
      for (unsigned sp = 0; sp < SPIN_CAP && !__all((unsigned)(sg >> 32) == tag); ++sp) sg = ldg64(sa);
      if (lane < 2) { *(LAS unsigned*)(lds0 + L_TOP + lane * 4) = lane ? k2 : k1; *(LAS unsigned*)(lds0 + L_TOP + 8 + lane * 4) = (unsigned)sg; }
    }
    BAR(); ST(2);
  }
  ST(4); FST(35);
  const u64 ftag = gran(tag, ~tag);

  const int tg3 = w & 3, kh = w >> 2, tok0 = 256 * xc + 64 * (i >> 3), n0 = 80 * cs80;
  const bool two = w == 4 || w == 5;
  const unsigned it0 = tid, it1 = two ? tid + 256 : tid, tk0 = (it0 * 6554) >> 16, tk1 = (it1 * 6554) >> 16, ch0 = it0 - 10 * tk0, ch1 = it1 - 10 * tk1;
  const unsigned eo0 = (tok0 + tk0) * 1280 + n0 * 2 + ch0 * 16, eo1 = (tok0 + tk1) * 1280 + n0 * 2 + ch1 * 16;
  u32x4 xv0, xv1; u64 sg0_, sg1_;
  auto pre3 = [&]() {
    xv0 = *(const GAS u32x4*)((const char*)p.x + eo0); sg0_ = ldg64(ws + WS_STATS + (tok0 + tk0) * 8);
    xv1 = *(const GAS u32x4*)((const char*)p.x + eo1); sg1_ = ldg64(ws + WS_STATS + (tok0 + tk1) * 8);
  };

  {
    const int R = i >> 2, hd = 4 * xc + (i & 3), p0 = (5 * hd) >> 1, oh = hd & 1;
    const int pf = p0 + oh, pp = oh ? p0 : p0 + 2, cc = (lane & 1 ? p0 + 2 : p0) >> 4;
    if (w < 4) {
      const int lo = w < 2, tu = 2 * w + 1 - lo, tv = 2 * w + lo;
      poll(ws + WS_FLAG1 + ((2 * R + (w >> 1)) * 16 + cc) * 256, lane < 2, ftag);
      ST(5); FST(36);
      bf16x8 qf[2][2];
      { const unsigned a0 = (pf + h) * 512 + l31 * 16, a1 = pp * 512 + l31 * 16 + oh * 8, ou = WS_QKV + (8 * R + tu) * 122880, ov = WS_QKV + (8 * R + tv) * 122880;
        const u32x4 u0 = __builtin_amdgcn_raw_buffer_load_b128(rs, a0, ou, 16), v0 = __builtin_amdgcn_raw_buffer_load_b128(rs, a0, ov, 16);
        const u32x2 u1 = __builtin_amdgcn_raw_buffer_load_b64(rs, a1, ou, 16), v1 = __builtin_amdgcn_raw_buffer_load_b64(rs, a1, ov, 16);
        qf[0][0] = __builtin_bit_cast(bf16x8, u0); qf[0][1] = __builtin_bit_cast(bf16x8, (u32x4){h ? 0u : u1[0], h ? 0u : u1[1], 0u, 0u});
        qf[1][0] = __builtin_bit_cast(bf16x8, v0); qf[1][1] = __builtin_bit_cast(bf16x8, (u32x4){h ? 0u : v1[0], h ? 0u : v1[1], 0u, 0u}); }
      pre3();
      f32x16 cz = {}, ca, cb;
#pragma unroll
      for (int r = 0; r < 16; ++r) {
        const bool le = (r & 3) + 8 * (r >> 2) + 4 * h <= l31;
        const float d = le ? 0.f : -1e30f, f = le || R == 0 ? -1e30f : 0.f;
        ca[r] = lo ? d : f; cb[r] = lo ? f : d;
      }
      const unsigned ka = lds0 + L_KV + h * 512 + l31 * 16;
      const int pi = min(4 * (g4 & 1) + p4, 5);
      const unsigned va = lds0 + L_KV + L_V + (pi >> 1) * VPS + (pi & 1) * 8 + (4 * h + q4) * 16;
      struct KF { bf16x8 a, b; };
      auto kread = [&](int kt) {
        const unsigned a = ka + kt * KVS;
        return KF{lds128(a), lds128(a + 1024)};
      };
      auto vread = [&](int kt) { const unsigned v = va + kt * KVS; return KF{trfrag(v, v + 128), trfrag(v + 256, v + 384)}; };
      auto ex = [&](const f32x16& s, int e) { return pk(__builtin_amdgcn_exp2f(s[2 * e]), __builtin_amdgcn_exp2f(s[2 * e + 1])); };
      const int P0 = lo ? 6 : 8, P1 = w == 0 ? 4 : w == 1 ? 8 : w == 2 ? 6 : 10, P2 = lo ? 2 + 2 * w : 6 + 2 * w;
      const int tp[10] = {P0, P0 + 1, P1, P1 + 1, lo ? 2 * w + 8 : 2 * w + 1, lo ? 2 * w + 9 : 2 * w, P2, P2 + 1, lo ? 2 * w + 1 : 2 * w + 8, lo ? 2 * w : 2 * w + 9};
      auto at = [&](int ps) { const int e = (ps == 1 || ps == 3 || ps == 7), t = tp[ps - e]; return (R == 0 && t < 8 ? 8 : t) + e; };
      auto setz = [&](int ps) {
        if (ps == 1 || ps == 3 || ps == 5 || ps == 7 || ps == 9) return;
        if (R == 0) { const float z = tp[ps] < 8 ? -1e30f : 0.f;
#pragma unroll
          for (int r = 0; r < 16; ++r) cz[r] = z;
          asm volatile("" : "+v"(cz)); } };
      setz(0);
      asm volatile("" : "+v"(ca), "+v"(cb), "+v"(cz), "+v"(qf[0][0]), "+v"(qf[0][1]), "+v"(qf[1][0]), "+v"(qf[1][1]));
      BAR(); ST(6); FST(39);
      KF K0, K1, K2, V0, V1, V2; f32x16 S0, S1, o[2] = {};
      K0 = kread(at(0)); V0 = vread(at(0));
      S0 = MFMA32(K0.b, qf[0][1], MFMA32(K0.a, qf[0][0], cz));
      u32x4 pa = {}, pb = {}; float x0, x1, x2, x3;
#define SET3(N, p) ((p) % 3 == 0 ? N##0 : (p) % 3 == 1 ? N##1 : N##2)
#define EXP4(s, e) do { x0 = __builtin_amdgcn_exp2f(s[4 * e]); x1 = __builtin_amdgcn_exp2f(s[4 * e + 1]); x2 = __builtin_amdgcn_exp2f(s[4 * e + 2]); x3 = __builtin_amdgcn_exp2f(s[4 * e + 3]); \
                        asm volatile("" : "+v"(x0), "+v"(x1), "+v"(x2), "+v"(x3)); FENCE(); } while (0)
#define PACK2(d, e) do { d[e] = pk(x0, x1); d[e + 1] = pk(x2, x3); asm volatile("" : "+v"(d)); FENCE(); } while (0)
      auto unit = [&](auto U, f32x16& sc, f32x16& sn) {
        constexpr int u = decltype(U)::value, np = u + 1 < 18 ? UP[u + 1] : 0, nq = u + 1 < 18 ? UQ[u + 1] : 0, pu = u > 0 ? u - 1 : 0;
        if constexpr (u == 2 || u == BC_AT) BAR();
        FST(40 + u);
        if constexpr (u + 1 < 18) { if constexpr (np != UP[u]) setz(np); sn = MFMA32(SET3(K, np).a, qf[nq][0], u + 1 == 8 || u + 1 == 10 ? ca : u + 1 >= 16 ? cb : cz); FENCE(); }
        if constexpr (u > 0) PACK2(pb, 2);
        EXP4(sc, 0);
        if constexpr (u > 0) { o[UQ[pu]] = MFMA32(SET3(V, UP[pu]).b, __builtin_bit_cast(bf16x8, pb), o[UQ[pu]]); FENCE(); }
        if constexpr (u + 2 < 18 && UP[u + 2] != UP[u + 1]) SET3(K, UP[u + 2]) = kread(at(UP[u + 2]));
        if constexpr (u + 1 < 18 && np != UP[u]) SET3(V, np) = vread(at(np));
        PACK2(pa, 0);
        EXP4(sc, 1);
        if constexpr (u + 1 < 18) { sn = MFMA32(SET3(K, np).b, qf[nq][1], sn); FENCE(); }
        PACK2(pa, 2);
        EXP4(sc, 2);
        o[UQ[u]] = MFMA32(SET3(V, UP[u]).a, __builtin_bit_cast(bf16x8, pa), o[UQ[u]]); FENCE();
        PACK2(pb, 0);
        EXP4(sc, 3);
      };
#define UNIT2(n) unit(IC<n>{}, S0, S1); unit(IC<n + 1>{}, S1, S0);
      UNIT2(0) UNIT2(2) UNIT2(4) UNIT2(6) UNIT2(8) UNIT2(10) UNIT2(12) UNIT2(14) UNIT2(16)
      PACK2(pb, 2);
      o[0] = MFMA32(V0.b, __builtin_bit_cast(bf16x8, pb), o[0]);
      auto rowsum = [&](const f32x16& a) { auto r = __builtin_amdgcn_permlane32_swap(__float_as_uint(a[8]), __float_as_uint(a[8]), false, false); return __uint_as_float(r[1]); };
      float l[2] = {rowsum(o[1]), rowsum(o[0])};
      if (FORCE_FB || !__all(l[0] > 0x1p-60f && l[0] < 0x1p60f && l[1] > 0x1p-60f && l[1] < 0x1p60f)) {
        int qt = tu;
#pragma unroll 1
        for (int e = 0; e < 2; ++e) {
          const int k0 = R > 0 ? qt : 8;
          auto score = [&](int kt, float base) {
            f32x16 c;
#pragma unroll
            for (int r = 0; r < 16; ++r) { const bool le = (r & 3) + 8 * (r >> 2) + 4 * h <= l31; c[r] = (kt == qt + 8 && !le) || (kt == qt && le) ? -1e30f : base; }
            const KF k = kread(kt);
            return MFMA32(k.b, qf[0][1], MFMA32(k.a, qf[0][0], c));
          };
          float m = -3e38f;
#pragma unroll 1
          for (int kt = k0; kt <= qt + 8; ++kt) { const f32x16 s = score(kt, 0.f);
#pragma unroll
            for (int r = 0; r < 16; ++r) m = fmaxf(m, s[r]); }
          { auto r = __builtin_amdgcn_permlane32_swap(__float_as_uint(m), __float_as_uint(m), false, false); m = fmaxf(__uint_as_float(r[0]), __uint_as_float(r[1])); }
          o[0] = (f32x16){};
#pragma unroll 1
          for (int kt = k0; kt <= qt + 8; ++kt) {
            const f32x16 s = score(kt, -m); const KF v = vread(kt);
            o[0] = MFMA32(v.a, __builtin_bit_cast(bf16x8, (u32x4){ex(s, 0), ex(s, 1), ex(s, 2), ex(s, 3)}), o[0]);
            o[0] = MFMA32(v.b, __builtin_bit_cast(bf16x8, (u32x4){ex(s, 4), ex(s, 5), ex(s, 6), ex(s, 7)}), o[0]);
          }
          const f32x16 to = o[0]; o[0] = o[1]; o[1] = to;
          const bf16x8 t0 = qf[0][0], t1 = qf[0][1]; qf[0][0] = qf[1][0]; qf[0][1] = qf[1][1]; qf[1][0] = t0; qf[1][1] = t1;
          qt = tv;
        }
        l[0] = rowsum(o[1]); l[1] = rowsum(o[0]);
      }
      ST(7); FST(59);
#pragma unroll
      for (int e = 0; e < 2; ++e) {
        const f32x16& a = o[1 - e]; const float ri = __builtin_amdgcn_rcpf(l[e]);
        const unsigned e0 = pk(a[0] * ri, a[1] * ri), e1 = pk(a[2] * ri, a[3] * ri), e2 = pk(a[4] * ri, a[5] * ri), e3 = pk(a[6] * ri, a[7] * ri);
        const u32x2 el = {pk(a[8] * ri, a[9] * ri), pk(a[10] * ri, a[11] * ri)};
        auto r0 = __builtin_amdgcn_permlane32_swap(e0, e2, false, false), r1 = __builtin_amdgcn_permlane32_swap(e1, e3, false, false);
        const unsigned ob = WS_O + (16 * R + 2 * (e ? tu : tv) + (l31 >> 4)) * 20480;
        __builtin_amdgcn_raw_buffer_store_b128((u32x4){r0[0], r1[0], r0[1], r1[1]}, rs, ob + (2 * hd + h) * 256 + (lane & 15) * 16, 0, 16);
        if (h == 0) __builtin_amdgcn_raw_buffer_store_b64(el, rs, ob + 16384 + hd * 128 + (lane & 15) * 8, 0, 16);
      }
      WAITV(0);
      if (lane == 0 && __hip_atomic_fetch_add((LAS unsigned*)(lds0 + L_CNT + 4), 1u, __ATOMIC_RELAXED, __HIP_MEMORY_SCOPE_WORKGROUP) == 3u)
        stg64(ws + WS_FLAG2 + (R * 32 + hd) * 256, ftag);
      ST(8); FST(60);
      BAR();
    } else {
      __builtin_amdgcn_s_setprio(2);
      const int j = w - 4;
      const WoOff woo = wo_offsets(lane);
    const int sp_ = 2 * xc + i;
    unsigned ga[3]; u32x2 pa_[30];
    auto gather = [&]() {
      asm volatile("" ::: "memory");
#pragma unroll
      for (int n = 0; n < 30; ++n) pa_[n] = __builtin_amdgcn_raw_buffer_load_b64(rs, ga[n / 10], WS_PARTA + (n % 10) * 3072, 16);
    };
#pragma unroll
      for (int r = 0; r < 3; ++r) { const unsigned e = min(144 * j + 64 * r + lane, 575), rc = (e * 683) >> 16, F = 96 * sp_ + e - 96 * rc; ga[r] = ((F >> 6) * 3840 + rc * 64 + (F & 63)) * 8; asm volatile("" : "+v"(ga[r])); }
      const int kt[4] = {6 + j, j < 2 ? 4 + j : 8 + j, j, 12 + j};
      { const int e = lane >> 2, T = 8 * R - 8 + (e == 0 ? kt[0] : e == 1 ? kt[1] : e == 2 ? kt[2] : kt[3]);
        const bool act = lane < 16 && T >= 0;
        poll(ws + WS_FLAG1 + (act ? ((T >> 2) * 16 + (lane & 2 ? 10 : 5) + cc) * 256 : 0), act, ftag); }
      ST(5); FST(36);
      const unsigned so0 = (80 + pf + h) * 512 + l31 * 16, so1 = (h ? (160 + pf) * 512 : (80 + pp) * 512 + 8 * oh) + l31 * 16, so2 = (h ? (160 + pp) * 512 + 8 * oh : (161 + pf) * 512) + l31 * 16;
      struct T3 { u32x4 a, b, c; };
      auto fetch = [&](int t) {
        const int T = 8 * R - 8 + t; const unsigned m = T < 0 ? 0x40000000u : 0u, o = WS_QKV + max(T, 0) * 122880;
        return T3{__builtin_amdgcn_raw_buffer_load_b128(rs, so0 | m, o, 16), __builtin_amdgcn_raw_buffer_load_b128(rs, so1 | m, o, 16), __builtin_amdgcn_raw_buffer_load_b128(rs, so2 | m, o, 16)};
      };
      auto stage = [&](int t, T3 v) {
        if (R == 0 && t < 8) return;
        const unsigned kd = lds0 + L_KV + t * KVS + l31 * 16;
        if (h) v.c[2] = v.c[3] = ONE2;
        *(LAS u32x4*)(kd + h * 512) = v.a; *(LAS u32x4*)(kd + 1024 + h * 512) = v.b; *(LAS u32x4*)(kd + L_V + (1 + h) * VPS) = v.c;
      };
      const T3 t0 = fetch(kt[0]), t1 = fetch(kt[1]);
      FENCE();
      stage(kt[0], t0);
      FENCE();
      const T3 t2 = fetch(kt[2]), t3 = fetch(kt[3]);
      FENCE();
      BAR(); ST(6); FST(39);
      auto pause = [&](int n) { for (int e = 0; e < n; ++e) __builtin_amdgcn_s_sleep(1); };
      pause(j); stage(kt[1], t1);
      BAR(); FST(40);
      pause(2 * j); stage(kt[2], t2); pause(1); stage(kt[3], t3);
      unsigned pf = 0;
      if (!spare) {
        const unsigned po = ((i >> 3) * 160 + j * 40 + (lane >> 1)) * 1280 + (lane & 1) * 128;
        const char* pb = (const char*)p.wo + ((cs80 * 160) & ~127);
        asm volatile("global_load_dword %0, %1, %2" : "+v"(pf) : "v"(po), "s"(pb) : "memory");
        if (lane < 16) asm volatile("global_load_dword %0, %1, %2" : "+v"(pf) : "v"(po + 32 * 1280), "s"(pb) : "memory");
        pre3();
        BAR(); FST(41);
        asm volatile("s_waitcnt vmcnt(0)" : "+v"(pf) :: "memory");
        FST(44);
        wo_steps(5 * j, 5 * j + 5, woo, p.wo, cs80, lds0);
      } else {
        pre3();
        gather();
        FENCE();
        BAR(); FST(41);
        wo_steps(5 * j, 5 * j + 5, woo, p.wo, cs80, lds0);
        FST(45);
#pragma unroll 1
        for (unsigned sp = 0; sp < SPIN_CAP; ++sp) {
          WAITV(0);
          unsigned bad[5];
#pragma unroll
          for (int n = 0; n < 5; ++n) bad[n] = (pa_[n][1] ^ tag) | (pa_[n + 5][1] ^ tag) | (pa_[n + 10][1] ^ tag) | (pa_[n + 15][1] ^ tag) | (pa_[n + 20][1] ^ tag) | (pa_[n + 25][1] ^ tag);
          if (__all((bad[0] | bad[1] | bad[2] | bad[3] | bad[4]) == 0)) break;
          gather();
        }
#pragma unroll
        for (int r = 0; r < 3; ++r) {
          auto v = [&](int kg) { return __uint_as_float(pa_[10 * r + kg][0]); };
          const float sm = (((v(0) + v(1)) + (v(2) + v(3))) + ((v(4) + v(5)) + (v(6) + v(7)))) + (v(8) + v(9));
          *(LAS float*)(lds0 + L_TMP + min(144 * j + 64 * r + lane, 575) * 4) = sm;
        }
        FST(46);
        __builtin_amdgcn_s_waitcnt(0xC07F);
        if (lane == 0) __hip_atomic_fetch_add((LAS unsigned*)(lds0 + L_CNT + 8), 1u, __ATOMIC_RELAXED, __HIP_MEMORY_SCOPE_WORKGROUP);
#pragma unroll 1
        for (unsigned sp = 0; sp < SPIN_CAP; ++sp) { if (__builtin_amdgcn_readfirstlane(*(volatile LAS unsigned*)(lds0 + L_CNT + 8)) >= 4u) break; __builtin_amdgcn_s_sleep(1); }
        asm volatile("" ::: "memory");
        FST(47);
      const unsigned hb = lds0 + L_TMP + 2304 + j * 384;
#pragma unroll
      for (int r = 0; r < 3; ++r) {
        const int e = 64 * r + lane, tk = e >= 96; const unsigned a = lds0 + L_TMP + (e - 96 * tk) * 4, sg = *(const LAS unsigned*)(lds0 + L_TOP + 8 + tk * 4);
        const float m = blo(sg), rd = __builtin_amdgcn_rcpf(bhi(sg));
        const float g = rb((*(const LAS float*)(a + tk * 384) - m * *(const LAS float*)(a + 768)) * rd);
        const float u = rb((*(const LAS float*)(a + 1152 + tk * 384) - m * *(const LAS float*)(a + 1920)) * rd);
        *(LAS u16*)(hb + e * 2) = (u16)pk(rb(g * __builtin_amdgcn_rcpf(1.f + __expf(-g))) * u, 0.f);
      }
      bf16x8 hf[3];
#pragma unroll
      for (int s = 0; s < 3; ++s) hf[s] = lds128(hb + (lane & 1) * 192 + g4 * 16 + s * 64);
      f32x4 y[10] = {};
#pragma unroll
      for (int s = 0; s < 3; ++s)
#pragma unroll
        for (int n = 0; n < 10; ++n) y[n] = MFMA16(hf[s], wdf[(n / 5) * 15 + 5 * s + n % 5], y[n]);
#pragma unroll
      for (int n = 0; n < 5; ++n) {
        const float v = g4 & 1 ? (g4 & 2 ? y[n + 5][1] : y[n + 5][0]) : (g4 & 2 ? y[n][1] : y[n][0]);
        __builtin_amdgcn_raw_buffer_store_b64((u32x2){__float_as_uint(v), tag}, rs, (h * 640 + 32 * j + l31) * 8 + n * 1024, WS_YPART + sp_ * 10240, 16);
      }
      ST(3);
      }
      FST(42);
      if (!spare) WAITV(0);
      FST(43);
      __builtin_amdgcn_s_setprio(0);
      BAR();
    }
  }

  {
    const int tg = tg3;
    const unsigned wa = lds0 + L_WO + (kh * 320 + 4 * g4 + q4) * 160 + 8 * p4;
    constexpr int NPRE = 6;
    bf16x8 wf[NPRE][5];
#pragma unroll
    for (int s = 0; s < NPRE; ++s)
#pragma unroll
      for (int n = 0; n < 5; ++n) wf[s][n] = trfrag(wa + s * 5120 + n * 32, wa + s * 5120 + 2560 + n * 32);
#pragma unroll 1
    for (unsigned sp = 0; sp < SPIN_CAP && !__all((unsigned)(sg0_ >> 32) == tag && (unsigned)(sg1_ >> 32) == tag); ++sp) {
      sg0_ = ldg64(ws + WS_STATS + (tok0 + tk0) * 8); sg1_ = ldg64(ws + WS_STATS + (tok0 + tk1) * 8); }
    f32x4 base[4];
#pragma unroll
    for (int e = 0; e < 4; ++e) {
      const unsigned sg = (unsigned)(e < 2 ? sg0_ : sg1_); const float m = blo(sg), rd = __builtin_amdgcn_rcpf(bhi(sg));
#pragma unroll
      for (int c = 0; c < 4; ++c) { const unsigned u = (e < 2 ? xv0 : xv1)[2 * (e & 1) + (c >> 1)]; const float v = c & 1 ? bhi(u) : blo(u); base[e][c] = v + (v - m) * rd; }
    }
    const u32x2 kk12 = *(const LAS u32x2*)(lds0 + L_TOP);
    const unsigned k1 = __builtin_amdgcn_readfirstlane(kk12[0]), k2 = __builtin_amdgcn_readfirstlane(kk12[1]);
    const unsigned i1 = 2047u - (k1 & 0xffffu) - tok0, i2 = 2047u - (k2 & 0xffffu) - tok0;
#pragma unroll
    for (int s = 0; s < NPRE; ++s)
#pragma unroll
      for (int n = 0; n < 5; ++n) asm volatile("" : "+v"(wf[s][n]));
    asm volatile("" : "+v"(base[0]), "+v"(base[1]), "+v"(base[2]), "+v"(base[3]));
    poll(ws + WS_FLAG2 + (xc * 32 + 16 * kh + (lane & 15)) * 256, lane < 16, ftag);
    ST(9); FST(61);
    const unsigned ob = WS_O + (tok0 / 16 + tg) * 20480;
    u32x4 of[10];
    auto oload = [&](int s) {
      if (s < 8) of[s] = __builtin_amdgcn_raw_buffer_load_b128(rs, lane * 16 + s * 1024, ob + kh * 8192, 16);
      else {
        const unsigned a = (8 * (s - 8) + 2 * g4) * 128 + (lane & 15) * 8;
        const u32x2 u0 = __builtin_amdgcn_raw_buffer_load_b64(rs, a, ob + 16384 + kh * 2048, 16), u1 = __builtin_amdgcn_raw_buffer_load_b64(rs, a + 128, ob + 16384 + kh * 2048, 16);
        of[s] = (u32x4){u0[0], u0[1], u1[0], u1[1]};
      }
    };
#pragma unroll
    for (int s = 0; s < OPRE; ++s) oload(s);
    FENCE();
    FST(62);
    const bool own = (i1 < 64u || i2 < 64u) && w < 5;
    const unsigned ya = own ? ((tid >= 160) * 640 + n0 + ((tid - 160 * (tid >= 160)) >> 1)) * 8 + (tid & 1) * 81920 : 0x40000000u;
    u32x2 yv[8];
    auto yload = [&]() {
      asm volatile("" ::: "memory");
#pragma unroll
      for (int e = 0; e < 8; ++e) yv[e] = __builtin_amdgcn_raw_buffer_load_b64(rs, ya, WS_YPART + e * 10240, 16);
    };
    f32x4 acc[5] = {};
#pragma unroll
    for (int s = 0; s < 10; ++s) {
      if (s + OPRE < 10) oload(s + OPRE);
      if (s + OPRE == 10 && own) yload();
      FENCE();
#pragma unroll
      for (int n = 0; n < 5; ++n) acc[n] = MFMA16(wf[s % NPRE][n], __builtin_bit_cast(bf16x8, of[s]), acc[n]);
      if (s + NPRE < 10) {
#pragma unroll
        for (int n = 0; n < 5; ++n) wf[s][n] = trfrag(wa + (s + NPRE) * 5120 + n * 32, wa + (s + NPRE) * 5120 + 2560 + n * 32);
      }
      FENCE();
    }
    ST(10); FST(63);
    if (own) {
#pragma unroll 1
      for (unsigned sp = 0; sp < SPIN_CAP; ++sp) {
        unsigned bad = 0;
#pragma unroll
        for (int e = 0; e < 8; ++e) bad |= yv[e][1] ^ tag;
        if (__all(bad == 0)) break;
        yload();
      }
      float s = 0.f;
#pragma unroll
      for (int e = 0; e < 8; ++e) s += __uint_as_float(yv[e][0]);
      s += __uint_as_float(dpp<0xB1>(__float_as_uint(s)));
      const unsigned kk = (tid >= 160 ? k2 : k1) >> 16;
      const float r = blo(kk & 0x8000u ? kk & 0x7fffu : ~kk & 0xffffu);
      if (!(tid & 1)) *(LAS float*)(lds0 + L_UPD + (tid >> 1) * 4) = rb(rb(s) * rb(__builtin_amdgcn_rcpf(1.f + __expf(-r))));
    }
#pragma unroll
    for (int n = 0; n < 5; ++n) *(LAS f32x4*)(lds0 + L_RED + kh * 21504 + (16 * tg + (lane & 15)) * 336 + (16 * n + 4 * g4) * 4) = acc[n];
    BAR();
    auto fin = [&](unsigned tk, unsigned ch, unsigned eo, f32x4 b0, f32x4 b1) {
      const unsigned a = lds0 + L_RED + tk * 336 + ch * 32;
      b0 += *(const LAS f32x4*)a + *(const LAS f32x4*)(a + 21504); b1 += *(const LAS f32x4*)(a + 16) + *(const LAS f32x4*)(a + 21504 + 16);
      if (tk == i1 || tk == i2) { const unsigned u = lds0 + L_UPD + (tk == i1 ? 0 : 320) + ch * 32; b0 += *(const LAS f32x4*)u; b1 += *(const LAS f32x4*)(u + 16); }
      *(GAS u32x4*)((char*)p.out + eo) = (u32x4){pk(b0[0], b0[1]), pk(b0[2], b0[3]), pk(b1[0], b1[1]), pk(b1[2], b1[3])};
    };
    fin(tk0, ch0, eo0, base[0], base[1]);
    if (two) fin(tk1, ch1, eo1, base[2], base[3]);
    ST(11);
  }
}

__global__ void k_spin(const unsigned long long* st) {
  __shared__ unsigned long long a[256], b[256];
  unsigned long long mn = ~0ull, mx = 0;
  for (int w = 0; w < 8; ++w) { unsigned long long s0 = st[((threadIdx.x * 8 + w) * 48 + 0) * 2], e = st[((threadIdx.x * 8 + w) * 48 + 2) * 2]; mn = s0 < mn ? s0 : mn; if (w < 4) mx = e > mx ? e : mx; }
  a[threadIdx.x] = mn; b[threadIdx.x] = mx;
  __syncthreads();
  if (threadIdx.x == 0) {
    unsigned long long s = ~0ull, e = 0, sum = 0;
    for (int i = 0; i < 256; ++i) s = a[i] < s ? a[i] : s;
    for (int i = 16; i < 256; ++i) { e = b[i] > e ? b[i] : e; sum += b[i] - s; }
    if (2 == 11 || 2 == 0) for (int i = 0; i < 16; ++i) e = b[i] > e ? b[i] : e;
    unsigned long long span = 1 ? sum * 100 / 240 : (e - s) * 100; if (span > 3000000) span = 3000000;
    unsigned long long t0 = __builtin_amdgcn_s_memrealtime();
    while (__builtin_amdgcn_s_memrealtime() - t0 < span) __builtin_amdgcn_s_sleep(8);
  }
}
extern "C" void kernel_launch(void* const* d_in, const int* in_sizes, int n_in, void* d_out, int out_size, void* d_ws, size_t ws_size,
                              hipStream_t stream) {
  Params p;
  p.x = (const u16*)d_in[0]; p.wqkv = (const u16*)d_in[1]; p.wo = (const u16*)d_in[2]; p.wr = (const u16*)d_in[4];
  p.wg = (const u16*)d_in[5]; p.wu = (const u16*)d_in[6]; p.wd = (const u16*)d_in[7]; p.out = (u16*)d_out; p.ws = (char*)d_ws;
  fused_block<<<256, 512, 0, stream>>>(p);
  k_spin<<<1, 256, 0, stream>>>((const unsigned long long*)((char*)d_ws + WS_STAMP));
}
```

```cpp
#define STAMP
#include <hip/hip_runtime.h>
#include <stdint.h>

typedef unsigned short u16;
typedef unsigned long long u64;
typedef short bf16x8 __attribute__((ext_vector_type(8)));
typedef short s16x4 __attribute__((ext_vector_type(4)));
typedef float f32x2 __attribute__((ext_vector_type(2)));
typedef float f32x4 __attribute__((ext_vector_type(4)));
typedef float f32x16 __attribute__((ext_vector_type(16)));
typedef unsigned u32x2 __attribute__((ext_vector_type(2)));
typedef unsigned u32x4 __attribute__((ext_vector_type(4)));
typedef __bf16 bf2 __attribute__((ext_vector_type(2)));
#define LAS __attribute__((address_space(3)))
#define GAS __attribute__((address_space(1)))
#define DI __device__ __forceinline__

constexpr unsigned WS_GEN = 0, WS_FLAG1 = 4096, WS_FLAG2 = 131072, WS_CAND = 262144, WS_STATS = 278528, WS_PARTA = 524288,
                   WS_YPART = 1572864, WS_QKV = 2097152, WS_O = 10485760, WS_END = 13631488, WS_STAMP = 16777216;
constexpr unsigned L_WO = 0, L_KV = 102400, L_FFNA = 65536;
constexpr unsigned KVS = 3264, L_V = 1536, VPS = 576;
constexpr unsigned L_RED = L_KV, L_UPD = L_KV + 43008;
constexpr unsigned L_KEY = 154624, L_TOP = L_KEY + 32, L_CNT = L_KEY + 48  , L_KEYS = L_KEY + 64, L_STAT = L_KEY + 96, L_CS = L_STAT + 1024, L_TMP = L_CS + 2048;
constexpr unsigned L_Y = L_KV + 44032;
constexpr unsigned SPIN_CAP = 1u << 20;
constexpr unsigned ONE2 = 0x3f803f80u;
#define CODE_KB 36

constexpr int UP[20] = {0, 0, 1, 1, 2, 2, 3, 3, 4, 4, 5, 6, 6, 7, 7, 8, 8, 9, 9, 9}, UQ[18] = {0, 1, 0, 1, 0, 1, 0, 1, 0, 1, 1, 0, 1, 0, 1, 0, 1, 0};
template <int N> struct IC { static constexpr int value = N; };

struct Params { const u16 *x, *wqkv, *wo, *wr, *wg, *wu, *wd; u16* out; char* ws; };

#define ST(i) do { if (lane == 0) { GAS u64* sp_ = (GAS u64*)(p.ws + WS_STAMP) + ((blockIdx.x * 8 + w) * 48 + (i)) * 2; sp_[0] = __builtin_amdgcn_s_memrealtime(); sp_[1] = __builtin_amdgcn_s_memtime(); } } while (0)

#define FST(i) do {} while (0)
#define POLLGAP 1
#define WAITV(n) asm volatile("s_waitcnt vmcnt(" #n ")" ::: "memory")
#define REQ0 3
#define LA 3
#define LAMODE 1
#define BC_AT 7
#define FORCE_FB 0
#define WO1 0
#define OPRE 4
#define TLBW 0
#define HPRIO2 1
#define ROT 3
#define KEYDELAY 200
#define QDELAY 18
#define PRIO 0
#define WAITN_(n) WAITV(n)
#define WAITN(n) WAITN_(n)
#define WO_PRE 6
#define BAR() do { __builtin_amdgcn_s_waitcnt(0xC07F); asm volatile("s_barrier" ::: "memory"); } while (0)
#define FENCE() __builtin_amdgcn_sched_barrier(0)
#define MFMA32(a, b, c) __builtin_amdgcn_mfma_f32_32x32x16_bf16((a), (b), (c), 0, 0, 0)
#define MFMA16(a, b, c) __builtin_amdgcn_mfma_f32_16x16x32_bf16((a), (b), (c), 0, 0, 0)

DI void glds(unsigned voff, const void* sbase, unsigned lds) {
  asm volatile("s_mov_b32 m0, %2\n\ts_nop 0\n\tglobal_load_lds_dwordx4 %0, %1" :: "v"(voff), "s"(sbase), "s"(lds) : "memory");
}
DI bf16x8 lds128(unsigned a) { return *(const LAS bf16x8*)a; }
DI s16x4 ldstr(unsigned a) { return __builtin_amdgcn_ds_read_tr16_b64_v4i16((LAS s16x4*)a); }
DI bf16x8 trfrag(unsigned a, unsigned a2) { s16x4 lo = ldstr(a), hi = ldstr(a2); return __builtin_shufflevector(lo, hi, 0, 1, 2, 3, 4, 5, 6, 7); }
DI unsigned pk(float a, float b) { f32x2 v = {a, b}; return __builtin_bit_cast(unsigned, __builtin_convertvector(v, bf2)); }
DI float rb(float a) { return __uint_as_float(pk(a, 0.f) << 16); }
DI float blo(unsigned v) { return __uint_as_float(v << 16); }
DI float bhi(unsigned v) { return __uint_as_float(v & 0xffff0000u); }
DI float dot2(unsigned a, unsigned b, float c) { return __builtin_amdgcn_fdot2_f32_bf16(__builtin_bit_cast(bf2, a), __builtin_bit_cast(bf2, b), c, false); }
DI float xhalf_sum(float v) { auto r = __builtin_amdgcn_permlane32_swap(__float_as_uint(v), __float_as_uint(v), false, false); return __uint_as_float(r[0]) + __uint_as_float(r[1]); }
DI u64 ldg64(const char* a) { return __hip_atomic_load((GAS u64*)a, __ATOMIC_RELAXED, __HIP_MEMORY_SCOPE_AGENT); }
DI void stg64(char* a, u64 v) { __hip_atomic_store((GAS u64*)a, v, __ATOMIC_RELAXED, __HIP_MEMORY_SCOPE_AGENT); }
DI u64 gran(unsigned v, unsigned tag) { return ((u64)tag << 32) | v; }
template <int CTRL> DI unsigned dpp(unsigned v) { return __builtin_amdgcn_update_dpp(0u, v, CTRL, 0xf, 0xf, true); }
DI unsigned wave_umax(unsigned v) {
  v = max(v, dpp<0x128>(v)); v = max(v, dpp<0x124>(v)); v = max(v, dpp<0x122>(v)); v = max(v, dpp<0x121>(v));
  auto r = __builtin_amdgcn_permlane16_swap(v, v, false, false); v = max(r[0], r[1]);
  auto q = __builtin_amdgcn_permlane32_swap(v, v, false, false); return max(q[0], q[1]);
}
DI float sum32(float v) {
  v += __uint_as_float(dpp<0x128>(__float_as_uint(v))); v += __uint_as_float(dpp<0x124>(__float_as_uint(v)));
  v += __uint_as_float(dpp<0x122>(__float_as_uint(v))); v += __uint_as_float(dpp<0x121>(__float_as_uint(v)));
  auto r = __builtin_amdgcn_permlane16_swap(__float_as_uint(v), __float_as_uint(v), false, false);
  return __uint_as_float(r[0]) + __uint_as_float(r[1]);
}
DI void ln_stats(float s1, float s2, float& mb, float& db) {
  float mean = s1 * (1.f / 640.f); float var = fmaxf(s2 * (1.f / 640.f) - mean * mean, 0.f);
  mb = rb(mean); db = rb(__builtin_amdgcn_sqrtf(rb(rb(var) + 1e-5f)));
}
DI void poll(const char* addr, bool act, u64 want) {
#pragma unroll 1
  for (unsigned sp = 0; sp < SPIN_CAP; ++sp) { u64 v = ldg64(addr); if (__all(!act || v == want)) break; if (POLLGAP) __builtin_amdgcn_s_sleep(POLLGAP); }
  asm volatile("" ::: "memory");
}
struct Keys { u32x4 g[4]; };
DI bool keys_top2(const Keys& k, unsigned tag, unsigned& k1, unsigned& k2) {
  unsigned bad = 0, a = 0, b = 0;
#pragma unroll
  for (int e = 0; e < 4; ++e) {
    bad |= (k.g[e][1] ^ tag) | (k.g[e][3] ^ tag);
    const unsigned hi = max(k.g[e][0], k.g[e][2]), lo = min(k.g[e][0], k.g[e][2]);
    b = max(max(b, lo), min(a, hi)); a = max(a, hi);
  }
  if (!__all(bad == 0)) return false;
  k1 = wave_umax(a); k2 = wave_umax(a == k1 ? b : a);
  k1 = __builtin_amdgcn_readfirstlane(k1); k2 = __builtin_amdgcn_readfirstlane(k2);
  return true;
}
template <class RS> DI void keys_wait(RS rs, int lane, unsigned tag, unsigned& k1, unsigned& k2) {
#pragma unroll 1
  for (unsigned sp = 0; sp < SPIN_CAP; ++sp) {
    Keys k;
    asm volatile("" ::: "memory");
#pragma unroll
    for (int e = 0; e < 4; ++e) k.g[e] = __builtin_amdgcn_raw_buffer_load_b128(rs, lane * 16 + e * 1024, WS_CAND, 16);
    if (keys_top2(k, tag, k1, k2)) break;
    __builtin_amdgcn_s_sleep(16);
  }
}
struct WoOff { unsigned r[5], l[5]; };
DI WoOff wo_offsets(int lane) {
  WoOff o;
#pragma unroll
  for (int m = 0; m < 5; ++m) {
    const unsigned L = 64 * m + lane, rho = (L * 205) >> 11, c = L - rho * 10, q = rho & 3, g = (rho >> 2) & 3, t = rho >> 4;
    o.r[m] = (24 * (g >> 1) + 8 * (g & 1) + 4 * t + q) * 1280 + c * 16; o.l[m] = (40 * g + 16 + 4 * t + q) * 1280 + c * 16;
    asm volatile("" : "+v"(o.r[m]), "+v"(o.l[m]));
  }
  return o;
}
DI void wo_steps(int s0, int s1, const WoOff& o, const u16* wo, int cs80, unsigned lds0) {
#pragma unroll 1
  for (int u = s0; u < s1; ++u) {
    const int kh = u >= 10, st = u - 10 * kh;
    const char* sp = (const char*)wo + cs80 * 160 + (kh * 320 + (st < 8 ? st * 40 : (st - 8) * 160)) * 1280;
    const unsigned d = lds0 + L_WO + u * 5120;
    if (st < 8) {
#pragma unroll
      for (int m = 0; m < 5; ++m) glds(o.r[m], sp, d + m * 1024);
    } else {
#pragma unroll
      for (int m = 0; m < 5; ++m) glds(o.l[m], sp, d + m * 1024);
    }
  }
}

__global__ __launch_bounds__(512, 2) void fused_block(Params p) {
  __shared__ __attribute__((aligned(1024))) char smem[163840];
  const unsigned lds0 = (unsigned)(size_t)smem;
  const u64 t_in = __builtin_amdgcn_s_memrealtime(), c_in = __builtin_amdgcn_s_memtime();
  u64 pc0; asm volatile("s_getpc_b64 %0" : "=s"(pc0));
  const int tid = threadIdx.x, lane = tid & 63, w = __builtin_amdgcn_readfirstlane(tid >> 6);
  const int b = blockIdx.x, xc = b & 7, i = b >> 3;
  const bool spare = i < 2;
  const int xp = xc >> 1, odd = xc & 1;
  const int cs80 = i & 7;
  const int h = lane >> 5, l31 = lane & 31, g4 = lane >> 4, q4 = (lane & 15) >> 2, p4 = lane & 3;
  unsigned tag = 0;
  auto lds_tag = [&]() { tag = __builtin_amdgcn_readfirstlane(*(const LAS unsigned*)(lds0 + L_KEY)); };
  const unsigned j30 = (unsigned)(i - 2) & 31u, j7 = (j30 * 37) >> 8;
  const unsigned cj = j30 - j7 * 7;
  const unsigned trl = j30 < 28 ? j7 : (2 * odd + j30) & 3, ct = j30 < 28 ? (cj < 2 ? cj + 3 * odd : cj + 3 + 5 * odd) : 2;
  unsigned sn = ROT == 3 ? 3 * (trl + ct - 3 * (((trl + ct) * 43) >> 7)) + ((xc * 3) >> 3) : ROT == 2 ? 5 * ((trl + ct) & 1) + ((xc * 5) >> 3) : ROT == 1 ? (xc * 5) >> 2 : ROT == 4 ? (5 * ((trl + ct) & 3)) / 2 + (xc & 1) : 0;
  const unsigned tr = 4 * xp + trl;
  const int rb8 = 64 * xp + 32 * odd + i;
  const unsigned x0 = (lane >> 3) * 1280 + (((lane & 7) ^ g4) * 16), x1 = (8 + (lane >> 3)) * 1280 + (((lane & 7) ^ (4 + g4)) * 16);
  const unsigned w0 = g4 * 3840 + (((lane & 15) ^ (g4 << 2)) * 16), w1 = w0 + 4 * 3840;
  if (w >= 4) sn = sn == 9 ? 0 : sn + 1;
  unsigned fx = tr * (128 * 1280) + sn * 128 + (w & 3) * (32 * 1280), fw = ct * 256 + (sn * 64 + (w & 3) * 16) * 3840, fd = lds0 + (w >> 2) * 32768 + (w & 3) * 4096;
  unsigned fv[8] = {x0 + fx, w0 + fw, x1 + fx, w1 + fw, x0 + fx + 16 * 1280, w0 + fw + 8 * 3840, x1 + fx + 16 * 1280, w1 + fw + 8 * 3840};
  sn = sn == 9 ? 0 : sn + 1;
  asm volatile("" : "+v"(fv[0]), "+v"(fv[1]), "+v"(fv[2]), "+v"(fv[3]), "+v"(fv[4]), "+v"(fv[5]), "+v"(fv[6]), "+v"(fv[7]));
  asm volatile("" : "+s"(fd), "+s"(sn));
  FENCE();
  if (QDELAY && ct < 5) __builtin_amdgcn_s_sleep(QDELAY);
  if (!spare) {
#pragma unroll
    for (int e = 0; e < 8; ++e) glds(fv[e], e & 1 ? (const void*)p.wqkv : (const void*)p.x, fd + (e & 1) * 16384 + (e >> 1) * 1024);
  }
  FST(28);
  char* const ws = p.ws;
  const auto rs = __builtin_amdgcn_make_buffer_rsrc(ws, 0, WS_END, 0x00020000);
  if (lane == 0) { GAS u64* sp_ = (GAS u64*)(p.ws + WS_STAMP) + ((blockIdx.x * 8 + w) * 48) * 2; sp_[0] = t_in; sp_[1] = c_in; }
  const char* const xt = (const char*)p.x + tr * (128 * 1280);
  const char* const wt = (const char*)p.wqkv + ct * 256;
  auto pieces = [&](unsigned e, unsigned slot) {
    const char* xs = xt + (sn * 128 + e * (16 * 1280)); const char* wsb = wt + (sn * 64 + e * 8) * 3840; const unsigned d = lds0 + slot * 32768 + e * 2048;
    glds(x0, xs, d); glds(w0, wsb, d + 16384); glds(x1, xs, d + 1024); glds(w1, wsb, d + 16384 + 1024);
  };

  const int rrow = 8 * rb8 + 2 * w + h;
  u32x4 rx[3], rw[3]; unsigned pfv = 0, genv; float a1 = 0.f, a2 = 0.f, ar = 0.f, sw = 0.f;
  auto router_load = [&]() {
    if (CODE_KB && lane < CODE_KB / 2) asm volatile("global_load_dword %0, %1, %2" : "+v"(pfv) : "v"(((i & 3) * 4 + w) * (CODE_KB * 64) + lane * 128), "s"(pc0) : "memory");
    if (TLBW) {
      const unsigned n = (i * 4 + w) * 3 + lane;
      const char* a = n < 208 ? ws + n * 65536 : n < 248 ? (const char*)p.out + (n - 208) * 65536 : n < 261 ? (const char*)p.wo + min((n - 248) * 65536, 819072u)
                    : n < 291 ? (const char*)p.wg + (n - 261) * 65536 : n < 321 ? (const char*)p.wu + (n - 291) * 65536 : (const char*)p.wd + (n - 321 < 30 ? n - 321 : 0) * 65536;
      if (lane < 3) asm volatile("global_load_dword %0, %1, off" : "+v"(pfv) : "v"(a) : "memory");
    }
    const char* xr = (const char*)p.x + rrow * 1280 + l31 * 16; const char* wr = (const char*)p.wr + l31 * 16;
    const int t3 = l31 < 16 ? 1024 : 0;
    rx[0] = *(const GAS u32x4*)xr; rx[1] = *(const GAS u32x4*)(xr + 512); rw[0] = *(const GAS u32x4*)wr; rw[1] = *(const GAS u32x4*)(wr + 512);
    rx[2] = *(const GAS u32x4*)(xr + t3); rw[2] = *(const GAS u32x4*)(wr + t3);
    genv = __hip_atomic_load((GAS unsigned*)(ws + WS_GEN), __ATOMIC_RELAXED, __HIP_MEMORY_SCOPE_AGENT);
    FENCE();
  };
  auto router_dots = [&]() {
    if (l31 >= 16) rx[2] = rw[2] = (u32x4){};
#pragma unroll
    for (int e = 0; e < 3; ++e)
#pragma unroll
      for (int c = 0; c < 4; ++c) { a1 = dot2(rx[e][c], ONE2, a1); a2 = dot2(rx[e][c], rx[e][c], a2); ar = dot2(rx[e][c], rw[e][c], ar); sw = dot2(rw[e][c], ONE2, sw); }
    tag = __builtin_amdgcn_readfirstlane(genv) + 1u;
    if (tid == 0) *(LAS unsigned*)(lds0 + L_KEY) = tag;
    asm volatile("" : "+v"(a1), "+v"(a2), "+v"(ar), "+v"(sw) : "v"(pfv));
  };
  float rmb = 0.f, rdb = 0.f; unsigned rk = 0, rv = 0;
  auto router_step = [&](int k) {
    auto fdpp = [&](float& v, int st) { const unsigned u = __float_as_uint(v); v += __uint_as_float(st == 0 ? dpp<0x128>(u) : st == 1 ? dpp<0x124>(u) : st == 2 ? dpp<0x122>(u) : dpp<0x121>(u)); };
    auto fswp = [&](float& v) { auto r = __builtin_amdgcn_permlane16_swap(__float_as_uint(v), __float_as_uint(v), false, false); v = __uint_as_float(r[0]) + __uint_as_float(r[1]); };
    if (k < 4) { fdpp(a1, k); fdpp(a2, k); fdpp(ar, k); fdpp(sw, k); }
    if (k == 4) { fswp(a1); fswp(a2); }
    if (k == 5) { fswp(ar); fswp(sw); }
    if (k == 6) { a1 *= 1.f / 640.f; a2 = fmaxf(a2 * (1.f / 640.f) - a1 * a1, 0.f); }
    if (k == 7) { rmb = rb(a1); a2 = rb(a2) + 1e-5f; }
    if (k == 8) { a2 = __builtin_amdgcn_sqrtf(rb(a2)); ar -= rmb * sw; }
    if (k == 9) { rdb = rb(a2); a2 = __builtin_amdgcn_rcpf(rdb); }
    if (k == 10) { rk = pk(ar * a2, 0.f) & 0xffffu; rv = (__float_as_uint(rmb) >> 16) | __float_as_uint(rdb); }
    if (k == 11) rk = (rk & 0x8000u ? (~rk & 0xffffu) : (rk | 0x8000u)) << 16;
    if (k == 12) { if (l31 == 0) *(LAS unsigned*)(lds0 + L_KEYS + (2 * w + h) * 4) = rk | (2047u - rrow); }
    if (k == 13) { if (l31 == 0) __builtin_amdgcn_raw_buffer_store_b64((u32x2){rv, tag}, rs, WS_STATS + rrow * 8, 0, 16); }
    auto max8 = [&](unsigned& v, int st) { v = max(v, st == 0 ? dpp<0xB1>(v) : st == 1 ? dpp<0x4E>(v) : dpp<0x141>(v)); };
    if (k == 14) rk = rv = *(const LAS unsigned*)(lds0 + L_KEYS + (lane & 7) * 4);
    if (k >= 16 && k < 19) max8(rv, k - 16);
    if (k == 19) rk = rk == rv ? 0u : rk;
    if (k >= 20 && k < 23) max8(rk, k - 20);
    if (k == 23) { if (w == 0 && lane < 2) __builtin_amdgcn_raw_buffer_store_b64((u32x2){lane ? rk : rv, tag}, rs, WS_CAND + (2 * rb8 + lane) * 8, 0, 16); }
    asm volatile("" : "+v"(a1), "+v"(a2), "+v"(ar), "+v"(sw), "+v"(rmb), "+v"(rdb), "+v"(rk), "+v"(rv));
  };

  bf16x8 wdf[30];
  if (!spare) {
    if (w >= 4) {
      const int j = w - 4;
      auto issue = [&](int slot) { pieces(2 * j, slot); pieces(2 * j + 1, slot); sn = sn == 9 ? 0 : sn + 1; };
      if (tid == 384) *(LAS u32x4*)(lds0 + L_CNT) = (u32x4){};
      const unsigned au = tr * 15 + ct, cg = (au * 205) >> 11, kg = au - cg * 10;
      FST(31); BAR();
      FST(1); issue(2); FST(2);
      WAITV(8); FST(3); BAR();
      issue(3); pieces(2 * j, 0);
      WAITV(12); BAR();
      pieces(2 * j + 1, 0); sn = sn == 9 ? 0 : sn + 1; issue(1);
      WAITV(16); BAR();
#pragma unroll 1
      for (int s = 6; s < 9; ++s) { FST(3 * s - 8); issue(s & 3); FST(3 * s - 7); WAITV(16); FST(3 * s - 6); BAR(); }
      lds_tag();
      auto ffn_dma = [&]() {
        const unsigned fo = (lane >> 3) * 3072 + (lane & 7) * 16;
        const char* fb = (const char*)(j < 2 ? p.wg : p.wu) + (kg * 64 + 32 * (j & 1)) * 3072 + cg * 128;
#pragma unroll
        for (int e = 0; e < 4; ++e) glds(fo, fb + e * 8 * 3072, lds0 + L_FFNA + (4 * j + e) * 1024);
      };
      auto ffn_landed = [&]() { WAITV(0); if (lane == 0) __hip_atomic_fetch_add((LAS unsigned*)(lds0 + L_CNT + 12), 1u, __ATOMIC_RELAXED, __HIP_MEMORY_SCOPE_WORKGROUP); };
      if (w == 6) {
        const char* ga = ws + WS_STATS + tr * 1024;
        u32x2 g0, g1;
        asm volatile("global_load_dwordx2 %0, %2, %3 sc1\n\tglobal_load_dwordx2 %1, %2, %3 offset:512 sc1" : "=&v"(g0), "=&v"(g1) : "v"(lane * 8), "s"(ga) : "memory");
        issue(1); WAITV(16); BAR();
        ffn_dma();
        asm volatile("s_waitcnt vmcnt(12)\n\ts_barrier" : "+v"(g0), "+v"(g1) :: "memory");
        if (!__all(g0[1] == tag && g1[1] == tag)) {
#pragma unroll 1
          for (unsigned sp = 0; sp < SPIN_CAP; ++sp) {
            g0 = __builtin_bit_cast(u32x2, ldg64(ga + lane * 8)); g1 = __builtin_bit_cast(u32x2, ldg64(ga + 512 + lane * 8));
            if (__all(g0[1] == tag && g1[1] == tag)) break; }
        }
        *(LAS f32x2*)(lds0 + L_STAT + lane * 8) = (f32x2){blo(g0[0]), __builtin_amdgcn_rcpf(bhi(g0[0]))};
        *(LAS f32x2*)(lds0 + L_STAT + 512 + lane * 8) = (f32x2){blo(g1[0]), __builtin_amdgcn_rcpf(bhi(g1[0]))};
        FST(23); WAITV(4); BAR();
        ffn_landed();
      } else if (w < 6) { issue(1); WAITV(16); BAR(); ffn_dma(); WAITV(12); BAR(); WAITV(4); BAR(); ffn_landed(); }
      else {
        Keys kk;
        asm volatile("buffer_load_dwordx4 %0, %4, %5, %6 offen sc1\n\tbuffer_load_dwordx4 %1, %4, %5, %6 offen offset:1024 sc1\n\t"
                     "buffer_load_dwordx4 %2, %4, %5, %6 offen offset:2048 sc1\n\tbuffer_load_dwordx4 %3, %4, %5, %6 offen offset:3072 sc1"
                     : "=&v"(kk.g[0]), "=&v"(kk.g[1]), "=&v"(kk.g[2]), "=&v"(kk.g[3]) : "v"(lane * 16), "s"(rs), "s"(WS_CAND) : "memory");
        issue(1); WAITV(16); BAR();
        ffn_dma();
        asm volatile("s_waitcnt vmcnt(12)\n\ts_barrier" : "+v"(kk.g[0]), "+v"(kk.g[1]), "+v"(kk.g[2]), "+v"(kk.g[3]) :: "memory");
        unsigned k1 = 0, k2 = 0;
        const bool early = keys_top2(kk, tag, k1, k2);
        u32x4 xc0 = {ONE2, ONE2, ONE2, ONE2}, xc1 = xc0;
        { const char* xq = (const char*)p.x + ((2047u - (lane & 1 ? k2 : k1)) & 2047u) * 1280 + kg * 128 + g4 * 16;
          if ((lane & 15) < 2) asm volatile("global_load_dwordx4 %0, %2, off\n\tglobal_load_dwordx4 %1, %2, off offset:64" : "+v"(xc0), "+v"(xc1) : "v"(xq) : "memory"); }
        asm volatile("s_waitcnt vmcnt(6)\n\ts_barrier\n\ts_waitcnt vmcnt(0)" : "+v"(xc0), "+v"(xc1) :: "memory");
        FST(23);
        if (!early) {
          keys_wait(rs, lane, tag, k1, k2);
          if ((lane & 15) < 2) { const char* xq = (const char*)p.x + ((2047u - (lane & 1 ? k2 : k1)) & 2047u) * 1280 + kg * 128 + g4 * 16; xc0 = *(const GAS u32x4*)xq; xc1 = *(const GAS u32x4*)(xq + 64); }
        }
        if (b == 16 && lane == 0) __hip_atomic_store((GAS unsigned*)(ws + WS_GEN), tag, __ATOMIC_RELAXED, __HIP_MEMORY_SCOPE_AGENT);
        if (lane == 0) *(LAS u32x2*)(lds0 + L_TOP) = (u32x2){k1, k2};
#pragma unroll 1
        for (unsigned sp = 0; sp < SPIN_CAP; ++sp) { if (__builtin_amdgcn_readfirstlane(*(volatile LAS unsigned*)(lds0 + L_CNT + 12)) >= 3u) break; __builtin_amdgcn_s_sleep(1); }
        asm volatile("" ::: "memory");
#pragma unroll
        for (int n = 0; n < 8; ++n) {
          const unsigned a = lds0 + L_FFNA + (n >> 2) * 8192 + (8 * g4 + q4) * 128 + (n & 3) * 32 + 8 * p4;
          const f32x4 y = MFMA16(trfrag(a + 4096, a + 4608), __builtin_bit_cast(bf16x8, xc1), MFMA16(trfrag(a, a + 512), __builtin_bit_cast(bf16x8, xc0), (f32x4){}));
          if ((lane & 15) < 3) *(LAS f32x4*)(lds0 + L_TMP + (((n >> 2) * 3 + (lane & 15)) * 64 + 16 * (n & 3) + 4 * g4) * 4) = y;
        }
#pragma unroll
        for (int n = 0; n < 6; ++n) stg64(ws + WS_PARTA + (au * 384 + n * 64 + lane) * 8, gran(*(const LAS unsigned*)(lds0 + L_TMP + (n * 64 + lane) * 4), tag));
        FST(25);
      }
    } else {
      router_load();
      const int fh = w >> 1, th = w & 1;
      unsigned xa[4], wa[2], ko[4];
#pragma unroll
      for (int e = 0; e < 4; ++e) { const int ks = (2 * th + e) & 3, row = 64 * th + l31; xa[e] = lds0 + row * 128 + (((2 * ks + h) ^ ((row >> 1) & 7)) * 16); ko[e] = ks * 4096; }
#pragma unroll
      for (int f = 0; f < 2; ++f) wa[f] = lds0 + 16384 + (8 * h + q4) * 256 + (((4 * (2 * fh + f) + 2 * (g4 & 1) + (p4 >> 1)) ^ (q4 << 2)) * 16) + 8 * (p4 & 1);
      f32x16 acc[2][2] = {};
      f32x4 cs0 = {}, cs1 = {};
      const unsigned selw = (lane & 15) == (g4 & 1) ? ONE2 : 0u;
      const bf16x8 sel = __builtin_bit_cast(bf16x8, (u32x4){selw, selw, selw, selw});
      struct Fr { bf16x8 v[4]; };
      Fr FA[4], FB[4];
      auto rd = [&](Fr* F, int n, unsigned so) {
        const int e = n >> 2, j = n & 3;
        if (j < 2) { const unsigned a = wa[j] + (so + ko[e]); F[e].v[j] = trfrag(a, a + 1024); } else F[e].v[j] = lds128(xa[e] + so + (j - 2) * 4096);
      };
      auto colsum = [&](const Fr* C, int e) { cs0 = MFMA16(C[e].v[0], sel, cs0); cs1 = MFMA16(C[e].v[1], sel, cs1); };
      bf16x8 ax[2], bx[2]; float rr[2], c0, c1; f32x2 st0, st1; float cp[4];
      const float qs = ct < 5 ? 0.22360679774997896f * 1.4426950408889634f : 1.f;
      const unsigned ca = lds0 + L_CS + fh * 512 + l31 * 4, cown = lds0 + L_CS + w * 256 + ((lane & 15) * 16 + 4 * g4) * 4;
      auto prep = [&](int n) {
        if (n == 12) { cp[0] = *(const LAS float*)ca; cp[1] = *(const LAS float*)(ca + 256); cp[2] = *(const LAS float*)(ca + 128); cp[3] = *(const LAS float*)(ca + 384); }
        if (n == 13) { st0 = *(const LAS f32x2*)(lds0 + L_STAT + (64 * th + l31) * 8); st1 = *(const LAS f32x2*)(lds0 + L_STAT + (64 * th + 32 + l31) * 8); }
      };
      auto body = [&](Fr* N, const Fr* C, unsigned so, int r0 = 99) {
#pragma unroll
        for (int n = 0; n < 16; ++n) {
          const int e = n >> 2, f = (n >> 1) & 1, t = n & 1;
          acc[f][t] = MFMA32(C[e].v[f], C[e].v[2 + t], acc[f][t]);
          if (n < 4) { rd(N, 2 * n, so); rd(N, 2 * n + 1, so); } else if (n < 12) rd(N, n + 4, so);
          if (n >= 2 && r0 + n - 2 < 24) router_step(r0 + n - 2);
          if (r0 == 100) prep(n);
          FENCE();
        }
        if (r0 == 100) { cs0 = cs1 = (f32x4){}; colsum(N, 2); colsum(N, 3); }
        colsum(N, 0); colsum(N, 1);
        FENCE();
      };
      FST(29);
      if (TLBW) WAITV(10); else WAITV(9);
      FST(30); BAR(); ST(1); FST(31);
#pragma unroll
      for (int n = 0; n < 16; ++n) rd(FA, n, 0);
      FST(32); router_dots(); FST(33);
      colsum(FA, 0); colsum(FA, 1);
      BAR();
#pragma unroll
      for (int s = 1; s <= 7; s += 2) {
        FST(2 * s); body(FB, FA, (s & 3) * 32768, s == 1 ? 0 : 99); FST(2 * s + 1); BAR();
        FST(2 * s + 2); body(FA, FB, ((s + 1) & 3) * 32768, s == 1 ? 14 : 99);
        if (s == 7 && (lane & 15) < 2) { *(LAS f32x4*)cown = cs0; *(LAS f32x4*)(cown + 128) = cs1; }
        FST(2 * s + 3); BAR();
      }
      FST(18);
      body(FB, FA, 32768, 100);
      FST(19);
      const unsigned qo = WS_QKV + (4 * tr + 2 * th) * 122880 + (16 * ct + 8 * fh + h) * 512 + l31 * 16;
      unsigned pw[8];
      auto split3 = [&](float c) {
        const float hi = rb(c), r1 = c - hi, mid = rb(r1);
        return __builtin_bit_cast(bf16x8, (u32x4){h ? 0u : pk(hi, mid), h ? 0u : pk(r1 - mid, 0.f), 0u, 0u});
      };
      auto epi = [&](int tile, int part) {
        const int f = tile & 1, t = tile >> 1;
#pragma unroll
        for (int g = 2 * part; g < 2 * part + 2; ++g) {
          float v0 = acc[f][t][2 * g] * rr[t]; asm("" : "+v"(v0));
          pw[g] = pk(v0, acc[f][t][2 * g + 1] * rr[t]);
        }
        if (part & 1) {
          const int g = part >> 1;
          auto r0 = __builtin_amdgcn_permlane32_swap(pw[4 * g], pw[4 * g + 2], false, false);
          auto r1 = __builtin_amdgcn_permlane32_swap(pw[4 * g + 1], pw[4 * g + 3], false, false);
          __builtin_amdgcn_raw_buffer_store_b128((u32x4){r0[0], r1[0], r0[1], r1[1]}, rs, qo + t * 122880 + (4 * f + 2 * g) * 512, 0, 16);
        }
      };
#pragma unroll
      for (int n = 0; n < 20; ++n) {
        const int tile = n < 8 ? n >> 2 : n < 10 ? n - 8 : n < 15 ? 2 : 3, e = n < 8 ? n & 3 : n < 10 ? 4 : (n - 10) % 5, f = tile & 1, t = tile >> 1;
        acc[f][t] = e < 4 ? MFMA32(FB[e].v[f], FB[e].v[2 + t], acc[f][t]) : MFMA32(ax[f], bx[t], acc[f][t]);
        if (n == 0) { const unsigned m = pk(-st0[0], -st0[0]); bx[0] = __builtin_bit_cast(bf16x8, (u32x4){m, m, 0u, 0u}); rr[0] = st0[1] * qs; c0 = cp[0] + cp[1]; asm volatile("" : "+v"(bx[0]), "+v"(rr[0]), "+v"(c0)); }
        if (n == 1) { const unsigned m = pk(-st1[0], -st1[0]); bx[1] = __builtin_bit_cast(bf16x8, (u32x4){m, m, 0u, 0u}); rr[1] = st1[1] * qs; c1 = cp[2] + cp[3]; asm volatile("" : "+v"(bx[1]), "+v"(rr[1]), "+v"(c1)); }
        if (n == 1 && (lane & 15) < 2) { *(LAS f32x4*)(cown + 1024) = cs0; *(LAS f32x4*)(cown + 1024 + 128) = cs1; }
        if (n == 2) { cp[0] = *(const LAS float*)(lds0 + L_CS + 1024 + w * 256 + l31 * 4); cp[1] = *(const LAS float*)(lds0 + L_CS + 1024 + w * 256 + 128 + l31 * 4); }
        if (n == 5) { ax[0] = split3(c0 + cp[0]); asm volatile("" : "+v"(ax[0])); }
        if (n == 6) { ax[1] = split3(c1 + cp[1]); asm volatile("" : "+v"(ax[1])); }
        if (n >= 10) epi((n - 10) >> 2, (n - 10) & 3);
        FENCE();
        if (n == 9 || n == 13 || n == 17) FST(24 + (n - 9) / 4);
      }
      ST(2); FST(20);
      epi(2, 2); epi(2, 3);
#pragma unroll
      for (int part = 0; part < 4; ++part) epi(3, part);
      FST(21);
      WAITV(0);
      if (lane == 0 && __hip_atomic_fetch_add((LAS unsigned*)(lds0 + L_CNT), 1u, __ATOMIC_RELAXED, __HIP_MEMORY_SCOPE_WORKGROUP) == 3u)
        stg64(ws + WS_FLAG1 + (tr * 16 + ct) * 256, gran(tag, ~tag));
      ST(3); FST(22);
    }
  } else {
    if (w < 4) router_load();
#pragma unroll 1
    for (int e = 0; e < 15; ++e) {
      const unsigned L = 512 * e + tid, r = (L * 52429) >> 22, pc = L - r * 80;
      glds(r * 1280 + (pc ^ (((r & 3) | ((r >> 1) & 4)) << 1)) * 16, (const char*)p.wd + (2 * xc + i) * 96 * 1280, lds0 + (8 * e + w) * 1024);
    }
    if (w < 4) {
      router_dots();
#pragma unroll
      for (int k = 0; k < 14; ++k) router_step(k);
    }
    if (tid == 384) *(LAS u32x4*)(lds0 + L_CNT) = (u32x4){};
    BAR();
    if (w == 0) {
#pragma unroll
      for (int k = 14; k < 24; ++k) router_step(k);
    }
    WAITV(0); BAR(); ST(1);
    if (w >= 4) {
      lds_tag();
#pragma unroll
      for (int n = 0; n < 30; ++n) {
        const unsigned a = lds0 + (8 * g4 + q4) * 1280 + (((2 * (w - 4) + n / 15) ^ (q4 | ((g4 & 1) << 2))) * 32) + 8 * p4 + (n % 15 / 5) * 40960 + (n % 5) * 256;
        wdf[n] = trfrag(a, a + 5120);
      }
    } else if (w == 0) {
      unsigned k1 = 0, k2 = 0;
      { const u64 t0 = __builtin_amdgcn_s_memrealtime() ; while (__builtin_amdgcn_s_memrealtime() - t0 < KEYDELAY) __builtin_amdgcn_s_sleep(8); }
      keys_wait(rs, lane, tag, k1, k2);
      const char* sa = ws + WS_STATS + ((2047u - (lane & 1 ? k2 : k1)) & 2047u) * 8;
      u64 sg = ldg64(sa);
#pragma unroll 1
      for (unsigned sp = 0; sp < SPIN_CAP && !__all((unsigned)(sg >> 32) == tag); ++sp) sg = ldg64(sa);
      if (lane < 2) { *(LAS unsigned*)(lds0 + L_TOP + lane * 4) = lane ? k2 : k1; *(LAS unsigned*)(lds0 + L_TOP + 8 + lane * 4) = (unsigned)sg; }
    }
    BAR(); ST(2);
  }
  ST(4); FST(35);
  const u64 ftag = gran(tag, ~tag);

  const int tg3 = w & 3, kh = w >> 2, tok0 = 256 * xc + 64 * (i >> 3), n0 = 80 * cs80;
  const bool two = w == 4 || w == 5;
  const unsigned it0 = tid, it1 = two ? tid + 256 : tid, tk0 = (it0 * 6554) >> 16, tk1 = (it1 * 6554) >> 16, ch0 = it0 - 10 * tk0, ch1 = it1 - 10 * tk1;
  const unsigned eo0 = (tok0 + tk0) * 1280 + n0 * 2 + ch0 * 16, eo1 = (tok0 + tk1) * 1280 + n0 * 2 + ch1 * 16;
  u32x4 xv0, xv1; u64 sg0_, sg1_;
  auto pre3 = [&]() {
    xv0 = *(const GAS u32x4*)((const char*)p.x + eo0); sg0_ = ldg64(ws + WS_STATS + (tok0 + tk0) * 8);
    xv1 = *(const GAS u32x4*)((const char*)p.x + eo1); sg1_ = ldg64(ws + WS_STATS + (tok0 + tk1) * 8);
  };

  {
    const int R = i >> 2, hd = 4 * xc + (i & 3), p0 = (5 * hd) >> 1, oh = hd & 1;
    const int pf = p0 + oh, pp = oh ? p0 : p0 + 2, cc = (lane & 1 ? p0 + 2 : p0) >> 4;
    if (w < 4) {
      const int lo = w < 2, tu = 2 * w + 1 - lo, tv = 2 * w + lo;
      poll(ws + WS_FLAG1 + ((2 * R + (w >> 1)) * 16 + cc) * 256, lane < 2, ftag);
      ST(5); FST(36);
      bf16x8 qf[2][2];
      { const unsigned a0 = (pf + h) * 512 + l31 * 16, a1 = pp * 512 + l31 * 16 + oh * 8, ou = WS_QKV + (8 * R + tu) * 122880, ov = WS_QKV + (8 * R + tv) * 122880;
        const u32x4 u0 = __builtin_amdgcn_raw_buffer_load_b128(rs, a0, ou, 16), v0 = __builtin_amdgcn_raw_buffer_load_b128(rs, a0, ov, 16);
        const u32x2 u1 = __builtin_amdgcn_raw_buffer_load_b64(rs, a1, ou, 16), v1 = __builtin_amdgcn_raw_buffer_load_b64(rs, a1, ov, 16);
        qf[0][0] = __builtin_bit_cast(bf16x8, u0); qf[0][1] = __builtin_bit_cast(bf16x8, (u32x4){h ? 0u : u1[0], h ? 0u : u1[1], 0u, 0u});
        qf[1][0] = __builtin_bit_cast(bf16x8, v0); qf[1][1] = __builtin_bit_cast(bf16x8, (u32x4){h ? 0u : v1[0], h ? 0u : v1[1], 0u, 0u}); }
      pre3();
      f32x16 cz = {}, ca, cb;
#pragma unroll
      for (int r = 0; r < 16; ++r) {
        const bool le = (r & 3) + 8 * (r >> 2) + 4 * h <= l31;
        const float d = le ? 0.f : -1e30f, f = le || R == 0 ? -1e30f : 0.f;
        ca[r] = lo ? d : f; cb[r] = lo ? f : d;
      }
      const unsigned ka = lds0 + L_KV + h * 512 + l31 * 16;
      const int pi = min(4 * (g4 & 1) + p4, 5);
      const unsigned va = lds0 + L_KV + L_V + (pi >> 1) * VPS + (pi & 1) * 8 + (4 * h + q4) * 16;
      struct KF { bf16x8 a, b; };
      auto kread = [&](int kt) {
        const unsigned a = ka + kt * KVS;
        return KF{lds128(a), lds128(a + 1024)};
      };
      auto vread = [&](int kt) { const unsigned v = va + kt * KVS; return KF{trfrag(v, v + 128), trfrag(v + 256, v + 384)}; };
      auto ex = [&](const f32x16& s, int e) { return pk(__builtin_amdgcn_exp2f(s[2 * e]), __builtin_amdgcn_exp2f(s[2 * e + 1])); };
      const int P0 = lo ? 6 : 8, P1 = w == 0 ? 4 : w == 1 ? 8 : w == 2 ? 6 : 10, P2 = lo ? 2 + 2 * w : 6 + 2 * w;
      const int tp[10] = {P0, P0 + 1, P1, P1 + 1, lo ? 2 * w + 8 : 2 * w + 1, lo ? 2 * w + 9 : 2 * w, P2, P2 + 1, lo ? 2 * w + 1 : 2 * w + 8, lo ? 2 * w : 2 * w + 9};
      auto at = [&](int ps) { const int e = (ps == 1 || ps == 3 || ps == 7), t = tp[ps - e]; return (R == 0 && t < 8 ? 8 : t) + e; };
      auto setz = [&](int ps) {
        if (ps == 1 || ps == 3 || ps == 5 || ps == 7 || ps == 9) return;
        if (R == 0) { const float z = tp[ps] < 8 ? -1e30f : 0.f;
#pragma unroll
          for (int r = 0; r < 16; ++r) cz[r] = z;
          asm volatile("" : "+v"(cz)); } };
      setz(0);
      asm volatile("" : "+v"(ca), "+v"(cb), "+v"(cz), "+v"(qf[0][0]), "+v"(qf[0][1]), "+v"(qf[1][0]), "+v"(qf[1][1]));
      BAR(); ST(6); FST(39);
      KF K0, K1, K2, V0, V1, V2; f32x16 S0, S1, o[2] = {};
      K0 = kread(at(0)); V0 = vread(at(0));
      S0 = MFMA32(K0.b, qf[0][1], MFMA32(K0.a, qf[0][0], cz));
      u32x4 pa = {}, pb = {}; float x0, x1, x2, x3;
#define SET3(N, p) ((p) % 3 == 0 ? N##0 : (p) % 3 == 1 ? N##1 : N##2)
#define EXP4(s, e) do { x0 = __builtin_amdgcn_exp2f(s[4 * e]); x1 = __builtin_amdgcn_exp2f(s[4 * e + 1]); x2 = __builtin_amdgcn_exp2f(s[4 * e + 2]); x3 = __builtin_amdgcn_exp2f(s[4 * e + 3]); \
                        asm volatile("" : "+v"(x0), "+v"(x1), "+v"(x2), "+v"(x3)); FENCE(); } while (0)
#define PACK2(d, e) do { d[e] = pk(x0, x1); d[e + 1] = pk(x2, x3); asm volatile("" : "+v"(d)); FENCE(); } while (0)
      auto unit = [&](auto U, f32x16& sc, f32x16& sn) {
        constexpr int u = decltype(U)::value, np = u + 1 < 18 ? UP[u + 1] : 0, nq = u + 1 < 18 ? UQ[u + 1] : 0, pu = u > 0 ? u - 1 : 0;
        if constexpr (u == 2 || u == BC_AT) BAR();
        FST(40 + u);
        if constexpr (u + 1 < 18) { if constexpr (np != UP[u]) setz(np); sn = MFMA32(SET3(K, np).a, qf[nq][0], u + 1 == 8 || u + 1 == 10 ? ca : u + 1 >= 16 ? cb : cz); FENCE(); }
        if constexpr (u > 0) PACK2(pb, 2);
        EXP4(sc, 0);
        if constexpr (u > 0) { o[UQ[pu]] = MFMA32(SET3(V, UP[pu]).b, __builtin_bit_cast(bf16x8, pb), o[UQ[pu]]); FENCE(); }
        if constexpr (u + 2 < 18 && UP[u + 2] != UP[u + 1]) SET3(K, UP[u + 2]) = kread(at(UP[u + 2]));
        if constexpr (u + 1 < 18 && np != UP[u]) SET3(V, np) = vread(at(np));
        PACK2(pa, 0);
        EXP4(sc, 1);
        if constexpr (u + 1 < 18) { sn = MFMA32(SET3(K, np).b, qf[nq][1], sn); FENCE(); }
        PACK2(pa, 2);
        EXP4(sc, 2);
        o[UQ[u]] = MFMA32(SET3(V, UP[u]).a, __builtin_bit_cast(bf16x8, pa), o[UQ[u]]); FENCE();
        PACK2(pb, 0);
        EXP4(sc, 3);
      };
#define UNIT2(n) unit(IC<n>{}, S0, S1); unit(IC<n + 1>{}, S1, S0);
      UNIT2(0) UNIT2(2) UNIT2(4) UNIT2(6) UNIT2(8) UNIT2(10) UNIT2(12) UNIT2(14) UNIT2(16)
      PACK2(pb, 2);
      o[0] = MFMA32(V0.b, __builtin_bit_cast(bf16x8, pb), o[0]);
      auto rowsum = [&](const f32x16& a) { auto r = __builtin_amdgcn_permlane32_swap(__float_as_uint(a[8]), __float_as_uint(a[8]), false, false); return __uint_as_float(r[1]); };
      float l[2] = {rowsum(o[1]), rowsum(o[0])};
      if (FORCE_FB || !__all(l[0] > 0x1p-60f && l[0] < 0x1p60f && l[1] > 0x1p-60f && l[1] < 0x1p60f)) {
        int qt = tu;
#pragma unroll 1
        for (int e = 0; e < 2; ++e) {
          const int k0 = R > 0 ? qt : 8;
          auto score = [&](int kt, float base) {
            f32x16 c;
#pragma unroll
            for (int r = 0; r < 16; ++r) { const bool le = (r & 3) + 8 * (r >> 2) + 4 * h <= l31; c[r] = (kt == qt + 8 && !le) || (kt == qt && le) ? -1e30f : base; }
            const KF k = kread(kt);
            return MFMA32(k.b, qf[0][1], MFMA32(k.a, qf[0][0], c));
          };
          float m = -3e38f;
#pragma unroll 1
          for (int kt = k0; kt <= qt + 8; ++kt) { const f32x16 s = score(kt, 0.f);
#pragma unroll
            for (int r = 0; r < 16; ++r) m = fmaxf(m, s[r]); }
          { auto r = __builtin_amdgcn_permlane32_swap(__float_as_uint(m), __float_as_uint(m), false, false); m = fmaxf(__uint_as_float(r[0]), __uint_as_float(r[1])); }
          o[0] = (f32x16){};
#pragma unroll 1
          for (int kt = k0; kt <= qt + 8; ++kt) {
            const f32x16 s = score(kt, -m); const KF v = vread(kt);
            o[0] = MFMA32(v.a, __builtin_bit_cast(bf16x8, (u32x4){ex(s, 0), ex(s, 1), ex(s, 2), ex(s, 3)}), o[0]);
            o[0] = MFMA32(v.b, __builtin_bit_cast(bf16x8, (u32x4){ex(s, 4), ex(s, 5), ex(s, 6), ex(s, 7)}), o[0]);
          }
          const f32x16 to = o[0]; o[0] = o[1]; o[1] = to;
          const bf16x8 t0 = qf[0][0], t1 = qf[0][1]; qf[0][0] = qf[1][0]; qf[0][1] = qf[1][1]; qf[1][0] = t0; qf[1][1] = t1;
          qt = tv;
        }
        l[0] = rowsum(o[1]); l[1] = rowsum(o[0]);
      }
      ST(7); FST(59);
#pragma unroll
      for (int e = 0; e < 2; ++e) {
        const f32x16& a = o[1 - e]; const float ri = __builtin_amdgcn_rcpf(l[e]);
        const unsigned e0 = pk(a[0] * ri, a[1] * ri), e1 = pk(a[2] * ri, a[3] * ri), e2 = pk(a[4] * ri, a[5] * ri), e3 = pk(a[6] * ri, a[7] * ri);
        const u32x2 el = {pk(a[8] * ri, a[9] * ri), pk(a[10] * ri, a[11] * ri)};
        auto r0 = __builtin_amdgcn_permlane32_swap(e0, e2, false, false), r1 = __builtin_amdgcn_permlane32_swap(e1, e3, false, false);
        const unsigned ob = WS_O + (16 * R + 2 * (e ? tu : tv) + (l31 >> 4)) * 20480;
        __builtin_amdgcn_raw_buffer_store_b128((u32x4){r0[0], r1[0], r0[1], r1[1]}, rs, ob + (2 * hd + h) * 256 + (lane & 15) * 16, 0, 16);
        if (h == 0) __builtin_amdgcn_raw_buffer_store_b64(el, rs, ob + 16384 + hd * 128 + (lane & 15) * 8, 0, 16);
      }
      WAITV(0);
      if (lane == 0 && __hip_atomic_fetch_add((LAS unsigned*)(lds0 + L_CNT + 4), 1u, __ATOMIC_RELAXED, __HIP_MEMORY_SCOPE_WORKGROUP) == 3u)
        stg64(ws + WS_FLAG2 + (R * 32 + hd) * 256, ftag);
      ST(8); FST(60);
      BAR();
    } else {
      __builtin_amdgcn_s_setprio(2);
      const int j = w - 4;
      const WoOff woo = wo_offsets(lane);
    const int sp_ = 2 * xc + i;
    unsigned ga[3]; u32x2 pa_[30];
    auto gather = [&]() {
      asm volatile("" ::: "memory");
#pragma unroll
      for (int n = 0; n < 30; ++n) pa_[n] = __builtin_amdgcn_raw_buffer_load_b64(rs, ga[n / 10], WS_PARTA + (n % 10) * 3072, 16);
    };
#pragma unroll
      for (int r = 0; r < 3; ++r) { const unsigned e = min(144 * j + 64 * r + lane, 575), rc = (e * 683) >> 16, F = 96 * sp_ + e - 96 * rc; ga[r] = ((F >> 6) * 3840 + rc * 64 + (F & 63)) * 8; asm volatile("" : "+v"(ga[r])); }
      const int kt[4] = {6 + j, j < 2 ? 4 + j : 8 + j, j, 12 + j};
      { const int e = lane >> 2, T = 8 * R - 8 + (e == 0 ? kt[0] : e == 1 ? kt[1] : e == 2 ? kt[2] : kt[3]);
        const bool act = lane < 16 && T >= 0;
        poll(ws + WS_FLAG1 + (act ? ((T >> 2) * 16 + (lane & 2 ? 10 : 5) + cc) * 256 : 0), act, ftag); }
      ST(5); FST(36);
      const unsigned so0 = (80 + pf + h) * 512 + l31 * 16, so1 = (h ? (160 + pf) * 512 : (80 + pp) * 512 + 8 * oh) + l31 * 16, so2 = (h ? (160 + pp) * 512 + 8 * oh : (161 + pf) * 512) + l31 * 16;
      struct T3 { u32x4 a, b, c; };
      auto fetch = [&](int t) {
        const int T = 8 * R - 8 + t; const unsigned m = T < 0 ? 0x40000000u : 0u, o = WS_QKV + max(T, 0) * 122880;
        return T3{__builtin_amdgcn_raw_buffer_load_b128(rs, so0 | m, o, 16), __builtin_amdgcn_raw_buffer_load_b128(rs, so1 | m, o, 16), __builtin_amdgcn_raw_buffer_load_b128(rs, so2 | m, o, 16)};
      };
      auto stage = [&](int t, T3 v) {
        if (R == 0 && t < 8) return;
        const unsigned kd = lds0 + L_KV + t * KVS + l31 * 16;
        if (h) v.c[2] = v.c[3] = ONE2;
        *(LAS u32x4*)(kd + h * 512) = v.a; *(LAS u32x4*)(kd + 1024 + h * 512) = v.b; *(LAS u32x4*)(kd + L_V + (1 + h) * VPS) = v.c;
      };
      const T3 t0 = fetch(kt[0]), t1 = fetch(kt[1]);
      FENCE();
      stage(kt[0], t0);
      FENCE();
      const T3 t2 = fetch(kt[2]), t3 = fetch(kt[3]);
      FENCE();
      BAR(); ST(6); FST(39);
      auto pause = [&](int n) { for (int e = 0; e < n; ++e) __builtin_amdgcn_s_sleep(1); };
      pause(j); stage(kt[1], t1);
      BAR(); FST(40);
      pause(2 * j); stage(kt[2], t2); pause(1); stage(kt[3], t3);
      unsigned pf = 0;
      if (!spare) {
        const unsigned po = ((i >> 3) * 160 + j * 40 + (lane >> 1)) * 1280 + (lane & 1) * 128;
        const char* pb = (const char*)p.wo + ((cs80 * 160) & ~127);
        asm volatile("global_load_dword %0, %1, %2" : "+v"(pf) : "v"(po), "s"(pb) : "memory");
        if (lane < 16) asm volatile("global_load_dword %0, %1, %2" : "+v"(pf) : "v"(po + 32 * 1280), "s"(pb) : "memory");
        pre3();
        BAR(); FST(41);
        asm volatile("s_waitcnt vmcnt(0)" : "+v"(pf) :: "memory");
        FST(44);
        wo_steps(5 * j, 5 * j + 5, woo, p.wo, cs80, lds0);
      } else {
        pre3();
        gather();
        FENCE();
        BAR(); FST(41);
        wo_steps(5 * j, 5 * j + 5, woo, p.wo, cs80, lds0);
        FST(45);
#pragma unroll 1
        for (unsigned sp = 0; sp < SPIN_CAP; ++sp) {
          WAITV(0);
          unsigned bad[5];
#pragma unroll
          for (int n = 0; n < 5; ++n) bad[n] = (pa_[n][1] ^ tag) | (pa_[n + 5][1] ^ tag) | (pa_[n + 10][1] ^ tag) | (pa_[n + 15][1] ^ tag) | (pa_[n + 20][1] ^ tag) | (pa_[n + 25][1] ^ tag);
          if (__all((bad[0] | bad[1] | bad[2] | bad[3] | bad[4]) == 0)) break;
          gather();
        }
#pragma unroll
        for (int r = 0; r < 3; ++r) {
          auto v = [&](int kg) { return __uint_as_float(pa_[10 * r + kg][0]); };
          const float sm = (((v(0) + v(1)) + (v(2) + v(3))) + ((v(4) + v(5)) + (v(6) + v(7)))) + (v(8) + v(9));
          *(LAS float*)(lds0 + L_TMP + min(144 * j + 64 * r + lane, 575) * 4) = sm;
        }
        FST(46);
        __builtin_amdgcn_s_waitcnt(0xC07F);
        if (lane == 0) __hip_atomic_fetch_add((LAS unsigned*)(lds0 + L_CNT + 8), 1u, __ATOMIC_RELAXED, __HIP_MEMORY_SCOPE_WORKGROUP);
#pragma unroll 1
        for (unsigned sp = 0; sp < SPIN_CAP; ++sp) { if (__builtin_amdgcn_readfirstlane(*(volatile LAS unsigned*)(lds0 + L_CNT + 8)) >= 4u) break; __builtin_amdgcn_s_sleep(1); }
        asm volatile("" ::: "memory");
        FST(47);
      const unsigned hb = lds0 + L_TMP + 2304 + j * 384;
#pragma unroll
      for (int r = 0; r < 3; ++r) {
        const int e = 64 * r + lane, tk = e >= 96; const unsigned a = lds0 + L_TMP + (e - 96 * tk) * 4, sg = *(const LAS unsigned*)(lds0 + L_TOP + 8 + tk * 4);
        const float m = blo(sg), rd = __builtin_amdgcn_rcpf(bhi(sg));
        const float g = rb((*(const LAS float*)(a + tk * 384) - m * *(const LAS float*)(a + 768)) * rd);
        const float u = rb((*(const LAS float*)(a + 1152 + tk * 384) - m * *(const LAS float*)(a + 1920)) * rd);
        *(LAS u16*)(hb + e * 2) = (u16)pk(rb(g * __builtin_amdgcn_rcpf(1.f + __expf(-g))) * u, 0.f);
      }
      bf16x8 hf[3];
#pragma unroll
      for (int s = 0; s < 3; ++s) hf[s] = lds128(hb + (lane & 1) * 192 + g4 * 16 + s * 64);
      f32x4 y[10] = {};
#pragma unroll
      for (int s = 0; s < 3; ++s)
#pragma unroll
        for (int n = 0; n < 10; ++n) y[n] = MFMA16(hf[s], wdf[(n / 5) * 15 + 5 * s + n % 5], y[n]);
#pragma unroll
      for (int n = 0; n < 5; ++n) {
        const float v = g4 & 1 ? (g4 & 2 ? y[n + 5][1] : y[n + 5][0]) : (g4 & 2 ? y[n][1] : y[n][0]);
        __builtin_amdgcn_raw_buffer_store_b64((u32x2){__float_as_uint(v), tag}, rs, (h * 640 + 32 * j + l31) * 8 + n * 1024, WS_YPART + sp_ * 10240, 16);
      }
      ST(3);
      }
      FST(42);
      if (!spare) WAITV(0);
      FST(43);
      __builtin_amdgcn_s_setprio(0);
      BAR();
    }
  }

  {
    const int tg = tg3;
    const unsigned wa = lds0 + L_WO + (kh * 320 + 4 * g4 + q4) * 160 + 8 * p4;
    constexpr int NPRE = 6;
    bf16x8 wf[NPRE][5];
#pragma unroll
    for (int s = 0; s < NPRE; ++s)
#pragma unroll
      for (int n = 0; n < 5; ++n) wf[s][n] = trfrag(wa + s * 5120 + n * 32, wa + s * 5120 + 2560 + n * 32);
#pragma unroll 1
    for (unsigned sp = 0; sp < SPIN_CAP && !__all((unsigned)(sg0_ >> 32) == tag && (unsigned)(sg1_ >> 32) == tag); ++sp) {
      sg0_ = ldg64(ws + WS_STATS + (tok0 + tk0) * 8); sg1_ = ldg64(ws + WS_STATS + (tok0 + tk1) * 8); }
    f32x4 base[4];
#pragma unroll
    for (int e = 0; e < 4; ++e) {
      const unsigned sg = (unsigned)(e < 2 ? sg0_ : sg1_); const float m = blo(sg), rd = __builtin_amdgcn_rcpf(bhi(sg));
#pragma unroll
      for (int c = 0; c < 4; ++c) { const unsigned u = (e < 2 ? xv0 : xv1)[2 * (e & 1) + (c >> 1)]; const float v = c & 1 ? bhi(u) : blo(u); base[e][c] = v + (v - m) * rd; }
    }
    const u32x2 kk12 = *(const LAS u32x2*)(lds0 + L_TOP);
    const unsigned k1 = __builtin_amdgcn_readfirstlane(kk12[0]), k2 = __builtin_amdgcn_readfirstlane(kk12[1]);
    const unsigned i1 = 2047u - (k1 & 0xffffu) - tok0, i2 = 2047u - (k2 & 0xffffu) - tok0;
#pragma unroll
    for (int s = 0; s < NPRE; ++s)
#pragma unroll
      for (int n = 0; n < 5; ++n) asm volatile("" : "+v"(wf[s][n]));
    asm volatile("" : "+v"(base[0]), "+v"(base[1]), "+v"(base[2]), "+v"(base[3]));
    poll(ws + WS_FLAG2 + (xc * 32 + 16 * kh + (lane & 15)) * 256, lane < 16, ftag);
    ST(9); FST(61);
    const unsigned ob = WS_O + (tok0 / 16 + tg) * 20480;
    u32x4 of[10];
    auto oload = [&](int s) {
      if (s < 8) of[s] = __builtin_amdgcn_raw_buffer_load_b128(rs, lane * 16 + s * 1024, ob + kh * 8192, 16);
      else {
        const unsigned a = (8 * (s - 8) + 2 * g4) * 128 + (lane & 15) * 8;
        const u32x2 u0 = __builtin_amdgcn_raw_buffer_load_b64(rs, a, ob + 16384 + kh * 2048, 16), u1 = __builtin_amdgcn_raw_buffer_load_b64(rs, a + 128, ob + 16384 + kh * 2048, 16);
        of[s] = (u32x4){u0[0], u0[1], u1[0], u1[1]};
      }
    };
#pragma unroll
    for (int s = 0; s < OPRE; ++s) oload(s);
    FENCE();
    FST(62);
    const bool own = (i1 < 64u || i2 < 64u) && w < 5;
    const unsigned ya = own ? ((tid >= 160) * 640 + n0 + ((tid - 160 * (tid >= 160)) >> 1)) * 8 + (tid & 1) * 81920 : 0x40000000u;
    u32x2 yv[8];
    auto yload = [&]() {
      asm volatile("" ::: "memory");
#pragma unroll
      for (int e = 0; e < 8; ++e) yv[e] = __builtin_amdgcn_raw_buffer_load_b64(rs, ya, WS_YPART + e * 10240, 16);
    };
    f32x4 acc[5] = {};
#pragma unroll
    for (int s = 0; s < 10; ++s) {
      if (s + OPRE < 10) oload(s + OPRE);
      if (s + OPRE == 10 && own) yload();
      FENCE();
#pragma unroll
      for (int n = 0; n < 5; ++n) acc[n] = MFMA16(wf[s % NPRE][n], __builtin_bit_cast(bf16x8, of[s]), acc[n]);
      if (s + NPRE < 10) {
#pragma unroll
        for (int n = 0; n < 5; ++n) wf[s][n] = trfrag(wa + (s + NPRE) * 5120 + n * 32, wa + (s + NPRE) * 5120 + 2560 + n * 32);
      }
      FENCE();
    }
    ST(10); FST(63);
    if (own) {
#pragma unroll 1
      for (unsigned sp = 0; sp < SPIN_CAP; ++sp) {
        unsigned bad = 0;
#pragma unroll
        for (int e = 0; e < 8; ++e) bad |= yv[e][1] ^ tag;
        if (__all(bad == 0)) break;
        yload();
      }
      float s = 0.f;
#pragma unroll
      for (int e = 0; e < 8; ++e) s += __uint_as_float(yv[e][0]);
      s += __uint_as_float(dpp<0xB1>(__float_as_uint(s)));
      const unsigned kk = (tid >= 160 ? k2 : k1) >> 16;
      const float r = blo(kk & 0x8000u ? kk & 0x7fffu : ~kk & 0xffffu);
      if (!(tid & 1)) *(LAS float*)(lds0 + L_UPD + (tid >> 1) * 4) = rb(rb(s) * rb(__builtin_amdgcn_rcpf(1.f + __expf(-r))));
    }
#pragma unroll
    for (int n = 0; n < 5; ++n) *(LAS f32x4*)(lds0 + L_RED + kh * 21504 + (16 * tg + (lane & 15)) * 336 + (16 * n + 4 * g4) * 4) = acc[n];
    BAR();
    auto fin = [&](unsigned tk, unsigned ch, unsigned eo, f32x4 b0, f32x4 b1) {
      const unsigned a = lds0 + L_RED + tk * 336 + ch * 32;
      b0 += *(const LAS f32x4*)a + *(const LAS f32x4*)(a + 21504); b1 += *(const LAS f32x4*)(a + 16) + *(const LAS f32x4*)(a + 21504 + 16);
      if (tk == i1 || tk == i2) { const unsigned u = lds0 + L_UPD + (tk == i1 ? 0 : 320) + ch * 32; b0 += *(const LAS f32x4*)u; b1 += *(const LAS f32x4*)(u + 16); }
      *(GAS u32x4*)((char*)p.out + eo) = (u32x4){pk(b0[0], b0[1]), pk(b0[2], b0[3]), pk(b1[0], b1[1]), pk(b1[2], b1[3])};
    };
    fin(tk0, ch0, eo0, base[0], base[1]);
    if (two) fin(tk1, ch1, eo1, base[2], base[3]);
    ST(11);
  }
}

__global__ void k_spin(const unsigned long long* st) {
  __shared__ unsigned long long a[256], b[256];
  unsigned long long mn = ~0ull, mx = 0;
  for (int w = 0; w < 8; ++w) { unsigned long long s0 = st[((threadIdx.x * 8 + w) * 48 + 0) * 2], e = st[((threadIdx.x * 8 + w) * 48 + 6) * 2]; mn = s0 < mn ? s0 : mn; if (true) mx = e > mx ? e : mx; }
  a[threadIdx.x] = mn; b[threadIdx.x] = mx;
  __syncthreads();
  if (threadIdx.x == 0) {
    unsigned long long s = ~0ull, e = 0, sum = 0;
    for (int i = 0; i < 256; ++i) s = a[i] < s ? a[i] : s;
    for (int i = 16; i < 256; ++i) { e = b[i] > e ? b[i] : e; sum += b[i] - s; }
    if (6 == 11 || 6 == 0) for (int i = 0; i < 16; ++i) e = b[i] > e ? b[i] : e;
    unsigned long long span = 1 ? sum * 100 / 240 : (e - s) * 100; if (span > 3000000) span = 3000000;
    unsigned long long t0 = __builtin_amdgcn_s_memrealtime();
    while (__builtin_amdgcn_s_memrealtime() - t0 < span) __builtin_amdgcn_s_sleep(8);
  }
}
extern "C" void kernel_launch(void* const* d_in, const int* in_sizes, int n_in, void* d_out, int out_size, void* d_ws, size_t ws_size,
                              hipStream_t stream) {
  Params p;
  p.x = (const u16*)d_in[0]; p.wqkv = (const u16*)d_in[1]; p.wo = (const u16*)d_in[2]; p.wr = (const u16*)d_in[4];
  p.wg = (const u16*)d_in[5]; p.wu = (const u16*)d_in[6]; p.wd = (const u16*)d_in[7]; p.out = (u16*)d_out; p.ws = (char*)d_ws;
  fused_block<<<256, 512, 0, stream>>>(p);
  k_spin<<<1, 256, 0, stream>>>((const unsigned long long*)((char*)d_ws + WS_STAMP));
}
```

```cpp
#include <hip/hip_runtime.h>
#include <stdint.h>

typedef unsigned short u16;
typedef unsigned long long u64;
typedef short bf16x8 __attribute__((ext_vector_type(8)));
typedef short s16x4 __attribute__((ext_vector_type(4)));
typedef float f32x2 __attribute__((ext_vector_type(2)));
typedef float f32x4 __attribute__((ext_vector_type(4)));
typedef float f32x16 __attribute__((ext_vector_type(16)));
typedef unsigned u32x2 __attribute__((ext_vector_type(2)));
typedef unsigned u32x4 __attribute__((ext_vector_type(4)));
typedef __bf16 bf2 __attribute__((ext_vector_type(2)));
#define LAS __attribute__((address_space(3)))
#define GAS __attribute__((address_space(1)))
#define DI __device__ __forceinline__

constexpr unsigned WS_GEN = 0, WS_FLAG1 = 4096, WS_FLAG2 = 131072, WS_CAND = 262144, WS_STATS = 278528, WS_PARTA = 524288,
                   WS_YPART = 1572864, WS_QKV = 2097152, WS_O = 10485760, WS_END = 13631488, WS_STAMP = 16777216;
constexpr unsigned L_WO = 0, L_KV = 102400, L_FFNA = 65536;
constexpr unsigned KVS = 3264, L_V = 1536, VPS = 576;
constexpr unsigned L_RED = L_KV, L_UPD = L_KV + 43008;
constexpr unsigned L_KEY = 154624, L_TOP = L_KEY + 32, L_CNT = L_KEY + 48  , L_KEYS = L_KEY + 64, L_STAT = L_KEY + 96, L_CS = L_STAT + 1024, L_TMP = L_CS + 2048;
constexpr unsigned L_Y = L_KV + 44032;
constexpr unsigned SPIN_CAP = 1u << 20;
constexpr unsigned ONE2 = 0x3f803f80u;
#define CODE_KB 36

constexpr int UP[20] = {0, 0, 1, 1, 2, 2, 3, 3, 4, 4, 5, 6, 6, 7, 7, 8, 8, 9, 9, 9}, UQ[18] = {0, 1, 0, 1, 0, 1, 0, 1, 0, 1, 1, 0, 1, 0, 1, 0, 1, 0};
template <int N> struct IC { static constexpr int value = N; };

struct Params { const u16 *x, *wqkv, *wo, *wr, *wg, *wu, *wd; u16* out; char* ws; };

#define ST(i) do {} while (0)

#define FST(i) do {} while (0)
#define POLLGAP 1
#define WAITV(n) asm volatile("s_waitcnt vmcnt(" #n ")" ::: "memory")
#define REQ0 3
#define LA 3
#define LAMODE 1
#define BC_AT 7
#define FORCE_FB 0
#define WO1 0
#define OPRE 4
#define TLBW 0
#define HPRIO2 1
#define ROT 3
#define KEYDELAY 200
#define PRIO 0
#define WAITN_(n) WAITV(n)
#define WAITN(n) WAITN_(n)
#define WO_PRE 6
#define BAR() do { __builtin_amdgcn_s_waitcnt(0xC07F); asm volatile("s_barrier" ::: "memory"); } while (0)
#define FENCE() __builtin_amdgcn_sched_barrier(0)
#define MFMA32(a, b, c) __builtin_amdgcn_mfma_f32_32x32x16_bf16((a), (b), (c), 0, 0, 0)
#define MFMA16(a, b, c) __builtin_amdgcn_mfma_f32_16x16x32_bf16((a), (b), (c), 0, 0, 0)

DI void glds(unsigned voff, const void* sbase, unsigned lds) {
  asm volatile("s_mov_b32 m0, %2\n\ts_nop 0\n\tglobal_load_lds_dwordx4 %0, %1" :: "v"(voff), "s"(sbase), "s"(lds) : "memory");
}
DI bf16x8 lds128(unsigned a) { return *(const LAS bf16x8*)a; }
DI s16x4 ldstr(unsigned a) { return __builtin_amdgcn_ds_read_tr16_b64_v4i16((LAS s16x4*)a); }
DI bf16x8 trfrag(unsigned a, unsigned a2) { s16x4 lo = ldstr(a), hi = ldstr(a2); return __builtin_shufflevector(lo, hi, 0, 1, 2, 3, 4, 5, 6, 7); }
DI unsigned pk(float a, float b) { f32x2 v = {a, b}; return __builtin_bit_cast(unsigned, __builtin_convertvector(v, bf2)); }
DI float rb(float a) { return __uint_as_float(pk(a, 0.f) << 16); }
DI float blo(unsigned v) { return __uint_as_float(v << 16); }
DI float bhi(unsigned v) { return __uint_as_float(v & 0xffff0000u); }
DI float dot2(unsigned a, unsigned b, float c) { return __builtin_amdgcn_fdot2_f32_bf16(__builtin_bit_cast(bf2, a), __builtin_bit_cast(bf2, b), c, false); }
DI float xhalf_sum(float v) { auto r = __builtin_amdgcn_permlane32_swap(__float_as_uint(v), __float_as_uint(v), false, false); return __uint_as_float(r[0]) + __uint_as_float(r[1]); }
DI u64 ldg64(const char* a) { return __hip_atomic_load((GAS u64*)a, __ATOMIC_RELAXED, __HIP_MEMORY_SCOPE_AGENT); }
DI void stg64(char* a, u64 v) { __hip_atomic_store((GAS u64*)a, v, __ATOMIC_RELAXED, __HIP_MEMORY_SCOPE_AGENT); }
DI u64 gran(unsigned v, unsigned tag) { return ((u64)tag << 32) | v; }
template <int CTRL> DI unsigned dpp(unsigned v) { return __builtin_amdgcn_update_dpp(0u, v, CTRL, 0xf, 0xf, true); }
DI unsigned wave_umax(unsigned v) {
  v = max(v, dpp<0x128>(v)); v = max(v, dpp<0x124>(v)); v = max(v, dpp<0x122>(v)); v = max(v, dpp<0x121>(v));
  auto r = __builtin_amdgcn_permlane16_swap(v, v, false, false); v = max(r[0], r[1]);
  auto q = __builtin_amdgcn_permlane32_swap(v, v, false, false); return max(q[0], q[1]);
}
DI float sum32(float v) {
  v += __uint_as_float(dpp<0x128>(__float_as_uint(v))); v += __uint_as_float(dpp<0x124>(__float_as_uint(v)));
  v += __uint_as_float(dpp<0x122>(__float_as_uint(v))); v += __uint_as_float(dpp<0x121>(__float_as_uint(v)));
  auto r = __builtin_amdgcn_permlane16_swap(__float_as_uint(v), __float_as_uint(v), false, false);
  return __uint_as_float(r[0]) + __uint_as_float(r[1]);
}
DI void ln_stats(float s1, float s2, float& mb, float& db) {
  float mean = s1 * (1.f / 640.f); float var = fmaxf(s2 * (1.f / 640.f) - mean * mean, 0.f);
  mb = rb(mean); db = rb(__builtin_amdgcn_sqrtf(rb(rb(var) + 1e-5f)));
}
DI void poll(const char* addr, bool act, u64 want) {
#pragma unroll 1
  for (unsigned sp = 0; sp < SPIN_CAP; ++sp) { u64 v = ldg64(addr); if (__all(!act || v == want)) break; if (POLLGAP) __builtin_amdgcn_s_sleep(POLLGAP); }
  asm volatile("" ::: "memory");
}
struct Keys { u32x4 g[4]; };
DI bool keys_top2(const Keys& k, unsigned tag, unsigned& k1, unsigned& k2) {
  unsigned bad = 0, a = 0, b = 0;
#pragma unroll
  for (int e = 0; e < 4; ++e) {
    bad |= (k.g[e][1] ^ tag) | (k.g[e][3] ^ tag);
    const unsigned hi = max(k.g[e][0], k.g[e][2]), lo = min(k.g[e][0], k.g[e][2]);
    b = max(max(b, lo), min(a, hi)); a = max(a, hi);
  }
  if (!__all(bad == 0)) return false;
  k1 = wave_umax(a); k2 = wave_umax(a == k1 ? b : a);
  k1 = __builtin_amdgcn_readfirstlane(k1); k2 = __builtin_amdgcn_readfirstlane(k2);
  return true;
}
template <class RS> DI void keys_wait(RS rs, int lane, unsigned tag, unsigned& k1, unsigned& k2) {
#pragma unroll 1
  for (unsigned sp = 0; sp < SPIN_CAP; ++sp) {
    Keys k;
    asm volatile("" ::: "memory");
#pragma unroll
    for (int e = 0; e < 4; ++e) k.g[e] = __builtin_amdgcn_raw_buffer_load_b128(rs, lane * 16 + e * 1024, WS_CAND, 16);
    if (keys_top2(k, tag, k1, k2)) break;
    __builtin_amdgcn_s_sleep(16);
  }
}
struct WoOff { unsigned r[5], l[5]; };
DI WoOff wo_offsets(int lane) {
  WoOff o;
#pragma unroll
  for (int m = 0; m < 5; ++m) {
    const unsigned L = 64 * m + lane, rho = (L * 205) >> 11, c = L - rho * 10, q = rho & 3, g = (rho >> 2) & 3, t = rho >> 4;
    o.r[m] = (24 * (g >> 1) + 8 * (g & 1) + 4 * t + q) * 1280 + c * 16; o.l[m] = (40 * g + 16 + 4 * t + q) * 1280 + c * 16;
    asm volatile("" : "+v"(o.r[m]), "+v"(o.l[m]));
  }
  return o;
}
DI void wo_steps(int s0, int s1, const WoOff& o, const u16* wo, int cs80, unsigned lds0) {
#pragma unroll 1
  for (int u = s0; u < s1; ++u) {
    const int kh = u >= 10, st = u - 10 * kh;
    const char* sp = (const char*)wo + cs80 * 160 + (kh * 320 + (st < 8 ? st * 40 : (st - 8) * 160)) * 1280;
    const unsigned d = lds0 + L_WO + u * 5120;
    if (st < 8) {
#pragma unroll
      for (int m = 0; m < 5; ++m) glds(o.r[m], sp, d + m * 1024);
    } else {
#pragma unroll
      for (int m = 0; m < 5; ++m) glds(o.l[m], sp, d + m * 1024);
    }
  }
}

__global__ __launch_bounds__(512, 2) void fused_block(Params p) {
  __shared__ __attribute__((aligned(1024))) char smem[163840];
  const unsigned lds0 = (unsigned)(size_t)smem;
  u64 pc0; asm volatile("s_getpc_b64 %0" : "=s"(pc0));
  const int tid = threadIdx.x, lane = tid & 63, w = __builtin_amdgcn_readfirstlane(tid >> 6);
  const int b = blockIdx.x, xc = b & 7, i = b >> 3;
  const bool spare = i < 2;
  const int xp = xc >> 1, odd = xc & 1;
  const int cs80 = i & 7;
  const int h = lane >> 5, l31 = lane & 31, g4 = lane >> 4, q4 = (lane & 15) >> 2, p4 = lane & 3;
  unsigned tag = 0;
  auto lds_tag = [&]() { tag = __builtin_amdgcn_readfirstlane(*(const LAS unsigned*)(lds0 + L_KEY)); };
  const unsigned j30 = (unsigned)(i - 2) & 31u, j7 = (j30 * 37) >> 8;
  const unsigned trl = j30 < 28 ? j7 : (2 * odd + j30) & 3, ct = j30 < 28 ? j30 - j7 * 7 + odd * 8 : 7;
  unsigned sn = ROT == 3 ? 3 * (trl + ct - 3 * (((trl + ct) * 43) >> 7)) + ((xc * 3) >> 3) : ROT == 2 ? 5 * ((trl + ct) & 1) + ((xc * 5) >> 3) : ROT == 1 ? (xc * 5) >> 2 : ROT == 4 ? (5 * ((trl + ct) & 3)) / 2 + (xc & 1) : 0;
  const unsigned tr = 4 * xp + trl;
  const int rb8 = 64 * xp + 32 * odd + i;
  const unsigned x0 = (lane >> 3) * 1280 + (((lane & 7) ^ g4) * 16), x1 = (8 + (lane >> 3)) * 1280 + (((lane & 7) ^ (4 + g4)) * 16);
  const unsigned w0 = g4 * 3840 + (((lane & 15) ^ (g4 << 2)) * 16), w1 = w0 + 4 * 3840;
  if (w >= 4) sn = sn == 9 ? 0 : sn + 1;
  unsigned fx = tr * (128 * 1280) + sn * 128 + (w & 3) * (32 * 1280), fw = ct * 256 + (sn * 64 + (w & 3) * 16) * 3840, fd = lds0 + (w >> 2) * 32768 + (w & 3) * 4096;
  unsigned fv[8] = {x0 + fx, w0 + fw, x1 + fx, w1 + fw, x0 + fx + 16 * 1280, w0 + fw + 8 * 3840, x1 + fx + 16 * 1280, w1 + fw + 8 * 3840};
  sn = sn == 9 ? 0 : sn + 1;
  asm volatile("" : "+v"(fv[0]), "+v"(fv[1]), "+v"(fv[2]), "+v"(fv[3]), "+v"(fv[4]), "+v"(fv[5]), "+v"(fv[6]), "+v"(fv[7]));
  asm volatile("" : "+s"(fd), "+s"(sn));
  FENCE();
  if (!spare) {
#pragma unroll
    for (int e = 0; e < 8; ++e) glds(fv[e], e & 1 ? (const void*)p.wqkv : (const void*)p.x, fd + (e & 1) * 16384 + (e >> 1) * 1024);
  }
  FST(28);
  char* const ws = p.ws;
  const auto rs = __builtin_amdgcn_make_buffer_rsrc(ws, 0, WS_END, 0x00020000);
  const char* const xt = (const char*)p.x + tr * (128 * 1280);
  const char* const wt = (const char*)p.wqkv + ct * 256;
  auto pieces = [&](unsigned e, unsigned slot) {
    const char* xs = xt + (sn * 128 + e * (16 * 1280)); const char* wsb = wt + (sn * 64 + e * 8) * 3840; const unsigned d = lds0 + slot * 32768 + e * 2048;
    glds(x0, xs, d); glds(w0, wsb, d + 16384); glds(x1, xs, d + 1024); glds(w1, wsb, d + 16384 + 1024);
  };

  const int rrow = 8 * rb8 + 2 * w + h;
  u32x4 rx[3], rw[3]; unsigned pfv = 0, genv; float a1 = 0.f, a2 = 0.f, ar = 0.f, sw = 0.f;
  auto router_load = [&]() {
    if (CODE_KB && lane < CODE_KB / 2) asm volatile("global_load_dword %0, %1, %2" : "+v"(pfv) : "v"(((i & 3) * 4 + w) * (CODE_KB * 64) + lane * 128), "s"(pc0) : "memory");
    const char* xr = (const char*)p.x + rrow * 1280 + l31 * 16; const char* wr = (const char*)p.wr + l31 * 16;
    const int t3 = l31 < 16 ? 1024 : 0;
    rx[0] = *(const GAS u32x4*)xr; rx[1] = *(const GAS u32x4*)(xr + 512); rw[0] = *(const GAS u32x4*)wr; rw[1] = *(const GAS u32x4*)(wr + 512);
    rx[2] = *(const GAS u32x4*)(xr + t3); rw[2] = *(const GAS u32x4*)(wr + t3);
    genv = __hip_atomic_load((GAS unsigned*)(ws + WS_GEN), __ATOMIC_RELAXED, __HIP_MEMORY_SCOPE_AGENT);
    FENCE();
  };
  auto router_dots = [&]() {
    if (l31 >= 16) rx[2] = rw[2] = (u32x4){};
#pragma unroll
    for (int e = 0; e < 3; ++e)
#pragma unroll
      for (int c = 0; c < 4; ++c) { a1 = dot2(rx[e][c], ONE2, a1); a2 = dot2(rx[e][c], rx[e][c], a2); ar = dot2(rx[e][c], rw[e][c], ar); sw = dot2(rw[e][c], ONE2, sw); }
    tag = __builtin_amdgcn_readfirstlane(genv) + 1u;
    if (tid == 0) *(LAS unsigned*)(lds0 + L_KEY) = tag;
    asm volatile("" : "+v"(a1), "+v"(a2), "+v"(ar), "+v"(sw) : "v"(pfv));
  };
  float rmb = 0.f, rdb = 0.f; unsigned rk = 0, rv = 0;
  auto router_step = [&](int k) {
    auto fdpp = [&](float& v, int st) { const unsigned u = __float_as_uint(v); v += __uint_as_float(st == 0 ? dpp<0x128>(u) : st == 1 ? dpp<0x124>(u) : st == 2 ? dpp<0x122>(u) : dpp<0x121>(u)); };
    auto fswp = [&](float& v) { auto r = __builtin_amdgcn_permlane16_swap(__float_as_uint(v), __float_as_uint(v), false, false); v = __uint_as_float(r[0]) + __uint_as_float(r[1]); };
    if (k < 4) { fdpp(a1, k); fdpp(a2, k); fdpp(ar, k); fdpp(sw, k); }
    if (k == 4) { fswp(a1); fswp(a2); }
    if (k == 5) { fswp(ar); fswp(sw); }
    if (k == 6) { a1 *= 1.f / 640.f; a2 = fmaxf(a2 * (1.f / 640.f) - a1 * a1, 0.f); }
    if (k == 7) { rmb = rb(a1); a2 = rb(a2) + 1e-5f; }
    if (k == 8) { a2 = __builtin_amdgcn_sqrtf(rb(a2)); ar -= rmb * sw; }
    if (k == 9) { rdb = rb(a2); a2 = __builtin_amdgcn_rcpf(rdb); }
    if (k == 10) { rk = pk(ar * a2, 0.f) & 0xffffu; rv = (__float_as_uint(rmb) >> 16) | __float_as_uint(rdb); }
    if (k == 11) rk = (rk & 0x8000u ? (~rk & 0xffffu) : (rk | 0x8000u)) << 16;
    if (k == 12) { if (l31 == 0) *(LAS unsigned*)(lds0 + L_KEYS + (2 * w + h) * 4) = rk | (2047u - rrow); }
    if (k == 13) { if (l31 == 0) __builtin_amdgcn_raw_buffer_store_b64((u32x2){rv, tag}, rs, WS_STATS + rrow * 8, 0, 16); }
    auto max8 = [&](unsigned& v, int st) { v = max(v, st == 0 ? dpp<0xB1>(v) : st == 1 ? dpp<0x4E>(v) : dpp<0x141>(v)); };
    if (k == 14) rk = rv = *(const LAS unsigned*)(lds0 + L_KEYS + (lane & 7) * 4);
    if (k >= 16 && k < 19) max8(rv, k - 16);
    if (k == 19) rk = rk == rv ? 0u : rk;
    if (k >= 20 && k < 23) max8(rk, k - 20);
    if (k == 23) { if (w == 0 && lane < 2) __builtin_amdgcn_raw_buffer_store_b64((u32x2){lane ? rk : rv, tag}, rs, WS_CAND + (2 * rb8 + lane) * 8, 0, 16); }
    asm volatile("" : "+v"(a1), "+v"(a2), "+v"(ar), "+v"(sw), "+v"(rmb), "+v"(rdb), "+v"(rk), "+v"(rv));
  };

  bf16x8 wdf[30];
  if (!spare) {
    if (w >= 4) {
      const int j = w - 4;
      auto issue = [&](int slot) { pieces(2 * j, slot); pieces(2 * j + 1, slot); sn = sn == 9 ? 0 : sn + 1; };
      if (tid == 384) *(LAS u32x4*)(lds0 + L_CNT) = (u32x4){};
      const unsigned au = tr * 15 + ct, cg = (au * 205) >> 11, kg = au - cg * 10;
      FST(31); BAR();
      FST(1); issue(2); FST(2);
      WAITV(8); FST(3); BAR();
      issue(3); pieces(2 * j, 0);
      WAITV(12); BAR();
      pieces(2 * j + 1, 0); sn = sn == 9 ? 0 : sn + 1; issue(1);
      WAITV(16); BAR();
#pragma unroll 1
      for (int s = 6; s < 9; ++s) { FST(3 * s - 8); issue(s & 3); FST(3 * s - 7); WAITV(16); FST(3 * s - 6); BAR(); }
      lds_tag();
      auto ffn_dma = [&]() {
        const unsigned fo = (lane >> 3) * 3072 + (lane & 7) * 16;
        const char* fb = (const char*)(j < 2 ? p.wg : p.wu) + (kg * 64 + 32 * (j & 1)) * 3072 + cg * 128;
#pragma unroll
        for (int e = 0; e < 4; ++e) glds(fo, fb + e * 8 * 3072, lds0 + L_FFNA + (4 * j + e) * 1024);
      };
      auto ffn_landed = [&]() { WAITV(0); if (lane == 0) __hip_atomic_fetch_add((LAS unsigned*)(lds0 + L_CNT + 12), 1u, __ATOMIC_RELAXED, __HIP_MEMORY_SCOPE_WORKGROUP); };
      if (w == 6) {
        const char* ga = ws + WS_STATS + tr * 1024;
        u32x2 g0, g1;
        asm volatile("global_load_dwordx2 %0, %2, %3 sc1\n\tglobal_load_dwordx2 %1, %2, %3 offset:512 sc1" : "=&v"(g0), "=&v"(g1) : "v"(lane * 8), "s"(ga) : "memory");
        issue(1); WAITV(16); BAR();
        ffn_dma();
        asm volatile("s_waitcnt vmcnt(12)\n\ts_barrier" : "+v"(g0), "+v"(g1) :: "memory");
        if (!__all(g0[1] == tag && g1[1] == tag)) {
#pragma unroll 1
          for (unsigned sp = 0; sp < SPIN_CAP; ++sp) {
            g0 = __builtin_bit_cast(u32x2, ldg64(ga + lane * 8)); g1 = __builtin_bit_cast(u32x2, ldg64(ga + 512 + lane * 8));
            if (__all(g0[1] == tag && g1[1] == tag)) break; }
        }
        *(LAS f32x2*)(lds0 + L_STAT + lane * 8) = (f32x2){blo(g0[0]), __builtin_amdgcn_rcpf(bhi(g0[0]))};
        *(LAS f32x2*)(lds0 + L_STAT + 512 + lane * 8) = (f32x2){blo(g1[0]), __builtin_amdgcn_rcpf(bhi(g1[0]))};
        FST(23); WAITV(4); BAR();
        ffn_landed();
      } else if (w < 6) { issue(1); WAITV(16); BAR(); ffn_dma(); WAITV(12); BAR(); WAITV(4); BAR(); ffn_landed(); }
      else {
        Keys kk;
        asm volatile("buffer_load_dwordx4 %0, %4, %5, %6 offen sc1\n\tbuffer_load_dwordx4 %1, %4, %5, %6 offen offset:1024 sc1\n\t"
                     "buffer_load_dwordx4 %2, %4, %5, %6 offen offset:2048 sc1\n\tbuffer_load_dwordx4 %3, %4, %5, %6 offen offset:3072 sc1"
                     : "=&v"(kk.g[0]), "=&v"(kk.g[1]), "=&v"(kk.g[2]), "=&v"(kk.g[3]) : "v"(lane * 16), "s"(rs), "s"(WS_CAND) : "memory");
        issue(1); WAITV(16); BAR();
        ffn_dma();
        asm volatile("s_waitcnt vmcnt(12)\n\ts_barrier" : "+v"(kk.g[0]), "+v"(kk.g[1]), "+v"(kk.g[2]), "+v"(kk.g[3]) :: "memory");
        unsigned k1 = 0, k2 = 0;
        const bool early = keys_top2(kk, tag, k1, k2);
        u32x4 xc0 = {ONE2, ONE2, ONE2, ONE2}, xc1 = xc0;
        { const char* xq = (const char*)p.x + ((2047u - (lane & 1 ? k2 : k1)) & 2047u) * 1280 + kg * 128 + g4 * 16;
          if ((lane & 15) < 2) asm volatile("global_load_dwordx4 %0, %2, off\n\tglobal_load_dwordx4 %1, %2, off offset:64" : "+v"(xc0), "+v"(xc1) : "v"(xq) : "memory"); }
        asm volatile("s_waitcnt vmcnt(6)\n\ts_barrier\n\ts_waitcnt vmcnt(0)" : "+v"(xc0), "+v"(xc1) :: "memory");
        FST(23);
        if (!early) {
          keys_wait(rs, lane, tag, k1, k2);
          if ((lane & 15) < 2) { const char* xq = (const char*)p.x + ((2047u - (lane & 1 ? k2 : k1)) & 2047u) * 1280 + kg * 128 + g4 * 16; xc0 = *(const GAS u32x4*)xq; xc1 = *(const GAS u32x4*)(xq + 64); }
        }
        if (b == 16 && lane == 0) __hip_atomic_store((GAS unsigned*)(ws + WS_GEN), tag, __ATOMIC_RELAXED, __HIP_MEMORY_SCOPE_AGENT);
        if (lane == 0) *(LAS u32x2*)(lds0 + L_TOP) = (u32x2){k1, k2};
#pragma unroll 1
        for (unsigned sp = 0; sp < SPIN_CAP; ++sp) { if (__builtin_amdgcn_readfirstlane(*(volatile LAS unsigned*)(lds0 + L_CNT + 12)) >= 3u) break; __builtin_amdgcn_s_sleep(1); }
        asm volatile("" ::: "memory");
#pragma unroll
        for (int n = 0; n < 8; ++n) {
          const unsigned a = lds0 + L_FFNA + (n >> 2) * 8192 + (8 * g4 + q4) * 128 + (n & 3) * 32 + 8 * p4;
          const f32x4 y = MFMA16(trfrag(a + 4096, a + 4608), __builtin_bit_cast(bf16x8, xc1), MFMA16(trfrag(a, a + 512), __builtin_bit_cast(bf16x8, xc0), (f32x4){}));
          if ((lane & 15) < 3) *(LAS f32x4*)(lds0 + L_TMP + (((n >> 2) * 3 + (lane & 15)) * 64 + 16 * (n & 3) + 4 * g4) * 4) = y;
        }
#pragma unroll
        for (int n = 0; n < 6; ++n) stg64(ws + WS_PARTA + (au * 384 + n * 64 + lane) * 8, gran(*(const LAS unsigned*)(lds0 + L_TMP + (n * 64 + lane) * 4), tag));
        FST(25);
      }
    } else {
      router_load();
      const int fh = w >> 1, th = w & 1;
      unsigned xa[4], wa[2], ko[4];
#pragma unroll
      for (int e = 0; e < 4; ++e) { const int ks = (2 * th + e) & 3, row = 64 * th + l31; xa[e] = lds0 + row * 128 + (((2 * ks + h) ^ ((row >> 1) & 7)) * 16); ko[e] = ks * 4096; }
#pragma unroll
      for (int f = 0; f < 2; ++f) wa[f] = lds0 + 16384 + (8 * h + q4) * 256 + (((4 * (2 * fh + f) + 2 * (g4 & 1) + (p4 >> 1)) ^ (q4 << 2)) * 16) + 8 * (p4 & 1);
      f32x16 acc[2][2] = {};
      f32x4 cs0 = {}, cs1 = {};
      const unsigned selw = (lane & 15) == (g4 & 1) ? ONE2 : 0u;
      const bf16x8 sel = __builtin_bit_cast(bf16x8, (u32x4){selw, selw, selw, selw});
      struct Fr { bf16x8 v[4]; };
      Fr FA[4], FB[4];
      auto rd = [&](Fr* F, int n, unsigned so) {
        const int e = n >> 2, j = n & 3;
        if (j < 2) { const unsigned a = wa[j] + (so + ko[e]); F[e].v[j] = trfrag(a, a + 1024); } else F[e].v[j] = lds128(xa[e] + so + (j - 2) * 4096);
      };
      auto colsum = [&](const Fr* C, int e) { cs0 = MFMA16(C[e].v[0], sel, cs0); cs1 = MFMA16(C[e].v[1], sel, cs1); };
      bf16x8 ax[2], bx[2]; float rr[2], c0, c1; f32x2 st0, st1; float cp[4];
      const float qs = ct < 5 ? 0.22360679774997896f * 1.4426950408889634f : 1.f;
      const unsigned ca = lds0 + L_CS + fh * 512 + l31 * 4, cown = lds0 + L_CS + w * 256 + ((lane & 15) * 16 + 4 * g4) * 4;
      auto prep = [&](int n) {
        if (n == 12) { cp[0] = *(const LAS float*)ca; cp[1] = *(const LAS float*)(ca + 256); cp[2] = *(const LAS float*)(ca + 128); cp[3] = *(const LAS float*)(ca + 384); }
        if (n == 13) { st0 = *(const LAS f32x2*)(lds0 + L_STAT + (64 * th + l31) * 8); st1 = *(const LAS f32x2*)(lds0 + L_STAT + (64 * th + 32 + l31) * 8); }
      };
      auto body = [&](Fr* N, const Fr* C, unsigned so, int r0 = 99) {
#pragma unroll
        for (int n = 0; n < 16; ++n) {
          const int e = n >> 2, f = (n >> 1) & 1, t = n & 1;
          acc[f][t] = MFMA32(C[e].v[f], C[e].v[2 + t], acc[f][t]);
          if (n < 4) { rd(N, 2 * n, so); rd(N, 2 * n + 1, so); } else if (n < 12) rd(N, n + 4, so);
          if (n >= 2 && r0 + n - 2 < 24) router_step(r0 + n - 2);
          if (r0 == 100) prep(n);
          FENCE();
        }
        if (r0 == 100) { cs0 = cs1 = (f32x4){}; colsum(N, 2); colsum(N, 3); }
        colsum(N, 0); colsum(N, 1);
        FENCE();
      };
      FST(29);
      if (CODE_KB) WAITV(8); else WAITV(7);
      FST(30); BAR(); ST(1); FST(31);
#pragma unroll
      for (int n = 0; n < 16; ++n) rd(FA, n, 0);
      FST(32); router_dots(); FST(33);
      colsum(FA, 0); colsum(FA, 1);
      BAR();
#pragma unroll
      for (int s = 1; s <= 7; s += 2) {
        FST(2 * s); body(FB, FA, (s & 3) * 32768, s == 1 ? 0 : 99); FST(2 * s + 1); BAR();
        FST(2 * s + 2); body(FA, FB, ((s + 1) & 3) * 32768, s == 1 ? 14 : 99);
        if (s == 7 && (lane & 15) < 2) { *(LAS f32x4*)cown = cs0; *(LAS f32x4*)(cown + 128) = cs1; }
        FST(2 * s + 3); BAR();
      }
      FST(18);
      body(FB, FA, 32768, 100);
      FST(19);
      const unsigned qo = WS_QKV + (4 * tr + 2 * th) * 122880 + (16 * ct + 8 * fh + h) * 512 + l31 * 16;
      unsigned pw[8];
      auto split3 = [&](float c) {
        const float hi = rb(c), r1 = c - hi, mid = rb(r1);
        return __builtin_bit_cast(bf16x8, (u32x4){h ? 0u : pk(hi, mid), h ? 0u : pk(r1 - mid, 0.f), 0u, 0u});
      };
      auto epi = [&](int tile, int part) {
        const int f = tile & 1, t = tile >> 1;
#pragma unroll
        for (int g = 2 * part; g < 2 * part + 2; ++g) {
          float v0 = acc[f][t][2 * g] * rr[t]; asm("" : "+v"(v0));
          pw[g] = pk(v0, acc[f][t][2 * g + 1] * rr[t]);
        }
        if (part & 1) {
          const int g = part >> 1;
          auto r0 = __builtin_amdgcn_permlane32_swap(pw[4 * g], pw[4 * g + 2], false, false);
          auto r1 = __builtin_amdgcn_permlane32_swap(pw[4 * g + 1], pw[4 * g + 3], false, false);
          __builtin_amdgcn_raw_buffer_store_b128((u32x4){r0[0], r1[0], r0[1], r1[1]}, rs, qo + t * 122880 + (4 * f + 2 * g) * 512, 0, 16);
        }
      };
#pragma unroll
      for (int n = 0; n < 20; ++n) {
        const int tile = n < 8 ? n >> 2 : n < 10 ? n - 8 : n < 15 ? 2 : 3, e = n < 8 ? n & 3 : n < 10 ? 4 : (n - 10) % 5, f = tile & 1, t = tile >> 1;
        acc[f][t] = e < 4 ? MFMA32(FB[e].v[f], FB[e].v[2 + t], acc[f][t]) : MFMA32(ax[f], bx[t], acc[f][t]);
        if (n == 0) { const unsigned m = pk(-st0[0], -st0[0]); bx[0] = __builtin_bit_cast(bf16x8, (u32x4){m, m, 0u, 0u}); rr[0] = st0[1] * qs; c0 = cp[0] + cp[1]; asm volatile("" : "+v"(bx[0]), "+v"(rr[0]), "+v"(c0)); }
        if (n == 1) { const unsigned m = pk(-st1[0], -st1[0]); bx[1] = __builtin_bit_cast(bf16x8, (u32x4){m, m, 0u, 0u}); rr[1] = st1[1] * qs; c1 = cp[2] + cp[3]; asm volatile("" : "+v"(bx[1]), "+v"(rr[1]), "+v"(c1)); }
        if (n == 1 && (lane & 15) < 2) { *(LAS f32x4*)(cown + 1024) = cs0; *(LAS f32x4*)(cown + 1024 + 128) = cs1; }
        if (n == 2) { cp[0] = *(const LAS float*)(lds0 + L_CS + 1024 + w * 256 + l31 * 4); cp[1] = *(const LAS float*)(lds0 + L_CS + 1024 + w * 256 + 128 + l31 * 4); }
        if (n == 5) { ax[0] = split3(c0 + cp[0]); asm volatile("" : "+v"(ax[0])); }
        if (n == 6) { ax[1] = split3(c1 + cp[1]); asm volatile("" : "+v"(ax[1])); }
        if (n >= 10) epi((n - 10) >> 2, (n - 10) & 3);
        FENCE();
        if (n == 9 || n == 13 || n == 17) FST(24 + (n - 9) / 4);
      }
      ST(2); FST(20);
      epi(2, 2); epi(2, 3);
#pragma unroll
      for (int part = 0; part < 4; ++part) epi(3, part);
      FST(21);
      WAITV(0);
      if (lane == 0 && __hip_atomic_fetch_add((LAS unsigned*)(lds0 + L_CNT), 1u, __ATOMIC_RELAXED, __HIP_MEMORY_SCOPE_WORKGROUP) == 3u)
        stg64(ws + WS_FLAG1 + (tr * 16 + ct) * 256, gran(tag, ~tag));
      ST(3); FST(22);
    }
  } else {
    if (w < 4) router_load();
#pragma unroll 1
    for (int e = 0; e < 15; ++e) {
      const unsigned L = 512 * e + tid, r = (L * 52429) >> 22, pc = L - r * 80;
      glds(r * 1280 + (pc ^ (((r & 3) | ((r >> 1) & 4)) << 1)) * 16, (const char*)p.wd + (2 * xc + i) * 96 * 1280, lds0 + (8 * e + w) * 1024);
    }
    if (w < 4) {
      router_dots();
#pragma unroll
      for (int k = 0; k < 14; ++k) router_step(k);
    }
    if (tid == 384) *(LAS u32x4*)(lds0 + L_CNT) = (u32x4){};
    BAR();
    if (w == 0) {
#pragma unroll
      for (int k = 14; k < 24; ++k) router_step(k);
    }
    WAITV(0); BAR(); ST(1);
    if (w >= 4) {
      lds_tag();
#pragma unroll
      for (int n = 0; n < 30; ++n) {
        const unsigned a = lds0 + (8 * g4 + q4) * 1280 + (((2 * (w - 4) + n / 15) ^ (q4 | ((g4 & 1) << 2))) * 32) + 8 * p4 + (n % 15 / 5) * 40960 + (n % 5) * 256;
        wdf[n] = trfrag(a, a + 5120);
      }
    } else if (w == 0) {
      unsigned k1 = 0, k2 = 0;
      { const u64 t0 = __builtin_amdgcn_s_memrealtime() ; while (__builtin_amdgcn_s_memrealtime() - t0 < KEYDELAY) __builtin_amdgcn_s_sleep(8); }
      keys_wait(rs, lane, tag, k1, k2);
      const char* sa = ws + WS_STATS + ((2047u - (lane & 1 ? k2 : k1)) & 2047u) * 8;
      u64 sg = ldg64(sa);
#pragma unroll 1
      for (unsigned sp = 0; sp < SPIN_CAP && !__all((unsigned)(sg >> 32) == tag); ++sp) sg = ldg64(sa);
      if (lane < 2) { *(LAS unsigned*)(lds0 + L_TOP + lane * 4) = lane ? k2 : k1; *(LAS unsigned*)(lds0 + L_TOP + 8 + lane * 4) = (unsigned)sg; }
    }
    BAR(); ST(2);
  }
  ST(4); FST(35);
  const u64 ftag = gran(tag, ~tag);

  const int tg3 = w & 3, kh = w >> 2, tok0 = 256 * xc + 64 * (i >> 3), n0 = 80 * cs80;
  const bool two = w == 4 || w == 5;
  const unsigned it0 = tid, it1 = two ? tid + 256 : tid, tk0 = (it0 * 6554) >> 16, tk1 = (it1 * 6554) >> 16, ch0 = it0 - 10 * tk0, ch1 = it1 - 10 * tk1;
  const unsigned eo0 = (tok0 + tk0) * 1280 + n0 * 2 + ch0 * 16, eo1 = (tok0 + tk1) * 1280 + n0 * 2 + ch1 * 16;
  u32x4 xv0, xv1; u64 sg0_, sg1_;
  auto pre3 = [&]() {
    xv0 = *(const GAS u32x4*)((const char*)p.x + eo0); sg0_ = ldg64(ws + WS_STATS + (tok0 + tk0) * 8);
    xv1 = *(const GAS u32x4*)((const char*)p.x + eo1); sg1_ = ldg64(ws + WS_STATS + (tok0 + tk1) * 8);
  };

  {
    const int R = i >> 2, hd = 4 * xc + (i & 3), p0 = (5 * hd) >> 1, oh = hd & 1;
    const int pf = p0 + oh, pp = oh ? p0 : p0 + 2, cc = (lane & 1 ? p0 + 2 : p0) >> 4;
    if (w < 4) {
      const int lo = w < 2, tu = 2 * w + 1 - lo, tv = 2 * w + lo;
      poll(ws + WS_FLAG1 + ((2 * R + (w >> 1)) * 16 + cc) * 256, lane < 2, ftag);
      ST(5); FST(36);
      bf16x8 qf[2][2];
      { const unsigned a0 = (pf + h) * 512 + l31 * 16, a1 = pp * 512 + l31 * 16 + oh * 8, ou = WS_QKV + (8 * R + tu) * 122880, ov = WS_QKV + (8 * R + tv) * 122880;
        const u32x4 u0 = __builtin_amdgcn_raw_buffer_load_b128(rs, a0, ou, 16), v0 = __builtin_amdgcn_raw_buffer_load_b128(rs, a0, ov, 16);
        const u32x2 u1 = __builtin_amdgcn_raw_buffer_load_b64(rs, a1, ou, 16), v1 = __builtin_amdgcn_raw_buffer_load_b64(rs, a1, ov, 16);
        qf[0][0] = __builtin_bit_cast(bf16x8, u0); qf[0][1] = __builtin_bit_cast(bf16x8, (u32x4){h ? 0u : u1[0], h ? 0u : u1[1], 0u, 0u});
        qf[1][0] = __builtin_bit_cast(bf16x8, v0); qf[1][1] = __builtin_bit_cast(bf16x8, (u32x4){h ? 0u : v1[0], h ? 0u : v1[1], 0u, 0u}); }
      pre3();
      f32x16 cz = {}, ca, cb;
#pragma unroll
      for (int r = 0; r < 16; ++r) {
        const bool le = (r & 3) + 8 * (r >> 2) + 4 * h <= l31;
        const float d = le ? 0.f : -1e30f, f = le || R == 0 ? -1e30f : 0.f;
        ca[r] = lo ? d : f; cb[r] = lo ? f : d;
      }
      const unsigned ka = lds0 + L_KV + h * 512 + l31 * 16;
      const int pi = min(4 * (g4 & 1) + p4, 5);
      const unsigned va = lds0 + L_KV + L_V + (pi >> 1) * VPS + (pi & 1) * 8 + (4 * h + q4) * 16;
      struct KF { bf16x8 a, b; };
      auto kread = [&](int kt) {
        const unsigned a = ka + kt * KVS;
        return KF{lds128(a), lds128(a + 1024)};
      };
      auto vread = [&](int kt) { const unsigned v = va + kt * KVS; return KF{trfrag(v, v + 128), trfrag(v + 256, v + 384)}; };
      auto ex = [&](const f32x16& s, int e) { return pk(__builtin_amdgcn_exp2f(s[2 * e]), __builtin_amdgcn_exp2f(s[2 * e + 1])); };
      const int P0 = lo ? 6 : 8, P1 = w == 0 ? 4 : w == 1 ? 8 : w == 2 ? 6 : 10, P2 = lo ? 2 + 2 * w : 6 + 2 * w;
      const int tp[10] = {P0, P0 + 1, P1, P1 + 1, lo ? 2 * w + 8 : 2 * w + 1, lo ? 2 * w + 9 : 2 * w, P2, P2 + 1, lo ? 2 * w + 1 : 2 * w + 8, lo ? 2 * w : 2 * w + 9};
      auto at = [&](int ps) { const int e = (ps == 1 || ps == 3 || ps == 7), t = tp[ps - e]; return (R == 0 && t < 8 ? 8 : t) + e; };
      auto setz = [&](int ps) {
        if (ps == 1 || ps == 3 || ps == 5 || ps == 7 || ps == 9) return;
        if (R == 0) { const float z = tp[ps] < 8 ? -1e30f : 0.f;
#pragma unroll
          for (int r = 0; r < 16; ++r) cz[r] = z;
          asm volatile("" : "+v"(cz)); } };
      setz(0);
      asm volatile("" : "+v"(ca), "+v"(cb), "+v"(cz), "+v"(qf[0][0]), "+v"(qf[0][1]), "+v"(qf[1][0]), "+v"(qf[1][1]));
      BAR(); ST(6); FST(39);
      KF K0, K1, K2, V0, V1, V2; f32x16 S0, S1, o[2] = {};
      K0 = kread(at(0)); V0 = vread(at(0));
      S0 = MFMA32(K0.b, qf[0][1], MFMA32(K0.a, qf[0][0], cz));
      u32x4 pa = {}, pb = {}; float x0, x1, x2, x3;
#define SET3(N, p) ((p) % 3 == 0 ? N##0 : (p) % 3 == 1 ? N##1 : N##2)
#define EXP4(s, e) do { x0 = __builtin_amdgcn_exp2f(s[4 * e]); x1 = __builtin_amdgcn_exp2f(s[4 * e + 1]); x2 = __builtin_amdgcn_exp2f(s[4 * e + 2]); x3 = __builtin_amdgcn_exp2f(s[4 * e + 3]); \
                        asm volatile("" : "+v"(x0), "+v"(x1), "+v"(x2), "+v"(x3)); FENCE(); } while (0)
#define PACK2(d, e) do { d[e] = pk(x0, x1); d[e + 1] = pk(x2, x3); asm volatile("" : "+v"(d)); FENCE(); } while (0)
      auto unit = [&](auto U, f32x16& sc, f32x16& sn) {
        constexpr int u = decltype(U)::value, np = u + 1 < 18 ? UP[u + 1] : 0, nq = u + 1 < 18 ? UQ[u + 1] : 0, pu = u > 0 ? u - 1 : 0;
        if constexpr (u == 2 || u == BC_AT) BAR();
        FST(40 + u);
        if constexpr (u + 1 < 18) { if constexpr (np != UP[u]) setz(np); sn = MFMA32(SET3(K, np).a, qf[nq][0], u + 1 == 8 || u + 1 == 10 ? ca : u + 1 >= 16 ? cb : cz); FENCE(); }
        if constexpr (u > 0) PACK2(pb, 2);
        EXP4(sc, 0);
        if constexpr (u > 0) { o[UQ[pu]] = MFMA32(SET3(V, UP[pu]).b, __builtin_bit_cast(bf16x8, pb), o[UQ[pu]]); FENCE(); }
        if constexpr (u + 2 < 18 && UP[u + 2] != UP[u + 1]) SET3(K, UP[u + 2]) = kread(at(UP[u + 2]));
        if constexpr (u + 1 < 18 && np != UP[u]) SET3(V, np) = vread(at(np));
        PACK2(pa, 0);
        EXP4(sc, 1);
        if constexpr (u + 1 < 18) { sn = MFMA32(SET3(K, np).b, qf[nq][1], sn); FENCE(); }
        PACK2(pa, 2);
        EXP4(sc, 2);
        o[UQ[u]] = MFMA32(SET3(V, UP[u]).a, __builtin_bit_cast(bf16x8, pa), o[UQ[u]]); FENCE();
        PACK2(pb, 0);
        EXP4(sc, 3);
      };
#define UNIT2(n) unit(IC<n>{}, S0, S1); unit(IC<n + 1>{}, S1, S0);
      UNIT2(0) UNIT2(2) UNIT2(4) UNIT2(6) UNIT2(8) UNIT2(10) UNIT2(12) UNIT2(14) UNIT2(16)
      PACK2(pb, 2);
      o[0] = MFMA32(V0.b, __builtin_bit_cast(bf16x8, pb), o[0]);
      auto rowsum = [&](const f32x16& a) { auto r = __builtin_amdgcn_permlane32_swap(__float_as_uint(a[8]), __float_as_uint(a[8]), false, false); return __uint_as_float(r[1]); };
      float l[2] = {rowsum(o[1]), rowsum(o[0])};
      if (FORCE_FB || !__all(l[0] > 0x1p-60f && l[0] < 0x1p60f && l[1] > 0x1p-60f && l[1] < 0x1p60f)) {
        int qt = tu;
#pragma unroll 1
        for (int e = 0; e < 2; ++e) {
          const int k0 = R > 0 ? qt : 8;
          auto score = [&](int kt, float base) {
            f32x16 c;
#pragma unroll
            for (int r = 0; r < 16; ++r) { const bool le = (r & 3) + 8 * (r >> 2) + 4 * h <= l31; c[r] = (kt == qt + 8 && !le) || (kt == qt && le) ? -1e30f : base; }
            const KF k = kread(kt);
            return MFMA32(k.b, qf[0][1], MFMA32(k.a, qf[0][0], c));
          };
          float m = -3e38f;
#pragma unroll 1
          for (int kt = k0; kt <= qt + 8; ++kt) { const f32x16 s = score(kt, 0.f);
#pragma unroll
            for (int r = 0; r < 16; ++r) m = fmaxf(m, s[r]); }
          { auto r = __builtin_amdgcn_permlane32_swap(__float_as_uint(m), __float_as_uint(m), false, false); m = fmaxf(__uint_as_float(r[0]), __uint_as_float(r[1])); }
          o[0] = (f32x16){};
#pragma unroll 1
          for (int kt = k0; kt <= qt + 8; ++kt) {
            const f32x16 s = score(kt, -m); const KF v = vread(kt);
            o[0] = MFMA32(v.a, __builtin_bit_cast(bf16x8, (u32x4){ex(s, 0), ex(s, 1), ex(s, 2), ex(s, 3)}), o[0]);
            o[0] = MFMA32(v.b, __builtin_bit_cast(bf16x8, (u32x4){ex(s, 4), ex(s, 5), ex(s, 6), ex(s, 7)}), o[0]);
          }
          const f32x16 to = o[0]; o[0] = o[1]; o[1] = to;
          const bf16x8 t0 = qf[0][0], t1 = qf[0][1]; qf[0][0] = qf[1][0]; qf[0][1] = qf[1][1]; qf[1][0] = t0; qf[1][1] = t1;
          qt = tv;
        }
        l[0] = rowsum(o[1]); l[1] = rowsum(o[0]);
      }
      ST(7); FST(59);
#pragma unroll
      for (int e = 0; e < 2; ++e) {
        const f32x16& a = o[1 - e]; const float ri = __builtin_amdgcn_rcpf(l[e]);
        const unsigned e0 = pk(a[0] * ri, a[1] * ri), e1 = pk(a[2] * ri, a[3] * ri), e2 = pk(a[4] * ri, a[5] * ri), e3 = pk(a[6] * ri, a[7] * ri);
        const u32x2 el = {pk(a[8] * ri, a[9] * ri), pk(a[10] * ri, a[11] * ri)};
        auto r0 = __builtin_amdgcn_permlane32_swap(e0, e2, false, false), r1 = __builtin_amdgcn_permlane32_swap(e1, e3, false, false);
        const unsigned ob = WS_O + (16 * R + 2 * (e ? tu : tv) + (l31 >> 4)) * 20480;
        __builtin_amdgcn_raw_buffer_store_b128((u32x4){r0[0], r1[0], r0[1], r1[1]}, rs, ob + (2 * hd + h) * 256 + (lane & 15) * 16, 0, 16);
        if (h == 0) __builtin_amdgcn_raw_buffer_store_b64(el, rs, ob + 16384 + hd * 128 + (lane & 15) * 8, 0, 16);
      }
      WAITV(0);
      if (lane == 0 && __hip_atomic_fetch_add((LAS unsigned*)(lds0 + L_CNT + 4), 1u, __ATOMIC_RELAXED, __HIP_MEMORY_SCOPE_WORKGROUP) == 3u)
        stg64(ws + WS_FLAG2 + (R * 32 + hd) * 256, ftag);
      ST(8); FST(60);
      BAR();
    } else {
      __builtin_amdgcn_s_setprio(2);
      const int j = w - 4;
      const WoOff woo = wo_offsets(lane);
    const int sp_ = 2 * xc + i;
    unsigned ga[3]; u32x2 pa_[30];
    auto gather = [&]() {
      asm volatile("" ::: "memory");
#pragma unroll
      for (int n = 0; n < 30; ++n) pa_[n] = __builtin_amdgcn_raw_buffer_load_b64(rs, ga[n / 10], WS_PARTA + (n % 10) * 3072, 16);
    };
#pragma unroll
      for (int r = 0; r < 3; ++r) { const unsigned e = min(144 * j + 64 * r + lane, 575), rc = (e * 683) >> 16, F = 96 * sp_ + e - 96 * rc; ga[r] = ((F >> 6) * 3840 + rc * 64 + (F & 63)) * 8; asm volatile("" : "+v"(ga[r])); }
      const int kt[4] = {6 + j, j < 2 ? 4 + j : 8 + j, j, 12 + j};
      { const int e = lane >> 2, T = 8 * R - 8 + (e == 0 ? kt[0] : e == 1 ? kt[1] : e == 2 ? kt[2] : kt[3]);
        const bool act = lane < 16 && T >= 0;
        poll(ws + WS_FLAG1 + (act ? ((T >> 2) * 16 + (lane & 2 ? 10 : 5) + cc) * 256 : 0), act, ftag); }
      ST(5); FST(36);
      const unsigned so0 = (80 + pf + h) * 512 + l31 * 16, so1 = (h ? (160 + pf) * 512 : (80 + pp) * 512 + 8 * oh) + l31 * 16, so2 = (h ? (160 + pp) * 512 + 8 * oh : (161 + pf) * 512) + l31 * 16;
      struct T3 { u32x4 a, b, c; };
      auto fetch = [&](int t) {
        const int T = 8 * R - 8 + t; const unsigned m = T < 0 ? 0x40000000u : 0u, o = WS_QKV + max(T, 0) * 122880;
        return T3{__builtin_amdgcn_raw_buffer_load_b128(rs, so0 | m, o, 16), __builtin_amdgcn_raw_buffer_load_b128(rs, so1 | m, o, 16), __builtin_amdgcn_raw_buffer_load_b128(rs, so2 | m, o, 16)};
      };
      auto stage = [&](int t, T3 v) {
        if (R == 0 && t < 8) return;
        const unsigned kd = lds0 + L_KV + t * KVS + l31 * 16;
        if (h) v.c[2] = v.c[3] = ONE2;
        *(LAS u32x4*)(kd + h * 512) = v.a; *(LAS u32x4*)(kd + 1024 + h * 512) = v.b; *(LAS u32x4*)(kd + L_V + (1 + h) * VPS) = v.c;
      };
      const T3 t0 = fetch(kt[0]), t1 = fetch(kt[1]);
      FENCE();
      stage(kt[0], t0);
      FENCE();
      const T3 t2 = fetch(kt[2]), t3 = fetch(kt[3]);
      FENCE();
      BAR(); ST(6); FST(39);
      auto pause = [&](int n) { for (int e = 0; e < n; ++e) __builtin_amdgcn_s_sleep(1); };
      pause(j); stage(kt[1], t1);
      BAR(); FST(40);
      pause(2 * j); stage(kt[2], t2); pause(1); stage(kt[3], t3);
      unsigned pf = 0;
      if (!spare) {
        const unsigned po = ((i >> 3) * 160 + j * 40 + (lane >> 1)) * 1280 + (lane & 1) * 128;
        const char* pb = (const char*)p.wo + ((cs80 * 160) & ~127);
        asm volatile("global_load_dword %0, %1, %2" : "+v"(pf) : "v"(po), "s"(pb) : "memory");
        if (lane < 16) asm volatile("global_load_dword %0, %1, %2" : "+v"(pf) : "v"(po + 32 * 1280), "s"(pb) : "memory");
        pre3();
        BAR(); FST(41);
        asm volatile("s_waitcnt vmcnt(0)" : "+v"(pf) :: "memory");
        FST(44);
        wo_steps(5 * j, 5 * j + 5, woo, p.wo, cs80, lds0);
      } else {
        pre3();
        gather();
        FENCE();
        BAR(); FST(41);
        wo_steps(5 * j, 5 * j + 5, woo, p.wo, cs80, lds0);
        FST(45);
#pragma unroll 1
        for (unsigned sp = 0; sp < SPIN_CAP; ++sp) {
          WAITV(0);
          unsigned bad[5];
#pragma unroll
          for (int n = 0; n < 5; ++n) bad[n] = (pa_[n][1] ^ tag) | (pa_[n + 5][1] ^ tag) | (pa_[n + 10][1] ^ tag) | (pa_[n + 15][1] ^ tag) | (pa_[n + 20][1] ^ tag) | (pa_[n + 25][1] ^ tag);
          if (__all((bad[0] | bad[1] | bad[2] | bad[3] | bad[4]) == 0)) break;
          gather();
        }
#pragma unroll
        for (int r = 0; r < 3; ++r) {
          auto v = [&](int kg) { return __uint_as_float(pa_[10 * r + kg][0]); };
          const float sm = (((v(0) + v(1)) + (v(2) + v(3))) + ((v(4) + v(5)) + (v(6) + v(7)))) + (v(8) + v(9));
          *(LAS float*)(lds0 + L_TMP + min(144 * j + 64 * r + lane, 575) * 4) = sm;
        }
        FST(46);
        __builtin_amdgcn_s_waitcnt(0xC07F);
        if (lane == 0) __hip_atomic_fetch_add((LAS unsigned*)(lds0 + L_CNT + 8), 1u, __ATOMIC_RELAXED, __HIP_MEMORY_SCOPE_WORKGROUP);
#pragma unroll 1
        for (unsigned sp = 0; sp < SPIN_CAP; ++sp) { if (__builtin_amdgcn_readfirstlane(*(volatile LAS unsigned*)(lds0 + L_CNT + 8)) >= 4u) break; __builtin_amdgcn_s_sleep(1); }
        asm volatile("" ::: "memory");
        FST(47);
      const unsigned hb = lds0 + L_TMP + 2304 + j * 384;
#pragma unroll
      for (int r = 0; r < 3; ++r) {
        const int e = 64 * r + lane, tk = e >= 96; const unsigned a = lds0 + L_TMP + (e - 96 * tk) * 4, sg = *(const LAS unsigned*)(lds0 + L_TOP + 8 + tk * 4);
        const float m = blo(sg), rd = __builtin_amdgcn_rcpf(bhi(sg));
        const float g = rb((*(const LAS float*)(a + tk * 384) - m * *(const LAS float*)(a + 768)) * rd);
        const float u = rb((*(const LAS float*)(a + 1152 + tk * 384) - m * *(const LAS float*)(a + 1920)) * rd);
        *(LAS u16*)(hb + e * 2) = (u16)pk(rb(g * __builtin_amdgcn_rcpf(1.f + __expf(-g))) * u, 0.f);
      }
      bf16x8 hf[3];
#pragma unroll
      for (int s = 0; s < 3; ++s) hf[s] = lds128(hb + (lane & 1) * 192 + g4 * 16 + s * 64);
      f32x4 y[10] = {};
#pragma unroll
      for (int s = 0; s < 3; ++s)
#pragma unroll
        for (int n = 0; n < 10; ++n) y[n] = MFMA16(hf[s], wdf[(n / 5) * 15 + 5 * s + n % 5], y[n]);
#pragma unroll
      for (int n = 0; n < 5; ++n) {
        const float v = g4 & 1 ? (g4 & 2 ? y[n + 5][1] : y[n + 5][0]) : (g4 & 2 ? y[n][1] : y[n][0]);
        __builtin_amdgcn_raw_buffer_store_b64((u32x2){__float_as_uint(v), tag}, rs, (h * 640 + 32 * j + l31) * 8 + n * 1024, WS_YPART + sp_ * 10240, 16);
      }
      ST(3);
      }
      FST(42);
      if (!spare) WAITV(0);
      FST(43);
      __builtin_amdgcn_s_setprio(0);
      BAR();
    }
  }

  {
    const int tg = tg3;
    const unsigned wa = lds0 + L_WO + (kh * 320 + 4 * g4 + q4) * 160 + 8 * p4;
    constexpr int NPRE = 6;
    bf16x8 wf[NPRE][5];
#pragma unroll
    for (int s = 0; s < NPRE; ++s)
#pragma unroll
      for (int n = 0; n < 5; ++n) wf[s][n] = trfrag(wa + s * 5120 + n * 32, wa + s * 5120 + 2560 + n * 32);
#pragma unroll 1
    for (unsigned sp = 0; sp < SPIN_CAP && !__all((unsigned)(sg0_ >> 32) == tag && (unsigned)(sg1_ >> 32) == tag); ++sp) {
      sg0_ = ldg64(ws + WS_STATS + (tok0 + tk0) * 8); sg1_ = ldg64(ws + WS_STATS + (tok0 + tk1) * 8); }
    f32x4 base[4];
#pragma unroll
    for (int e = 0; e < 4; ++e) {
      const unsigned sg = (unsigned)(e < 2 ? sg0_ : sg1_); const float m = blo(sg), rd = __builtin_amdgcn_rcpf(bhi(sg));
#pragma unroll
      for (int c = 0; c < 4; ++c) { const unsigned u = (e < 2 ? xv0 : xv1)[2 * (e & 1) + (c >> 1)]; const float v = c & 1 ? bhi(u) : blo(u); base[e][c] = v + (v - m) * rd; }
    }
    const u32x2 kk12 = *(const LAS u32x2*)(lds0 + L_TOP);
    const unsigned k1 = __builtin_amdgcn_readfirstlane(kk12[0]), k2 = __builtin_amdgcn_readfirstlane(kk12[1]);
    const unsigned i1 = 2047u - (k1 & 0xffffu) - tok0, i2 = 2047u - (k2 & 0xffffu) - tok0;
#pragma unroll
    for (int s = 0; s < NPRE; ++s)
#pragma unroll
      for (int n = 0; n < 5; ++n) asm volatile("" : "+v"(wf[s][n]));
    asm volatile("" : "+v"(base[0]), "+v"(base[1]), "+v"(base[2]), "+v"(base[3]));
    poll(ws + WS_FLAG2 + (xc * 32 + 16 * kh + (lane & 15)) * 256, lane < 16, ftag);
    ST(9); FST(61);
    const unsigned ob = WS_O + (tok0 / 16 + tg) * 20480;
    u32x4 of[10];
    auto oload = [&](int s) {
      if (s < 8) of[s] = __builtin_amdgcn_raw_buffer_load_b128(rs, lane * 16 + s * 1024, ob + kh * 8192, 16);
      else {
        const unsigned a = (8 * (s - 8) + 2 * g4) * 128 + (lane & 15) * 8;
        const u32x2 u0 = __builtin_amdgcn_raw_buffer_load_b64(rs, a, ob + 16384 + kh * 2048, 16), u1 = __builtin_amdgcn_raw_buffer_load_b64(rs, a + 128, ob + 16384 + kh * 2048, 16);
        of[s] = (u32x4){u0[0], u0[1], u1[0], u1[1]};
      }
    };
#pragma unroll
    for (int s = 0; s < OPRE; ++s) oload(s);
    FENCE();
    FST(62);
    const bool own = (i1 < 64u || i2 < 64u) && w < 5;
    const unsigned ya = own ? ((tid >= 160) * 640 + n0 + ((tid - 160 * (tid >= 160)) >> 1)) * 8 + (tid & 1) * 81920 : 0x40000000u;
    u32x2 yv[8];
    auto yload = [&]() {
      asm volatile("" ::: "memory");
#pragma unroll
      for (int e = 0; e < 8; ++e) yv[e] = __builtin_amdgcn_raw_buffer_load_b64(rs, ya, WS_YPART + e * 10240, 16);
    };
    f32x4 acc[5] = {};
#pragma unroll
    for (int s = 0; s < 10; ++s) {
      if (s + OPRE < 10) oload(s + OPRE);
      if (s + OPRE == 10 && own) yload();
      FENCE();
#pragma unroll
      for (int n = 0; n < 5; ++n) acc[n] = MFMA16(wf[s % NPRE][n], __builtin_bit_cast(bf16x8, of[s]), acc[n]);
      if (s + NPRE < 10) {
#pragma unroll
        for (int n = 0; n < 5; ++n) wf[s][n] = trfrag(wa + (s + NPRE) * 5120 + n * 32, wa + (s + NPRE) * 5120 + 2560 + n * 32);
      }
      FENCE();
    }
    ST(10); FST(63);
    if (own) {
#pragma unroll 1
      for (unsigned sp = 0; sp < SPIN_CAP; ++sp) {
        unsigned bad = 0;
#pragma unroll
        for (int e = 0; e < 8; ++e) bad |= yv[e][1] ^ tag;
        if (__all(bad == 0)) break;
        yload();
      }
      float s = 0.f;
#pragma unroll
      for (int e = 0; e < 8; ++e) s += __uint_as_float(yv[e][0]);
      s += __uint_as_float(dpp<0xB1>(__float_as_uint(s)));
      const unsigned kk = (tid >= 160 ? k2 : k1) >> 16;
      const float r = blo(kk & 0x8000u ? kk & 0x7fffu : ~kk & 0xffffu);
      if (!(tid & 1)) *(LAS float*)(lds0 + L_UPD + (tid >> 1) * 4) = rb(rb(s) * rb(__builtin_amdgcn_rcpf(1.f + __expf(-r))));
    }
#pragma unroll
    for (int n = 0; n < 5; ++n) *(LAS f32x4*)(lds0 + L_RED + kh * 21504 + (16 * tg + (lane & 15)) * 336 + (16 * n + 4 * g4) * 4) = acc[n];
    BAR();
    auto fin = [&](unsigned tk, unsigned ch, unsigned eo, f32x4 b0, f32x4 b1) {
      const unsigned a = lds0 + L_RED + tk * 336 + ch * 32;
      b0 += *(const LAS f32x4*)a + *(const LAS f32x4*)(a + 21504); b1 += *(const LAS f32x4*)(a + 16) + *(const LAS f32x4*)(a + 21504 + 16);
      if (tk == i1 || tk == i2) { const unsigned u = lds0 + L_UPD + (tk == i1 ? 0 : 320) + ch * 32; b0 += *(const LAS f32x4*)u; b1 += *(const LAS f32x4*)(u + 16); }
      *(GAS u32x4*)((char*)p.out + eo) = (u32x4){pk(b0[0], b0[1]), pk(b0[2], b0[3]), pk(b1[0], b1[1]), pk(b1[2], b1[3])};
    };
    fin(tk0, ch0, eo0, base[0], base[1]);
    if (two) fin(tk1, ch1, eo1, base[2], base[3]);
    ST(11);
  }
}

extern "C" void kernel_launch(void* const* d_in, const int* in_sizes, int n_in, void* d_out, int out_size, void* d_ws, size_t ws_size,
                              hipStream_t stream) {
  Params p;
  p.x = (const u16*)d_in[0]; p.wqkv = (const u16*)d_in[1]; p.wo = (const u16*)d_in[2]; p.wr = (const u16*)d_in[4];
  p.wg = (const u16*)d_in[5]; p.wu = (const u16*)d_in[6]; p.wd = (const u16*)d_in[7]; p.out = (u16*)d_out; p.ws = (char*)d_ws;
  fused_block<<<256, 512, 0, stream>>>(p);
}
```
